```python
import torch

def _softmax(x, axis=-1):
    return torch.softmax(x, dim=axis)
def _gelu(x):
    return torch.nn.functional.gelu(x, approximate="tanh")
def _silu(x):
    return torch.nn.functional.silu(x)
def _sigmoid(x):
    return torch.sigmoid(x)
def _layernorm(x, axis=-1, eps=1e-5):
    m = x.mean(dim=axis, keepdim=True)
    v = x.var(dim=axis, keepdim=True, unbiased=False)
    return (x - m) / torch.sqrt(v + eps)
def _scatter_add(tgt, idx, src, axis=0):
    return tgt.clone().index_add_(axis, idx, src)
def _segment_sum(src, seg, n):
    out = torch.zeros((n,) + src.shape[1:], dtype=src.dtype)
    return out.index_add_(0, seg, src)
def _pad(x, pw):
    flat = tuple(v for pair in reversed(pw) for v in pair)
    return torch.nn.functional.pad(x, flat)

kernel_name = 'standalone__mha_block_cached__near_ridge'

def setup_inputs():

    _w = (lambda a: (lambda t: t.to(torch.bfloat16) if t.is_floating_point() else t)(torch.as_tensor(a)))
    B, Sq, D = 16, 240, 448
    H, Sk, Dh = 16, 512, 256
    return {
        "x": _w((lambda *s: torch.randn(*s, dtype=torch.bfloat16) * 0.8)(B, Sq, D)),
        "k_cache": _w((lambda *s: torch.randn(*s, dtype=torch.bfloat16) * 0.8)(B, H, Sk, Dh)),
        "v_cache": _w((lambda *s: torch.randn(*s, dtype=torch.bfloat16) * 0.05)(B, H, Sk, Dh)),
        "wq": _w((lambda *s: torch.randn(*s, dtype=torch.bfloat16) * 0.8)(D, H * Dh) * (D ** -0.5)),
        "wo": _w((lambda *s: torch.randn(*s, dtype=torch.bfloat16) * 0.05)(H * Dh, D) * ((H * Dh) ** -0.5)),
    }

def reference(x, k_cache, v_cache, wq, wo):
    B, Sq, D = x.shape
    H, Dh = 16, 256
    q = (lambda x, *d: x.permute(*d))((x @ wq).reshape(B, Sq, H, Dh), 0, 2, 1, 3)
    scores = torch.einsum("bhqd,bhkd->bhqk", q, k_cache) * (Dh ** -0.5)
    p = _softmax(scores, axis=-1)
    o = torch.einsum("bhqk,bhkd->bhqd", p, v_cache)
    return (lambda x, *d: x.permute(*d))(o, 0, 2, 1, 3).reshape(B, Sq, H * Dh) @ wo
```

```cpp
#include <hip/hip_runtime.h>
#include <hip/hip_bf16.h>

__global__ __launch_bounds__(256, 1) void mha_block_kernel(const void* x, const void* k, const void* v, const void* wq, const void* wo, void* ws, void* out) {
  __shared__ __attribute__((aligned(16))) char smem[163840];
  unsigned wid = __builtin_amdgcn_readfirstlane(threadIdx.x >> 6);
  unsigned blk = blockIdx.x;
  unsigned lds = (unsigned)(size_t)smem;
  const void* ka = (const void*)__builtin_amdgcn_kernarg_segment_ptr();
  asm volatile(
"s_load_dwordx8 s[4:11], %0, 0x0\n"
"s_load_dwordx4 s[12:15], %0, 0x20\n"
"s_load_dwordx2 s[16:17], %0, 0x30\n"
"s_mov_b32 s83, %2\n"
"v_mbcnt_lo_u32_b32 v0, -1, 0\n"
"v_mbcnt_hi_u32_b32 v0, -1, v0\n"
"s_getpc_b64 s[78:79]\n"
"s_and_b32 s78, s78, 0xfffff000\n"
"s_lshr_b32 s77, %1, 3\n"
"s_lshl2_add_u32 s77, s77, s83\n"
"s_lshl_b32 s77, s77, 9\n"
"v_and_b32_e32 v9, 3, v0\n"
"v_lshl_add_u32 v8, v9, 7, s77\n"
"v_min_u32_e32 v8, 58556, v8\n"
"global_load_dword v248, v8, s[78:79]\n"
"s_cmp_lg_u32 s83, 0\n"
"s_cbranch_scc1 .Ltkb0\n"
"s_call_b64 s[78:79], .Lpg1\n"
"s_call_b64 s[78:79], .Lpg5\n"
"s_call_b64 s[78:79], .Lpg9\n"
"s_call_b64 s[78:79], .Lpg13\n"
".Ltkb0:\n"
"s_cmp_lg_u32 s83, 1\n"
"s_cbranch_scc1 .Ltkb1\n"
"s_call_b64 s[78:79], .Lpg2\n"
"s_call_b64 s[78:79], .Lpg6\n"
"s_call_b64 s[78:79], .Lpg10\n"
"s_call_b64 s[78:79], .Lpg14\n"
".Ltkb1:\n"
"s_cmp_lg_u32 s83, 2\n"
"s_cbranch_scc1 .Ltkb2\n"
"s_call_b64 s[78:79], .Lpg3\n"
"s_call_b64 s[78:79], .Lpg7\n"
"s_call_b64 s[78:79], .Lpg11\n"
".Ltkb2:\n"
"s_cmp_lg_u32 s83, 3\n"
"s_cbranch_scc1 .Ltkb3\n"
"s_call_b64 s[78:79], .Lpg4\n"
"s_call_b64 s[78:79], .Lpg8\n"
"s_call_b64 s[78:79], .Lpg12\n"
".Ltkb3:\n"
"s_lshr_b32 s84, s83, 1\n"
"s_and_b32 s85, s83, 1\n"
"s_bfe_u32 s80, %1, 0x10004\n"
"s_and_b32 s81, %1, 6\n"
"s_or_b32 s87, s81, s80\n"
"s_lshl1_add_u32 s82, s87, s84\n"
"v_lshrrev_b32_e32 v6, 5, v0\n"
"v_and_b32_e32 v7, 15, v0\n"
"v_lshlrev_b32_e32 v7, 4, v7\n"
"v_lshl_or_b32 v7, v6, 18, v7\n"
"v_bfe_u32 v11, v0, 4, 1\n"
"v_lshl_or_b32 v252, v11, 15, v7\n"
"s_mul_i32 s77, s84, 66560\n"
"s_add_u32 s89, s77, %3\n"
"s_mul_i32 s80, s85, 8320\n"
"s_add_u32 s68, s89, s80\n"
"s_lshl_b32 s81, s82, 9\n"
"s_lshl_b32 s80, s85, 14\n"
"s_add_u32 s81, s81, s80\n"
"s_waitcnt lgkmcnt(0)\n"
"s_add_u32 s40, s10, s81\n"
"s_addc_u32 s41, s11, 0\n"
"s_and_b32 s41, s41, 0xffff\n"
"s_sub_u32 s42, 3670016, s81\n"
"s_mov_b32 s43, 0x20000\n"
"s_mov_b32 s69, 0\n"
"s_add_u32 m0, s68, 0\n"
"s_nop 0\n"
"buffer_load_dwordx4 v252, s[40:43], s69 offen offset:0 lds\n"
"s_mov_b32 s70, 130032\n"
"buffer_load_dwordx4 v252, s[40:43], s70 offen offset:1040 lds\n"
"s_mov_b32 s69, 63456\n"
"buffer_load_dwordx4 v252, s[40:43], s69 offen offset:2080 lds\n"
"s_mov_b32 s70, 193488\n"
"buffer_load_dwordx4 v252, s[40:43], s70 offen offset:3120 lds\n"
"s_mov_b32 s69, 8192\n"
"s_add_u32 m0, s68, 4160\n"
"s_nop 0\n"
"buffer_load_dwordx4 v252, s[40:43], s69 offen offset:0 lds\n"
"s_mov_b32 s70, 138224\n"
"buffer_load_dwordx4 v252, s[40:43], s70 offen offset:1040 lds\n"
"s_mov_b32 s69, 71648\n"
"buffer_load_dwordx4 v252, s[40:43], s69 offen offset:2080 lds\n"
"s_mov_b32 s70, 201680\n"
"buffer_load_dwordx4 v252, s[40:43], s70 offen offset:3120 lds\n"
"s_bfe_u32 s86, %1, 0x10003\n"
"s_lshr_b32 s80, %1, 5\n"
"s_and_b32 s81, %1, 1\n"
"s_lshl3_add_u32 s88, s81, s80\n"
"v_lshrrev_b32_e32 v1, 3, v0\n"
"v_bfe_u32 v2, v0, 4, 2\n"
"v_and_b32_e32 v3, 7, v0\n"
"v_lshlrev_b32_e32 v4, 1, v2\n"
"v_xor_b32_e32 v4, v4, v3\n"
"v_mul_u32_u24_e32 v5, 896, v1\n"
"v_lshl_add_u32 v253, v4, 4, v5\n"
"s_lshl_b32 s77, s83, 12\n"
"s_add_u32 s77, s77, %3\n"
"s_add_u32 s74, s77, 116480\n"
"s_lshl2_add_u32 s77, s86, s83\n"
"s_mul_i32 s77, s77, 28672\n"
"s_sub_u32 s38, 215040, s77\n"
"s_mul_i32 s80, s88, 215040\n"
"s_add_u32 s77, s77, s80\n"
"s_add_u32 s36, s4, s77\n"
"s_addc_u32 s37, s5, 0\n"
"s_and_b32 s37, s37, 0xffff\n"
"s_mov_b32 s39, 0x20000\n"
"s_mov_b32 s69, 0\n"
"s_add_u32 m0, s74, 16640\n"
"s_nop 0\n"
"buffer_load_dwordx4 v253, s[36:39], s69 offen offset:0 lds\n"
"s_mov_b32 s70, 6144\n"
"buffer_load_dwordx4 v253, s[36:39], s70 offen offset:1024 lds\n"
"s_mov_b32 s69, 12288\n"
"buffer_load_dwordx4 v253, s[36:39], s69 offen offset:2048 lds\n"
"s_mov_b32 s70, 18432\n"
"buffer_load_dwordx4 v253, s[36:39], s70 offen offset:3072 lds\n"
"v_lshlrev_b32_e32 v242, 4, v0\n"
"v_bfe_u32 v11, v0, 4, 1\n"
"v_lshl_or_b32 v7, v11, 1, v6\n"
"v_bfe_u32 v8, v0, 2, 2\n"
"v_lshl_or_b32 v8, v8, 2, v7\n"
"v_mul_u32_u24_e32 v8, 1040, v8\n"
"v_add_u32_e32 v8, s89, v8\n"
"v_and_b32_e32 v9, 3, v0\n"
"v_lshl_add_u32 v241, v9, 3, v8\n"
"v_and_b32_e32 v10, 1, v0\n"
"v_bfe_u32 v12, v0, 1, 1\n"
"v_lshl_or_b32 v10, v10, 2, v12\n"
"v_lshl_add_u32 v251, v10, 4, v8\n"
"v_bfe_u32 v10, v0, 2, 1\n"
"v_bfe_u32 v12, v0, 3, 1\n"
"v_lshl_or_b32 v10, v10, 1, v12\n"
"v_lshl_or_b32 v10, v9, 2, v10\n"
"v_mul_u32_u24_e32 v10, 1040, v10\n"
"v_add_u32_e32 v10, s89, v10\n"
"v_lshl_or_b32 v13, v11, 2, v6\n"
"v_lshl_add_u32 v240, v13, 4, v10\n"
"s_mul_i32 s77, s84, 4294834176\n"
"s_add_u32 s77, s77, 66560\n"
"v_add_u32_e32 v254, s77, v241\n"
"v_lshlrev_b32_e32 v10, 10, v12\n"
"v_lshl_or_b32 v10, v3, 7, v10\n"
"v_and_b32_e32 v13, 6, v0\n"
"v_xor_b32_e32 v13, v13, v2\n"
"s_lshl_b32 s77, s85, 13\n"
"s_add_u32 s77, s77, %3\n"
"s_add_u32 s77, s77, 116480\n"
"s_lshl_b32 s80, s84, 12\n"
"s_add_u32 s81, s77, s80\n"
"s_sub_u32 s77, s77, s80\n"
"s_add_u32 s77, s77, 4096\n"
"v_lshl_add_u32 v14, v13, 4, v10\n"
"v_xor_b32_e32 v15, 64, v14\n"
"v_add_u32_e32 v243, s81, v14\n"
"v_add_u32_e32 v244, s77, v14\n"
"v_add_u32_e32 v245, s81, v15\n"
"v_add_u32_e32 v246, s77, v15\n"
"s_lshl_b32 s77, s87, 7\n"
"s_lshl_b32 s80, s87, 2\n"
"v_lshrrev_b32_e32 v10, 2, v242\n"
"v_add_u32_e32 v250, s77, v10\n"
"v_and_b32_e32 v247, 32, v10\n"
"v_add_u32_e32 v247, s80, v247\n"
"v_bfe_u32 v10, v242, 4, 3\n"
"v_lshl_add_u32 v247, v10, 7, v247\n"
"v_cmp_gt_u32_e32 vcc, 256, v242\n"
"v_mov_b32_e32 v10, 0x10000000\n"
"s_nop 0\n"
"v_cndmask_b32_e32 v250, v10, v250, vcc\n"
"v_cndmask_b32_e32 v247, v10, v247, vcc\n"
"s_mov_b32 s69, 256\n"
"s_add_u32 m0, s68, 16640\n"
"s_nop 0\n"
"buffer_load_dwordx4 v252, s[40:43], s69 offen offset:0 lds\n"
"s_mov_b32 s70, 130288\n"
"buffer_load_dwordx4 v252, s[40:43], s70 offen offset:1040 lds\n"
"s_mov_b32 s69, 63712\n"
"buffer_load_dwordx4 v252, s[40:43], s69 offen offset:2080 lds\n"
"s_mov_b32 s70, 193744\n"
"buffer_load_dwordx4 v252, s[40:43], s70 offen offset:3120 lds\n"
"s_mov_b32 s69, 8448\n"
"s_add_u32 m0, s68, 20800\n"
"s_nop 0\n"
"buffer_load_dwordx4 v252, s[40:43], s69 offen offset:0 lds\n"
"s_mov_b32 s70, 138480\n"
"buffer_load_dwordx4 v252, s[40:43], s70 offen offset:1040 lds\n"
"s_mov_b32 s69, 71904\n"
"buffer_load_dwordx4 v252, s[40:43], s69 offen offset:2080 lds\n"
"s_mov_b32 s70, 201936\n"
"buffer_load_dwordx4 v252, s[40:43], s70 offen offset:3120 lds\n"
"s_waitcnt vmcnt(8)\n"
"s_barrier\n"
"ds_read_b64_tr_b16 v[224:225], v251 offset:0\n"
"ds_read_b64_tr_b16 v[226:227], v251 offset:256\n"
"ds_read_b64_tr_b16 v[228:229], v251 offset:512\n"
"ds_read_b64_tr_b16 v[230:231], v251 offset:768\n"
"ds_read_b128 a[0:3], v243 offset:16640\n"
"ds_read_b128 a[16:19], v245 offset:16640\n"
"ds_read_b128 a[4:7], v243 offset:18688\n"
"ds_read_b128 a[20:23], v245 offset:18688\n"
"ds_read_b128 a[8:11], v244 offset:16640\n"
"ds_read_b128 a[24:27], v246 offset:16640\n"
"ds_read_b128 a[12:15], v244 offset:18688\n"
"ds_read_b128 a[28:31], v246 offset:18688\n"
"ds_read_b64_tr_b16 v[232:233], v251 offset:8\n"
"ds_read_b64_tr_b16 v[234:235], v251 offset:264\n"
"s_waitcnt lgkmcnt(2)\n"
"v_mfma_f32_16x16x32_bf16 v[0:3], v[224:227], a[0:3], 0\n"
"v_mfma_f32_16x16x32_bf16 v[0:3], v[228:231], a[16:19], v[0:3]\n"
"v_mfma_f32_16x16x32_bf16 v[8:11], v[224:227], a[4:7], 0\n"
"ds_read_b64_tr_b16 v[236:237], v251 offset:520\n"
"s_mov_b32 s69, 128\n"
"v_mfma_f32_16x16x32_bf16 v[8:11], v[228:231], a[20:23], v[8:11]\n"
"ds_read_b64_tr_b16 v[238:239], v251 offset:776\n"
"s_add_u32 m0, s74, 0\n"
"v_mfma_f32_16x16x32_bf16 v[16:19], v[224:227], a[8:11], 0\n"
"buffer_load_dwordx4 v253, s[36:39], s69 offen offset:0 lds\n"
"v_mfma_f32_16x16x32_bf16 v[16:19], v[228:231], a[24:27], v[16:19]\n"
"s_mov_b32 s70, 6272\n"
"v_mfma_f32_16x16x32_bf16 v[24:27], v[224:227], a[12:15], 0\n"
"buffer_load_dwordx4 v253, s[36:39], s70 offen offset:1024 lds\n"
"v_mfma_f32_16x16x32_bf16 v[24:27], v[228:231], a[28:31], v[24:27]\n"
"s_waitcnt lgkmcnt(0)\n"
"v_mfma_f32_16x16x32_bf16 v[28:31], v[232:235], a[12:15], 0\n"
"ds_read_b64_tr_b16 v[224:225], v251 offset:32\n"
"v_mfma_f32_16x16x32_bf16 v[28:31], v[236:239], a[28:31], v[28:31]\n"
"ds_read_b64_tr_b16 v[226:227], v251 offset:288\n"
"v_mfma_f32_16x16x32_bf16 v[20:23], v[232:235], a[8:11], 0\n"
"ds_read_b64_tr_b16 v[228:229], v251 offset:544\n"
"v_mfma_f32_16x16x32_bf16 v[20:23], v[236:239], a[24:27], v[20:23]\n"
"ds_read_b64_tr_b16 v[230:231], v251 offset:800\n"
"s_mov_b32 s69, 12416\n"
"v_mfma_f32_16x16x32_bf16 v[12:15], v[232:235], a[4:7], 0\n"
"buffer_load_dwordx4 v253, s[36:39], s69 offen offset:2048 lds\n"
"v_mfma_f32_16x16x32_bf16 v[12:15], v[236:239], a[20:23], v[12:15]\n"
"s_mov_b32 s70, 18560\n"
"v_mfma_f32_16x16x32_bf16 v[4:7], v[232:235], a[0:3], 0\n"
"buffer_load_dwordx4 v253, s[36:39], s70 offen offset:3072 lds\n"
"v_mfma_f32_16x16x32_bf16 v[4:7], v[236:239], a[16:19], v[4:7]\n"
"s_waitcnt lgkmcnt(0)\n"
"v_mfma_f32_16x16x32_bf16 v[128:131], v[224:227], a[0:3], 0\n"
"ds_read_b64_tr_b16 v[232:233], v251 offset:40\n"
"v_mfma_f32_16x16x32_bf16 v[128:131], v[228:231], a[16:19], v[128:131]\n"
"ds_read_b64_tr_b16 v[234:235], v251 offset:296\n"
"v_mfma_f32_16x16x32_bf16 v[136:139], v[224:227], a[4:7], 0\n"
"ds_read_b64_tr_b16 v[236:237], v251 offset:552\n"
"v_mfma_f32_16x16x32_bf16 v[136:139], v[228:231], a[20:23], v[136:139]\n"
"ds_read_b64_tr_b16 v[238:239], v251 offset:808\n"
"s_mov_b32 s69, 524288\n"
"v_mfma_f32_16x16x32_bf16 v[144:147], v[224:227], a[8:11], 0\n"
"s_add_u32 m0, s68, 33280\n"
"v_mfma_f32_16x16x32_bf16 v[144:147], v[228:231], a[24:27], v[144:147]\n"
"buffer_load_dwordx4 v252, s[40:43], s69 offen offset:0 lds\n"
"v_mfma_f32_16x16x32_bf16 v[152:155], v[224:227], a[12:15], 0\n"
"s_mov_b32 s70, 654320\n"
"v_mfma_f32_16x16x32_bf16 v[152:155], v[228:231], a[28:31], v[152:155]\n"
"buffer_load_dwordx4 v252, s[40:43], s70 offen offset:1040 lds\n"
"s_waitcnt lgkmcnt(0)\n"
"v_mfma_f32_16x16x32_bf16 v[156:159], v[232:235], a[12:15], 0\n"
"ds_read_b64_tr_b16 v[224:225], v251 offset:128\n"
"v_mfma_f32_16x16x32_bf16 v[156:159], v[236:239], a[28:31], v[156:159]\n"
"ds_read_b64_tr_b16 v[226:227], v251 offset:384\n"
"v_mfma_f32_16x16x32_bf16 v[148:151], v[232:235], a[8:11], 0\n"
"ds_read_b64_tr_b16 v[228:229], v251 offset:640\n"
"v_mfma_f32_16x16x32_bf16 v[148:151], v[236:239], a[24:27], v[148:151]\n"
"ds_read_b64_tr_b16 v[230:231], v251 offset:896\n"
"v_mfma_f32_16x16x32_bf16 v[140:143], v[232:235], a[4:7], 0\n"
"s_mov_b32 s69, 587744\n"
"v_mfma_f32_16x16x32_bf16 v[140:143], v[236:239], a[20:23], v[140:143]\n"
"buffer_load_dwordx4 v252, s[40:43], s69 offen offset:2080 lds\n"
"v_mfma_f32_16x16x32_bf16 v[132:135], v[232:235], a[0:3], 0\n"
"s_mov_b32 s70, 717776\n"
"v_mfma_f32_16x16x32_bf16 v[132:135], v[236:239], a[16:19], v[132:135]\n"
"buffer_load_dwordx4 v252, s[40:43], s70 offen offset:3120 lds\n"
"s_waitcnt lgkmcnt(0)\n"
"v_mfma_f32_16x16x32_bf16 v[32:35], v[224:227], a[0:3], 0\n"
"ds_read_b64_tr_b16 v[232:233], v251 offset:136\n"
"v_mfma_f32_16x16x32_bf16 v[32:35], v[228:231], a[16:19], v[32:35]\n"
"ds_read_b64_tr_b16 v[234:235], v251 offset:392\n"
"v_mfma_f32_16x16x32_bf16 v[40:43], v[224:227], a[4:7], 0\n"
"ds_read_b64_tr_b16 v[236:237], v251 offset:648\n"
"v_mfma_f32_16x16x32_bf16 v[40:43], v[228:231], a[20:23], v[40:43]\n"
"ds_read_b64_tr_b16 v[238:239], v251 offset:904\n"
"v_mfma_f32_16x16x32_bf16 v[48:51], v[224:227], a[8:11], 0\n"
"s_mov_b32 s69, 532480\n"
"v_mfma_f32_16x16x32_bf16 v[48:51], v[228:231], a[24:27], v[48:51]\n"
"s_add_u32 m0, s68, 37440\n"
"v_mfma_f32_16x16x32_bf16 v[56:59], v[224:227], a[12:15], 0\n"
"buffer_load_dwordx4 v252, s[40:43], s69 offen offset:0 lds\n"
"v_mfma_f32_16x16x32_bf16 v[56:59], v[228:231], a[28:31], v[56:59]\n"
"s_waitcnt lgkmcnt(0)\n"
"v_mfma_f32_16x16x32_bf16 v[60:63], v[232:235], a[12:15], 0\n"
"ds_read_b64_tr_b16 v[224:225], v251 offset:160\n"
"v_mfma_f32_16x16x32_bf16 v[60:63], v[236:239], a[28:31], v[60:63]\n"
"ds_read_b64_tr_b16 v[226:227], v251 offset:416\n"
"v_mfma_f32_16x16x32_bf16 v[52:55], v[232:235], a[8:11], 0\n"
"ds_read_b64_tr_b16 v[228:229], v251 offset:672\n"
"v_mfma_f32_16x16x32_bf16 v[52:55], v[236:239], a[24:27], v[52:55]\n"
"ds_read_b64_tr_b16 v[230:231], v251 offset:928\n"
"s_mov_b32 s70, 662512\n"
"v_mfma_f32_16x16x32_bf16 v[44:47], v[232:235], a[4:7], 0\n"
"buffer_load_dwordx4 v252, s[40:43], s70 offen offset:1040 lds\n"
"v_mfma_f32_16x16x32_bf16 v[44:47], v[236:239], a[20:23], v[44:47]\n"
"s_mov_b32 s69, 595936\n"
"v_mfma_f32_16x16x32_bf16 v[36:39], v[232:235], a[0:3], 0\n"
"buffer_load_dwordx4 v252, s[40:43], s69 offen offset:2080 lds\n"
"v_mfma_f32_16x16x32_bf16 v[36:39], v[236:239], a[16:19], v[36:39]\n"
"s_waitcnt lgkmcnt(0)\n"
"v_mfma_f32_16x16x32_bf16 a[224:227], v[224:227], a[0:3], 0\n"
"ds_read_b64_tr_b16 v[232:233], v251 offset:168\n"
"v_mfma_f32_16x16x32_bf16 a[224:227], v[228:231], a[16:19], a[224:227]\n"
"ds_read_b64_tr_b16 v[234:235], v251 offset:424\n"
"v_mfma_f32_16x16x32_bf16 a[232:235], v[224:227], a[4:7], 0\n"
"ds_read_b64_tr_b16 v[236:237], v251 offset:680\n"
"v_mfma_f32_16x16x32_bf16 a[232:235], v[228:231], a[20:23], a[232:235]\n"
"ds_read_b64_tr_b16 v[238:239], v251 offset:936\n"
"s_mov_b32 s70, 725968\n"
"v_mfma_f32_16x16x32_bf16 a[240:243], v[224:227], a[8:11], 0\n"
"buffer_load_dwordx4 v252, s[40:43], s70 offen offset:3120 lds\n"
"v_mfma_f32_16x16x32_bf16 a[240:243], v[228:231], a[24:27], a[240:243]\n"
"v_mfma_f32_16x16x32_bf16 a[248:251], v[224:227], a[12:15], 0\n"
"v_mfma_f32_16x16x32_bf16 a[248:251], v[228:231], a[28:31], a[248:251]\n"
"s_waitcnt vmcnt(8) lgkmcnt(0)\n"
"s_barrier\n"
"v_mfma_f32_16x16x32_bf16 a[252:255], v[232:235], a[12:15], 0\n"
"ds_read_b64_tr_b16 v[224:225], v251 offset:16640\n"
"v_mfma_f32_16x16x32_bf16 a[252:255], v[236:239], a[28:31], a[252:255]\n"
"ds_read_b64_tr_b16 v[226:227], v251 offset:16896\n"
"v_mfma_f32_16x16x32_bf16 a[244:247], v[232:235], a[8:11], 0\n"
"ds_read_b64_tr_b16 v[228:229], v251 offset:17152\n"
"v_mfma_f32_16x16x32_bf16 a[244:247], v[236:239], a[24:27], a[244:247]\n"
"ds_read_b64_tr_b16 v[230:231], v251 offset:17408\n"
"v_mfma_f32_16x16x32_bf16 a[236:239], v[232:235], a[4:7], 0\n"
"s_mov_b32 s69, 524544\n"
"v_mfma_f32_16x16x32_bf16 a[236:239], v[236:239], a[20:23], a[236:239]\n"
"s_add_u32 m0, s68, 0\n"
"v_mfma_f32_16x16x32_bf16 a[228:231], v[232:235], a[0:3], 0\n"
"buffer_load_dwordx4 v252, s[40:43], s69 offen offset:0 lds\n"
"v_mfma_f32_16x16x32_bf16 a[228:231], v[236:239], a[16:19], a[228:231]\n"
"s_waitcnt lgkmcnt(0)\n"
"v_mfma_f32_16x16x32_bf16 v[64:67], v[224:227], a[0:3], 0\n"
"ds_read_b64_tr_b16 v[232:233], v251 offset:16648\n"
"v_mfma_f32_16x16x32_bf16 v[64:67], v[228:231], a[16:19], v[64:67]\n"
"ds_read_b64_tr_b16 v[234:235], v251 offset:16904\n"
"v_mfma_f32_16x16x32_bf16 v[72:75], v[224:227], a[4:7], 0\n"
"ds_read_b64_tr_b16 v[236:237], v251 offset:17160\n"
"s_mov_b32 s70, 256\n"
"v_mfma_f32_16x16x32_bf16 v[72:75], v[228:231], a[20:23], v[72:75]\n"
"ds_read_b64_tr_b16 v[238:239], v251 offset:17416\n"
"s_add_u32 m0, s74, 16640\n"
"v_mfma_f32_16x16x32_bf16 v[80:83], v[224:227], a[8:11], 0\n"
"buffer_load_dwordx4 v253, s[36:39], s70 offen offset:0 lds\n"
"s_mov_b32 s69, 654576\n"
"v_mfma_f32_16x16x32_bf16 v[80:83], v[228:231], a[24:27], v[80:83]\n"
"ds_read_b128 a[32:35], v243 offset:0\n"
"s_add_u32 m0, s68, 0\n"
"v_mfma_f32_16x16x32_bf16 v[88:91], v[224:227], a[12:15], 0\n"
"buffer_load_dwordx4 v252, s[40:43], s69 offen offset:1040 lds\n"
"v_mfma_f32_16x16x32_bf16 v[88:91], v[228:231], a[28:31], v[88:91]\n"
"ds_read_b128 a[36:39], v243 offset:2048\n"
"s_waitcnt lgkmcnt(2)\n"
"v_mfma_f32_16x16x32_bf16 v[92:95], v[232:235], a[12:15], 0\n"
"ds_read_b64_tr_b16 v[224:225], v251 offset:16672\n"
"v_mfma_f32_16x16x32_bf16 v[92:95], v[236:239], a[28:31], v[92:95]\n"
"ds_read_b64_tr_b16 v[226:227], v251 offset:16928\n"
"v_mfma_f32_16x16x32_bf16 v[84:87], v[232:235], a[8:11], 0\n"
"ds_read_b64_tr_b16 v[228:229], v251 offset:17184\n"
"v_mfma_f32_16x16x32_bf16 v[84:87], v[236:239], a[24:27], v[84:87]\n"
"ds_read_b64_tr_b16 v[230:231], v251 offset:17440\n"
"v_mfma_f32_16x16x32_bf16 v[76:79], v[232:235], a[4:7], 0\n"
"v_mfma_f32_16x16x32_bf16 v[76:79], v[236:239], a[20:23], v[76:79]\n"
"s_mov_b32 s70, 588000\n"
"v_mfma_f32_16x16x32_bf16 v[68:71], v[232:235], a[0:3], 0\n"
"buffer_load_dwordx4 v252, s[40:43], s70 offen offset:2080 lds\n"
"v_mfma_f32_16x16x32_bf16 v[68:71], v[236:239], a[16:19], v[68:71]\n"
"ds_read_b128 a[40:43], v244 offset:0\n"
"s_waitcnt lgkmcnt(1)\n"
"v_mfma_f32_16x16x32_bf16 v[192:195], v[224:227], a[0:3], 0\n"
"ds_read_b64_tr_b16 v[232:233], v251 offset:16680\n"
"v_mfma_f32_16x16x32_bf16 v[192:195], v[228:231], a[16:19], v[192:195]\n"
"ds_read_b64_tr_b16 v[234:235], v251 offset:16936\n"
"v_mfma_f32_16x16x32_bf16 v[200:203], v[224:227], a[4:7], 0\n"
"ds_read_b64_tr_b16 v[236:237], v251 offset:17192\n"
"s_mov_b32 s69, 6400\n"
"v_mfma_f32_16x16x32_bf16 v[200:203], v[228:231], a[20:23], v[200:203]\n"
"ds_read_b64_tr_b16 v[238:239], v251 offset:17448\n"
"s_add_u32 m0, s74, 16640\n"
"v_mfma_f32_16x16x32_bf16 v[208:211], v[224:227], a[8:11], 0\n"
"buffer_load_dwordx4 v253, s[36:39], s69 offen offset:1024 lds\n"
"s_mov_b32 s70, 718032\n"
"v_mfma_f32_16x16x32_bf16 v[208:211], v[228:231], a[24:27], v[208:211]\n"
"s_add_u32 m0, s68, 0\n"
"v_mfma_f32_16x16x32_bf16 v[216:219], v[224:227], a[12:15], 0\n"
"buffer_load_dwordx4 v252, s[40:43], s70 offen offset:3120 lds\n"
"v_mfma_f32_16x16x32_bf16 v[216:219], v[228:231], a[28:31], v[216:219]\n"
"ds_read_b128 a[44:47], v244 offset:2048\n"
"s_waitcnt lgkmcnt(1)\n"
"v_mfma_f32_16x16x32_bf16 v[220:223], v[232:235], a[12:15], 0\n"
"ds_read_b64_tr_b16 v[224:225], v251 offset:16768\n"
"v_mfma_f32_16x16x32_bf16 v[220:223], v[236:239], a[28:31], v[220:223]\n"
"ds_read_b64_tr_b16 v[226:227], v251 offset:17024\n"
"v_mfma_f32_16x16x32_bf16 v[212:215], v[232:235], a[8:11], 0\n"
"ds_read_b64_tr_b16 v[228:229], v251 offset:17280\n"
"v_mfma_f32_16x16x32_bf16 v[212:215], v[236:239], a[24:27], v[212:215]\n"
"ds_read_b64_tr_b16 v[230:231], v251 offset:17536\n"
"v_mfma_f32_16x16x32_bf16 v[204:207], v[232:235], a[4:7], 0\n"
"s_mov_b32 s69, 532736\n"
"v_mfma_f32_16x16x32_bf16 v[204:207], v[236:239], a[20:23], v[204:207]\n"
"s_add_u32 m0, s68, 4160\n"
"v_mfma_f32_16x16x32_bf16 v[196:199], v[232:235], a[0:3], 0\n"
"buffer_load_dwordx4 v252, s[40:43], s69 offen offset:0 lds\n"
"v_mfma_f32_16x16x32_bf16 v[196:199], v[236:239], a[16:19], v[196:199]\n"
"ds_read_b128 a[48:51], v245 offset:0\n"
"s_waitcnt lgkmcnt(1)\n"
"v_mfma_f32_16x16x32_bf16 v[96:99], v[224:227], a[0:3], 0\n"
"ds_read_b64_tr_b16 v[232:233], v251 offset:16776\n"
"v_mfma_f32_16x16x32_bf16 v[96:99], v[228:231], a[16:19], v[96:99]\n"
"ds_read_b64_tr_b16 v[234:235], v251 offset:17032\n"
"v_mfma_f32_16x16x32_bf16 v[104:107], v[224:227], a[4:7], 0\n"
"ds_read_b64_tr_b16 v[236:237], v251 offset:17288\n"
"s_mov_b32 s70, 12544\n"
"v_mfma_f32_16x16x32_bf16 v[104:107], v[228:231], a[20:23], v[104:107]\n"
"ds_read_b64_tr_b16 v[238:239], v251 offset:17544\n"
"s_add_u32 m0, s74, 16640\n"
"v_mfma_f32_16x16x32_bf16 v[112:115], v[224:227], a[8:11], 0\n"
"buffer_load_dwordx4 v253, s[36:39], s70 offen offset:2048 lds\n"
"s_mov_b32 s69, 662768\n"
"v_mfma_f32_16x16x32_bf16 v[112:115], v[228:231], a[24:27], v[112:115]\n"
"s_add_u32 m0, s68, 4160\n"
"v_mfma_f32_16x16x32_bf16 v[120:123], v[224:227], a[12:15], 0\n"
"buffer_load_dwordx4 v252, s[40:43], s69 offen offset:1040 lds\n"
"v_mfma_f32_16x16x32_bf16 v[120:123], v[228:231], a[28:31], v[120:123]\n"
"ds_read_b128 a[52:55], v245 offset:2048\n"
"s_waitcnt lgkmcnt(1)\n"
"v_mfma_f32_16x16x32_bf16 v[124:127], v[232:235], a[12:15], 0\n"
"ds_read_b64_tr_b16 v[224:225], v251 offset:16800\n"
"v_mfma_f32_16x16x32_bf16 v[124:127], v[236:239], a[28:31], v[124:127]\n"
"ds_read_b64_tr_b16 v[226:227], v251 offset:17056\n"
"v_mfma_f32_16x16x32_bf16 v[116:119], v[232:235], a[8:11], 0\n"
"ds_read_b64_tr_b16 v[228:229], v251 offset:17312\n"
"v_mfma_f32_16x16x32_bf16 v[116:119], v[236:239], a[24:27], v[116:119]\n"
"ds_read_b64_tr_b16 v[230:231], v251 offset:17568\n"
"v_mfma_f32_16x16x32_bf16 v[108:111], v[232:235], a[4:7], 0\n"
"v_mfma_f32_16x16x32_bf16 v[108:111], v[236:239], a[20:23], v[108:111]\n"
"ds_read_b128 a[56:59], v246 offset:0\n"
"s_mov_b32 s70, 596192\n"
"v_mfma_f32_16x16x32_bf16 v[100:103], v[232:235], a[0:3], 0\n"
"buffer_load_dwordx4 v252, s[40:43], s70 offen offset:2080 lds\n"
"v_mfma_f32_16x16x32_bf16 v[100:103], v[236:239], a[16:19], v[100:103]\n"
"ds_read_b128 a[60:63], v246 offset:2048\n"
"s_waitcnt lgkmcnt(2)\n"
"v_mfma_f32_16x16x32_bf16 v[160:163], v[224:227], a[0:3], 0\n"
"ds_read_b64_tr_b16 v[232:233], v251 offset:16808\n"
"v_mfma_f32_16x16x32_bf16 v[160:163], v[228:231], a[16:19], v[160:163]\n"
"ds_read_b64_tr_b16 v[234:235], v251 offset:17064\n"
"v_mfma_f32_16x16x32_bf16 v[168:171], v[224:227], a[4:7], 0\n"
"ds_read_b64_tr_b16 v[236:237], v251 offset:17320\n"
"s_mov_b32 s69, 18688\n"
"v_mfma_f32_16x16x32_bf16 v[168:171], v[228:231], a[20:23], v[168:171]\n"
"ds_read_b64_tr_b16 v[238:239], v251 offset:17576\n"
"s_add_u32 m0, s74, 16640\n"
"v_mfma_f32_16x16x32_bf16 v[176:179], v[224:227], a[8:11], 0\n"
"buffer_load_dwordx4 v253, s[36:39], s69 offen offset:3072 lds\n"
"s_mov_b32 s70, 726224\n"
"v_mfma_f32_16x16x32_bf16 v[176:179], v[228:231], a[24:27], v[176:179]\n"
"s_add_u32 m0, s68, 4160\n"
"v_mfma_f32_16x16x32_bf16 v[184:187], v[224:227], a[12:15], 0\n"
"buffer_load_dwordx4 v252, s[40:43], s70 offen offset:3120 lds\n"
"v_mfma_f32_16x16x32_bf16 v[184:187], v[228:231], a[28:31], v[184:187]\n"
"s_waitcnt vmcnt(12) lgkmcnt(0)\n"
"s_barrier\n"
"v_mfma_f32_16x16x32_bf16 v[188:191], v[232:235], a[12:15], 0\n"
"ds_read_b64_tr_b16 v[224:225], v251 offset:33280\n"
"v_mfma_f32_16x16x32_bf16 v[188:191], v[236:239], a[28:31], v[188:191]\n"
"ds_read_b64_tr_b16 v[226:227], v251 offset:33536\n"
"v_mfma_f32_16x16x32_bf16 v[180:183], v[232:235], a[8:11], 0\n"
"ds_read_b64_tr_b16 v[228:229], v251 offset:33792\n"
"v_mfma_f32_16x16x32_bf16 v[180:183], v[236:239], a[24:27], v[180:183]\n"
"ds_read_b64_tr_b16 v[230:231], v251 offset:34048\n"
"v_mfma_f32_16x16x32_bf16 v[172:175], v[232:235], a[4:7], 0\n"
"s_mov_b32 s69, 1048576\n"
"v_mfma_f32_16x16x32_bf16 v[172:175], v[236:239], a[20:23], v[172:175]\n"
"s_add_u32 m0, s68, 16640\n"
"v_mfma_f32_16x16x32_bf16 v[164:167], v[232:235], a[0:3], 0\n"
"buffer_load_dwordx4 v252, s[40:43], s69 offen offset:0 lds\n"
"v_mfma_f32_16x16x32_bf16 v[164:167], v[236:239], a[16:19], v[164:167]\n"
"s_waitcnt lgkmcnt(0)\n"
"v_mfma_f32_16x16x32_bf16 v[0:3], v[224:227], a[32:35], v[0:3]\n"
"ds_read_b64_tr_b16 v[232:233], v251 offset:33288\n"
"s_lshl_b32 s77, s88, 1\n"
"v_mfma_f32_16x16x32_bf16 v[0:3], v[228:231], a[48:51], v[0:3]\n"
"ds_read_b64_tr_b16 v[234:235], v251 offset:33544\n"
"s_or_b32 s77, s77, s86\n"
"v_mfma_f32_16x16x32_bf16 v[8:11], v[224:227], a[36:39], v[8:11]\n"
"ds_read_b64_tr_b16 v[236:237], v251 offset:33800\n"
"s_mov_b32 s70, 384\n"
"v_mfma_f32_16x16x32_bf16 v[8:11], v[228:231], a[52:55], v[8:11]\n"
"ds_read_b64_tr_b16 v[238:239], v251 offset:34056\n"
"s_add_u32 m0, s74, 0\n"
"v_mfma_f32_16x16x32_bf16 v[16:19], v[224:227], a[40:43], v[16:19]\n"
"buffer_load_dwordx4 v253, s[36:39], s70 offen offset:0 lds\n"
"s_mov_b32 s69, 1178608\n"
"v_mfma_f32_16x16x32_bf16 v[16:19], v[228:231], a[56:59], v[16:19]\n"
"s_add_u32 m0, s68, 16640\n"
"s_lshl_b32 s77, s77, 10\n"
"v_mfma_f32_16x16x32_bf16 v[24:27], v[224:227], a[44:47], v[24:27]\n"
"buffer_load_dwordx4 v252, s[40:43], s69 offen offset:1040 lds\n"
"s_add_u32 s77, s77, 41943040\n"
"v_mfma_f32_16x16x32_bf16 v[24:27], v[228:231], a[60:63], v[24:27]\n"
"s_waitcnt lgkmcnt(0)\n"
"v_mfma_f32_16x16x32_bf16 v[28:31], v[232:235], a[44:47], v[28:31]\n"
"ds_read_b64_tr_b16 v[224:225], v251 offset:33312\n"
"v_mfma_f32_16x16x32_bf16 v[28:31], v[236:239], a[60:63], v[28:31]\n"
"ds_read_b64_tr_b16 v[226:227], v251 offset:33568\n"
"v_mfma_f32_16x16x32_bf16 v[20:23], v[232:235], a[40:43], v[20:23]\n"
"ds_read_b64_tr_b16 v[228:229], v251 offset:33824\n"
"v_mfma_f32_16x16x32_bf16 v[20:23], v[236:239], a[56:59], v[20:23]\n"
"ds_read_b64_tr_b16 v[230:231], v251 offset:34080\n"
"v_mfma_f32_16x16x32_bf16 v[12:15], v[232:235], a[36:39], v[12:15]\n"
"s_cmp_eq_u32 s83, 0\n"
"s_cselect_b32 s22, 66560, 0\n"
"v_mfma_f32_16x16x32_bf16 v[12:15], v[236:239], a[52:55], v[12:15]\n"
"s_mov_b32 s70, 1112032\n"
"v_mfma_f32_16x16x32_bf16 v[4:7], v[232:235], a[32:35], v[4:7]\n"
"buffer_load_dwordx4 v252, s[40:43], s70 offen offset:2080 lds\n"
"v_mfma_f32_16x16x32_bf16 v[4:7], v[236:239], a[48:51], v[4:7]\n"
"s_waitcnt lgkmcnt(0)\n"
"v_mfma_f32_16x16x32_bf16 v[128:131], v[224:227], a[32:35], v[128:131]\n"
"ds_read_b64_tr_b16 v[232:233], v251 offset:33320\n"
"v_mfma_f32_16x16x32_bf16 v[128:131], v[228:231], a[48:51], v[128:131]\n"
"ds_read_b64_tr_b16 v[234:235], v251 offset:33576\n"
"v_mfma_f32_16x16x32_bf16 v[136:139], v[224:227], a[36:39], v[136:139]\n"
"ds_read_b64_tr_b16 v[236:237], v251 offset:33832\n"
"s_mov_b32 s69, 6528\n"
"v_mfma_f32_16x16x32_bf16 v[136:139], v[228:231], a[52:55], v[136:139]\n"
"ds_read_b64_tr_b16 v[238:239], v251 offset:34088\n"
"s_add_u32 m0, s74, 0\n"
"v_mfma_f32_16x16x32_bf16 v[144:147], v[224:227], a[40:43], v[144:147]\n"
"buffer_load_dwordx4 v253, s[36:39], s69 offen offset:1024 lds\n"
"s_mov_b32 s70, 1242064\n"
"v_mfma_f32_16x16x32_bf16 v[144:147], v[228:231], a[56:59], v[144:147]\n"
"s_add_u32 m0, s68, 16640\n"
"v_mfma_f32_16x16x32_bf16 v[152:155], v[224:227], a[44:47], v[152:155]\n"
"buffer_load_dwordx4 v252, s[40:43], s70 offen offset:3120 lds\n"
"v_mfma_f32_16x16x32_bf16 v[152:155], v[228:231], a[60:63], v[152:155]\n"
"s_waitcnt lgkmcnt(0)\n"
"v_mfma_f32_16x16x32_bf16 v[156:159], v[232:235], a[44:47], v[156:159]\n"
"ds_read_b64_tr_b16 v[224:225], v251 offset:33408\n"
"v_mfma_f32_16x16x32_bf16 v[156:159], v[236:239], a[60:63], v[156:159]\n"
"ds_read_b64_tr_b16 v[226:227], v251 offset:33664\n"
"v_mfma_f32_16x16x32_bf16 v[148:151], v[232:235], a[40:43], v[148:151]\n"
"ds_read_b64_tr_b16 v[228:229], v251 offset:33920\n"
"v_mfma_f32_16x16x32_bf16 v[148:151], v[236:239], a[56:59], v[148:151]\n"
"ds_read_b64_tr_b16 v[230:231], v251 offset:34176\n"
"v_mfma_f32_16x16x32_bf16 v[140:143], v[232:235], a[36:39], v[140:143]\n"
"s_mov_b32 s69, 1056768\n"
"v_mfma_f32_16x16x32_bf16 v[140:143], v[236:239], a[52:55], v[140:143]\n"
"s_add_u32 m0, s68, 20800\n"
"v_mfma_f32_16x16x32_bf16 v[132:135], v[232:235], a[32:35], v[132:135]\n"
"buffer_load_dwordx4 v252, s[40:43], s69 offen offset:0 lds\n"
"v_mfma_f32_16x16x32_bf16 v[132:135], v[236:239], a[48:51], v[132:135]\n"
"s_waitcnt lgkmcnt(0)\n"
"v_mfma_f32_16x16x32_bf16 v[32:35], v[224:227], a[32:35], v[32:35]\n"
"ds_read_b64_tr_b16 v[232:233], v251 offset:33416\n"
"v_mfma_f32_16x16x32_bf16 v[32:35], v[228:231], a[48:51], v[32:35]\n"
"ds_read_b64_tr_b16 v[234:235], v251 offset:33672\n"
"v_mfma_f32_16x16x32_bf16 v[40:43], v[224:227], a[36:39], v[40:43]\n"
"ds_read_b64_tr_b16 v[236:237], v251 offset:33928\n"
"s_mov_b32 s70, 12672\n"
"v_mfma_f32_16x16x32_bf16 v[40:43], v[228:231], a[52:55], v[40:43]\n"
"ds_read_b64_tr_b16 v[238:239], v251 offset:34184\n"
"s_add_u32 m0, s74, 0\n"
"v_mfma_f32_16x16x32_bf16 v[48:51], v[224:227], a[40:43], v[48:51]\n"
"buffer_load_dwordx4 v253, s[36:39], s70 offen offset:2048 lds\n"
"s_mov_b32 s69, 1186800\n"
"v_mfma_f32_16x16x32_bf16 v[48:51], v[228:231], a[56:59], v[48:51]\n"
"s_branch .Lpo1\n"
".Lpg1:\n"
"s_setpc_b64 s[78:79]\n"
".Lpo1:\n"
"s_add_u32 m0, s68, 20800\n"
"v_mfma_f32_16x16x32_bf16 v[56:59], v[224:227], a[44:47], v[56:59]\n"
"buffer_load_dwordx4 v252, s[40:43], s69 offen offset:1040 lds\n"
"v_mfma_f32_16x16x32_bf16 v[56:59], v[228:231], a[60:63], v[56:59]\n"
"s_waitcnt lgkmcnt(0)\n"
"v_mfma_f32_16x16x32_bf16 v[60:63], v[232:235], a[44:47], v[60:63]\n"
"ds_read_b64_tr_b16 v[224:225], v251 offset:33440\n"
"v_mfma_f32_16x16x32_bf16 v[60:63], v[236:239], a[60:63], v[60:63]\n"
"ds_read_b64_tr_b16 v[226:227], v251 offset:33696\n"
"v_mfma_f32_16x16x32_bf16 v[52:55], v[232:235], a[40:43], v[52:55]\n"
"ds_read_b64_tr_b16 v[228:229], v251 offset:33952\n"
"v_mfma_f32_16x16x32_bf16 v[52:55], v[236:239], a[56:59], v[52:55]\n"
"ds_read_b64_tr_b16 v[230:231], v251 offset:34208\n"
"v_mfma_f32_16x16x32_bf16 v[44:47], v[232:235], a[36:39], v[44:47]\n"
"s_add_u32 s20, s14, s77\n"
"s_addc_u32 s21, s15, 0\n"
"v_mfma_f32_16x16x32_bf16 v[44:47], v[236:239], a[52:55], v[44:47]\n"
"s_mov_b32 s70, 1120224\n"
"s_and_b32 s21, s21, 0xffff\n"
"v_mfma_f32_16x16x32_bf16 v[36:39], v[232:235], a[32:35], v[36:39]\n"
"buffer_load_dwordx4 v252, s[40:43], s70 offen offset:2080 lds\n"
"s_mov_b32 s23, 0x20000\n"
"v_mfma_f32_16x16x32_bf16 v[36:39], v[236:239], a[48:51], v[36:39]\n"
"s_waitcnt lgkmcnt(0)\n"
"v_mfma_f32_16x16x32_bf16 a[224:227], v[224:227], a[32:35], a[224:227]\n"
"ds_read_b64_tr_b16 v[232:233], v251 offset:33448\n"
"v_mfma_f32_16x16x32_bf16 a[224:227], v[228:231], a[48:51], a[224:227]\n"
"ds_read_b64_tr_b16 v[234:235], v251 offset:33704\n"
"v_mfma_f32_16x16x32_bf16 a[232:235], v[224:227], a[36:39], a[232:235]\n"
"ds_read_b64_tr_b16 v[236:237], v251 offset:33960\n"
"s_mov_b32 s69, 18816\n"
"v_mfma_f32_16x16x32_bf16 a[232:235], v[228:231], a[52:55], a[232:235]\n"
"ds_read_b64_tr_b16 v[238:239], v251 offset:34216\n"
"s_add_u32 m0, s74, 0\n"
"v_mfma_f32_16x16x32_bf16 a[240:243], v[224:227], a[40:43], a[240:243]\n"
"buffer_load_dwordx4 v253, s[36:39], s69 offen offset:3072 lds\n"
"s_mov_b32 s70, 1250256\n"
"v_mfma_f32_16x16x32_bf16 a[240:243], v[228:231], a[56:59], a[240:243]\n"
"s_add_u32 m0, s68, 20800\n"
"buffer_load_dword v250, v250, s[20:23], 0 offen offset:0 sc1\n"
"v_mfma_f32_16x16x32_bf16 a[248:251], v[224:227], a[44:47], a[248:251]\n"
"buffer_load_dwordx4 v252, s[40:43], s70 offen offset:3120 lds\n"
"v_mfma_f32_16x16x32_bf16 a[248:251], v[228:231], a[60:63], a[248:251]\n"
"s_waitcnt vmcnt(13) lgkmcnt(0)\n"
"s_barrier\n"
"v_mfma_f32_16x16x32_bf16 a[252:255], v[232:235], a[44:47], a[252:255]\n"
"ds_read_b64_tr_b16 v[224:225], v251 offset:0\n"
"v_mfma_f32_16x16x32_bf16 a[252:255], v[236:239], a[60:63], a[252:255]\n"
"ds_read_b64_tr_b16 v[226:227], v251 offset:256\n"
"v_mfma_f32_16x16x32_bf16 a[244:247], v[232:235], a[40:43], a[244:247]\n"
"ds_read_b64_tr_b16 v[228:229], v251 offset:512\n"
"v_mfma_f32_16x16x32_bf16 a[244:247], v[236:239], a[56:59], a[244:247]\n"
"ds_read_b64_tr_b16 v[230:231], v251 offset:768\n"
"v_mfma_f32_16x16x32_bf16 a[236:239], v[232:235], a[36:39], a[236:239]\n"
"s_mov_b32 s69, 1048832\n"
"v_mfma_f32_16x16x32_bf16 a[236:239], v[236:239], a[52:55], a[236:239]\n"
"s_add_u32 m0, s68, 33280\n"
"v_mfma_f32_16x16x32_bf16 a[228:231], v[232:235], a[32:35], a[228:231]\n"
"buffer_load_dwordx4 v252, s[40:43], s69 offen offset:0 lds\n"
"v_mfma_f32_16x16x32_bf16 a[228:231], v[236:239], a[48:51], a[228:231]\n"
"s_waitcnt lgkmcnt(0)\n"
"v_mfma_f32_16x16x32_bf16 v[64:67], v[224:227], a[32:35], v[64:67]\n"
"ds_read_b64_tr_b16 v[232:233], v251 offset:8\n"
"s_mov_b32 s35, 65536\n"
"v_mfma_f32_16x16x32_bf16 v[64:67], v[228:231], a[48:51], v[64:67]\n"
"ds_read_b64_tr_b16 v[234:235], v251 offset:264\n"
"s_lshl_b32 s77, s88, 4\n"
"v_mfma_f32_16x16x32_bf16 v[72:75], v[224:227], a[36:39], v[72:75]\n"
"ds_read_b64_tr_b16 v[236:237], v251 offset:520\n"
"s_or_b32 s77, s77, s82\n"
"v_mfma_f32_16x16x32_bf16 v[72:75], v[228:231], a[52:55], v[72:75]\n"
"ds_read_b64_tr_b16 v[238:239], v251 offset:776\n"
"s_lshl_b32 s77, s77, 18\n"
"v_mfma_f32_16x16x32_bf16 v[80:83], v[224:227], a[40:43], v[80:83]\n"
"s_lshl_b32 s80, s85, 13\n"
"s_add_u32 s77, s77, s80\n"
"v_mfma_f32_16x16x32_bf16 v[80:83], v[228:231], a[56:59], v[80:83]\n"
"ds_read_b128 a[64:67], v243 offset:16640\n"
"s_mov_b32 s70, 1178864\n"
"v_mfma_f32_16x16x32_bf16 v[88:91], v[224:227], a[44:47], v[88:91]\n"
"buffer_load_dwordx4 v252, s[40:43], s70 offen offset:1040 lds\n"
"v_mfma_f32_16x16x32_bf16 v[88:91], v[228:231], a[60:63], v[88:91]\n"
"ds_read_b128 a[68:71], v243 offset:18688\n"
"s_waitcnt lgkmcnt(2)\n"
"v_mfma_f32_16x16x32_bf16 v[92:95], v[232:235], a[44:47], v[92:95]\n"
"ds_read_b64_tr_b16 v[224:225], v251 offset:32\n"
"v_mfma_f32_16x16x32_bf16 v[92:95], v[236:239], a[60:63], v[92:95]\n"
"ds_read_b64_tr_b16 v[226:227], v251 offset:288\n"
"v_mfma_f32_16x16x32_bf16 v[84:87], v[232:235], a[40:43], v[84:87]\n"
"ds_read_b64_tr_b16 v[228:229], v251 offset:544\n"
"v_mfma_f32_16x16x32_bf16 v[84:87], v[236:239], a[56:59], v[84:87]\n"
"ds_read_b64_tr_b16 v[230:231], v251 offset:800\n"
"v_mfma_f32_16x16x32_bf16 v[76:79], v[232:235], a[36:39], v[76:79]\n"
"s_sub_u32 s6, s6, 48\n"
"s_subb_u32 s7, s7, 0\n"
"v_mfma_f32_16x16x32_bf16 v[76:79], v[236:239], a[52:55], v[76:79]\n"
"s_mov_b32 s69, 1112288\n"
"v_mfma_f32_16x16x32_bf16 v[68:71], v[232:235], a[32:35], v[68:71]\n"
"buffer_load_dwordx4 v252, s[40:43], s69 offen offset:2080 lds\n"
"v_mfma_f32_16x16x32_bf16 v[68:71], v[236:239], a[48:51], v[68:71]\n"
"ds_read_b128 a[72:75], v244 offset:16640\n"
"s_waitcnt lgkmcnt(1)\n"
"v_mfma_f32_16x16x32_bf16 v[192:195], v[224:227], a[32:35], v[192:195]\n"
"ds_read_b64_tr_b16 v[232:233], v251 offset:40\n"
"v_mfma_f32_16x16x32_bf16 v[192:195], v[228:231], a[48:51], v[192:195]\n"
"ds_read_b64_tr_b16 v[234:235], v251 offset:296\n"
"v_mfma_f32_16x16x32_bf16 v[200:203], v[224:227], a[36:39], v[200:203]\n"
"ds_read_b64_tr_b16 v[236:237], v251 offset:552\n"
"v_mfma_f32_16x16x32_bf16 v[200:203], v[228:231], a[52:55], v[200:203]\n"
"ds_read_b64_tr_b16 v[238:239], v251 offset:808\n"
"v_mfma_f32_16x16x32_bf16 v[208:211], v[224:227], a[40:43], v[208:211]\n"
"s_sub_u32 s8, s8, 48\n"
"s_subb_u32 s9, s9, 0\n"
"v_mfma_f32_16x16x32_bf16 v[208:211], v[228:231], a[56:59], v[208:211]\n"
"s_mov_b32 s70, 1242320\n"
"s_sub_u32 s54, 262192, s80\n"
"v_mfma_f32_16x16x32_bf16 v[216:219], v[224:227], a[44:47], v[216:219]\n"
"buffer_load_dwordx4 v252, s[40:43], s70 offen offset:3120 lds\n"
"v_mfma_f32_16x16x32_bf16 v[216:219], v[228:231], a[60:63], v[216:219]\n"
"ds_read_b128 a[76:79], v244 offset:18688\n"
"s_waitcnt lgkmcnt(1)\n"
"v_mfma_f32_16x16x32_bf16 v[220:223], v[232:235], a[44:47], v[220:223]\n"
"ds_read_b64_tr_b16 v[224:225], v251 offset:128\n"
"v_mfma_f32_16x16x32_bf16 v[220:223], v[236:239], a[60:63], v[220:223]\n"
"ds_read_b64_tr_b16 v[226:227], v251 offset:384\n"
"v_mfma_f32_16x16x32_bf16 v[212:215], v[232:235], a[40:43], v[212:215]\n"
"ds_read_b64_tr_b16 v[228:229], v251 offset:640\n"
"v_mfma_f32_16x16x32_bf16 v[212:215], v[236:239], a[56:59], v[212:215]\n"
"ds_read_b64_tr_b16 v[230:231], v251 offset:896\n"
"v_mfma_f32_16x16x32_bf16 v[204:207], v[232:235], a[36:39], v[204:207]\n"
"s_mov_b32 s69, 1057024\n"
"v_mfma_f32_16x16x32_bf16 v[204:207], v[236:239], a[52:55], v[204:207]\n"
"s_add_u32 m0, s68, 37440\n"
"v_mfma_f32_16x16x32_bf16 v[196:199], v[232:235], a[32:35], v[196:199]\n"
"buffer_load_dwordx4 v252, s[40:43], s69 offen offset:0 lds\n"
"v_mfma_f32_16x16x32_bf16 v[196:199], v[236:239], a[48:51], v[196:199]\n"
"ds_read_b128 a[80:83], v245 offset:16640\n"
"s_waitcnt lgkmcnt(1)\n"
"v_mfma_f32_16x16x32_bf16 v[96:99], v[224:227], a[32:35], v[96:99]\n"
"ds_read_b64_tr_b16 v[232:233], v251 offset:136\n"
"v_mfma_f32_16x16x32_bf16 v[96:99], v[228:231], a[48:51], v[96:99]\n"
"ds_read_b64_tr_b16 v[234:235], v251 offset:392\n"
"v_mfma_f32_16x16x32_bf16 v[104:107], v[224:227], a[36:39], v[104:107]\n"
"ds_read_b64_tr_b16 v[236:237], v251 offset:648\n"
"v_mfma_f32_16x16x32_bf16 v[104:107], v[228:231], a[52:55], v[104:107]\n"
"ds_read_b64_tr_b16 v[238:239], v251 offset:904\n"
"v_mfma_f32_16x16x32_bf16 v[112:115], v[224:227], a[40:43], v[112:115]\n"
"s_add_u32 s52, s8, s77\n"
"s_addc_u32 s53, s9, 0\n"
"v_mfma_f32_16x16x32_bf16 v[112:115], v[228:231], a[56:59], v[112:115]\n"
"s_mov_b32 s70, 1187056\n"
"s_and_b32 s53, s53, 0xffff\n"
"v_mfma_f32_16x16x32_bf16 v[120:123], v[224:227], a[44:47], v[120:123]\n"
"buffer_load_dwordx4 v252, s[40:43], s70 offen offset:1040 lds\n"
"s_mov_b32 s55, 0x20000\n"
"v_mfma_f32_16x16x32_bf16 v[120:123], v[228:231], a[60:63], v[120:123]\n"
"ds_read_b128 a[84:87], v245 offset:18688\n"
"s_waitcnt lgkmcnt(1)\n"
"v_mfma_f32_16x16x32_bf16 v[124:127], v[232:235], a[44:47], v[124:127]\n"
"ds_read_b64_tr_b16 v[224:225], v251 offset:160\n"
"s_mov_b32 s46, s54\n"
"v_mfma_f32_16x16x32_bf16 v[124:127], v[236:239], a[60:63], v[124:127]\n"
"ds_read_b64_tr_b16 v[226:227], v251 offset:416\n"
"v_mfma_f32_16x16x32_bf16 v[116:119], v[232:235], a[40:43], v[116:119]\n"
"ds_read_b64_tr_b16 v[228:229], v251 offset:672\n"
"v_mfma_f32_16x16x32_bf16 v[116:119], v[236:239], a[56:59], v[116:119]\n"
"ds_read_b64_tr_b16 v[230:231], v251 offset:928\n"
"v_mfma_f32_16x16x32_bf16 v[108:111], v[232:235], a[36:39], v[108:111]\n"
"s_add_u32 s44, s6, s77\n"
"s_addc_u32 s45, s7, 0\n"
"v_mfma_f32_16x16x32_bf16 v[108:111], v[236:239], a[52:55], v[108:111]\n"
"ds_read_b128 a[88:91], v246 offset:16640\n"
"s_mov_b32 s69, 1120480\n"
"v_mfma_f32_16x16x32_bf16 v[100:103], v[232:235], a[32:35], v[100:103]\n"
"buffer_load_dwordx4 v252, s[40:43], s69 offen offset:2080 lds\n"
"s_and_b32 s45, s45, 0xffff\n"
"v_mfma_f32_16x16x32_bf16 v[100:103], v[236:239], a[48:51], v[100:103]\n"
"ds_read_b128 a[92:95], v246 offset:18688\n"
"s_waitcnt lgkmcnt(2)\n"
"v_mfma_f32_16x16x32_bf16 v[160:163], v[224:227], a[32:35], v[160:163]\n"
"ds_read_b64_tr_b16 v[232:233], v251 offset:168\n"
"s_mov_b32 s47, 0x20000\n"
"v_mfma_f32_16x16x32_bf16 v[160:163], v[228:231], a[48:51], v[160:163]\n"
"ds_read_b64_tr_b16 v[234:235], v251 offset:424\n"
"s_add_u32 s77, s77, 4096\n"
"v_mfma_f32_16x16x32_bf16 v[168:171], v[224:227], a[36:39], v[168:171]\n"
"ds_read_b64_tr_b16 v[236:237], v251 offset:680\n"
"s_sub_u32 s58, s54, 4096\n"
"v_mfma_f32_16x16x32_bf16 v[168:171], v[228:231], a[52:55], v[168:171]\n"
"ds_read_b64_tr_b16 v[238:239], v251 offset:936\n"
"v_mfma_f32_16x16x32_bf16 v[176:179], v[224:227], a[40:43], v[176:179]\n"
"s_add_u32 s56, s8, s77\n"
"s_addc_u32 s57, s9, 0\n"
"v_mfma_f32_16x16x32_bf16 v[176:179], v[228:231], a[56:59], v[176:179]\n"
"s_mov_b32 s70, 1250512\n"
"s_and_b32 s57, s57, 0xffff\n"
"v_mfma_f32_16x16x32_bf16 v[184:187], v[224:227], a[44:47], v[184:187]\n"
"buffer_load_dwordx4 v252, s[40:43], s70 offen offset:3120 lds\n"
"s_mov_b32 s59, 0x20000\n"
"v_mfma_f32_16x16x32_bf16 v[184:187], v[228:231], a[60:63], v[184:187]\n"
"s_waitcnt vmcnt(8) lgkmcnt(0)\n"
"s_barrier\n"
"v_mfma_f32_16x16x32_bf16 v[188:191], v[232:235], a[44:47], v[188:191]\n"
"ds_read_b64_tr_b16 v[224:225], v251 offset:16640\n"
"s_mov_b32 s50, s58\n"
"v_mfma_f32_16x16x32_bf16 v[188:191], v[236:239], a[60:63], v[188:191]\n"
"ds_read_b64_tr_b16 v[226:227], v251 offset:16896\n"
"v_mfma_f32_16x16x32_bf16 v[180:183], v[232:235], a[40:43], v[180:183]\n"
"ds_read_b64_tr_b16 v[228:229], v251 offset:17152\n"
"v_mfma_f32_16x16x32_bf16 v[180:183], v[236:239], a[56:59], v[180:183]\n"
"ds_read_b64_tr_b16 v[230:231], v251 offset:17408\n"
"v_mfma_f32_16x16x32_bf16 v[172:175], v[232:235], a[36:39], v[172:175]\n"
"s_mov_b32 s69, 1572864\n"
"v_mfma_f32_16x16x32_bf16 v[172:175], v[236:239], a[52:55], v[172:175]\n"
"s_add_u32 m0, s68, 0\n"
"v_mfma_f32_16x16x32_bf16 v[164:167], v[232:235], a[32:35], v[164:167]\n"
"buffer_load_dwordx4 v252, s[40:43], s69 offen offset:0 lds\n"
"v_mfma_f32_16x16x32_bf16 v[164:167], v[236:239], a[48:51], v[164:167]\n"
"s_waitcnt lgkmcnt(0)\n"
"v_mfma_f32_16x16x32_bf16 v[0:3], v[224:227], a[64:67], v[0:3]\n"
"ds_read_b64_tr_b16 v[232:233], v251 offset:16648\n"
"v_mfma_f32_16x16x32_bf16 v[0:3], v[228:231], a[80:83], v[0:3]\n"
"ds_read_b64_tr_b16 v[234:235], v251 offset:16904\n"
"v_mfma_f32_16x16x32_bf16 v[8:11], v[224:227], a[68:71], v[8:11]\n"
"ds_read_b64_tr_b16 v[236:237], v251 offset:17160\n"
"s_mov_b32 s70, 512\n"
"v_mfma_f32_16x16x32_bf16 v[8:11], v[228:231], a[84:87], v[8:11]\n"
"ds_read_b64_tr_b16 v[238:239], v251 offset:17416\n"
"s_add_u32 m0, s74, 16640\n"
"v_mfma_f32_16x16x32_bf16 v[16:19], v[224:227], a[72:75], v[16:19]\n"
"buffer_load_dwordx4 v253, s[36:39], s70 offen offset:0 lds\n"
"s_mov_b32 s69, 1702896\n"
"v_mfma_f32_16x16x32_bf16 v[16:19], v[228:231], a[88:91], v[16:19]\n"
"ds_read_b128 a[96:99], v243 offset:0\n"
"s_add_u32 m0, s68, 0\n"
"v_mfma_f32_16x16x32_bf16 v[24:27], v[224:227], a[76:79], v[24:27]\n"
"buffer_load_dwordx4 v252, s[40:43], s69 offen offset:1040 lds\n"
"v_mfma_f32_16x16x32_bf16 v[24:27], v[228:231], a[92:95], v[24:27]\n"
"ds_read_b128 a[100:103], v243 offset:2048\n"
"s_waitcnt lgkmcnt(2)\n"
"v_mfma_f32_16x16x32_bf16 v[28:31], v[232:235], a[76:79], v[28:31]\n"
"ds_read_b64_tr_b16 v[224:225], v251 offset:16672\n"
"v_mfma_f32_16x16x32_bf16 v[28:31], v[236:239], a[92:95], v[28:31]\n"
"ds_read_b64_tr_b16 v[226:227], v251 offset:16928\n"
"v_mfma_f32_16x16x32_bf16 v[20:23], v[232:235], a[72:75], v[20:23]\n"
"ds_read_b64_tr_b16 v[228:229], v251 offset:17184\n"
"v_mfma_f32_16x16x32_bf16 v[20:23], v[236:239], a[88:91], v[20:23]\n"
"ds_read_b64_tr_b16 v[230:231], v251 offset:17440\n"
"v_mfma_f32_16x16x32_bf16 v[12:15], v[232:235], a[68:71], v[12:15]\n"
"s_add_u32 s48, s6, s77\n"
"s_addc_u32 s49, s7, 0\n"
"v_mfma_f32_16x16x32_bf16 v[12:15], v[236:239], a[84:87], v[12:15]\n"
"s_mov_b32 s70, 1636320\n"
"s_and_b32 s49, s49, 0xffff\n"
"v_mfma_f32_16x16x32_bf16 v[4:7], v[232:235], a[64:67], v[4:7]\n"
"buffer_load_dwordx4 v252, s[40:43], s70 offen offset:2080 lds\n"
"s_mov_b32 s51, 0x20000\n"
"v_mfma_f32_16x16x32_bf16 v[4:7], v[236:239], a[80:83], v[4:7]\n"
"ds_read_b128 a[104:107], v244 offset:0\n"
"s_waitcnt lgkmcnt(1)\n"
"v_mfma_f32_16x16x32_bf16 v[128:131], v[224:227], a[64:67], v[128:131]\n"
"ds_read_b64_tr_b16 v[232:233], v251 offset:16680\n"
"s_mov_b32 s72, 0xffffc000\n"
"v_mfma_f32_16x16x32_bf16 v[128:131], v[228:231], a[80:83], v[128:131]\n"
"ds_read_b64_tr_b16 v[234:235], v251 offset:16936\n"
"s_mov_b32 s73, 0xffffc000\n"
"v_mfma_f32_16x16x32_bf16 v[136:139], v[224:227], a[68:71], v[136:139]\n"
"ds_read_b64_tr_b16 v[236:237], v251 offset:17192\n"
"s_mov_b32 s69, 6656\n"
"v_mfma_f32_16x16x32_bf16 v[136:139], v[228:231], a[84:87], v[136:139]\n"
"ds_read_b64_tr_b16 v[238:239], v251 offset:17448\n"
"s_add_u32 m0, s74, 16640\n"
"v_mfma_f32_16x16x32_bf16 v[144:147], v[224:227], a[72:75], v[144:147]\n"
"buffer_load_dwordx4 v253, s[36:39], s69 offen offset:1024 lds\n"
"s_mov_b32 s70, 1766352\n"
"v_mfma_f32_16x16x32_bf16 v[144:147], v[228:231], a[88:91], v[144:147]\n"
"s_add_u32 m0, s68, 0\n"
"s_mov_b32 s71, 0x3db8aa3b\n"
"v_mfma_f32_16x16x32_bf16 v[152:155], v[224:227], a[76:79], v[152:155]\n"
"buffer_load_dwordx4 v252, s[40:43], s70 offen offset:3120 lds\n"
"s_mov_b32 s76, 0x41000000\n"
"v_mfma_f32_16x16x32_bf16 v[152:155], v[228:231], a[92:95], v[152:155]\n"
"ds_read_b128 a[108:111], v244 offset:2048\n"
"s_waitcnt lgkmcnt(1)\n"
"v_mfma_f32_16x16x32_bf16 v[156:159], v[232:235], a[76:79], v[156:159]\n"
"ds_read_b64_tr_b16 v[224:225], v251 offset:16768\n"
"s_lshl_b32 s77, s86, 3\n"
"v_mfma_f32_16x16x32_bf16 v[156:159], v[236:239], a[92:95], v[156:159]\n"
"ds_read_b64_tr_b16 v[226:227], v251 offset:17024\n"
"s_and_b32 s80, s87, 2\n"
"v_mfma_f32_16x16x32_bf16 v[148:151], v[232:235], a[72:75], v[148:151]\n"
"ds_read_b64_tr_b16 v[228:229], v251 offset:17280\n"
"s_lshl_b32 s80, s80, 1\n"
"v_mfma_f32_16x16x32_bf16 v[148:151], v[236:239], a[88:91], v[148:151]\n"
"ds_read_b64_tr_b16 v[230:231], v251 offset:17536\n"
"s_or_b32 s77, s77, s80\n"
"v_mfma_f32_16x16x32_bf16 v[140:143], v[232:235], a[68:71], v[140:143]\n"
"s_mov_b32 s69, 1581056\n"
"s_lshr_b32 s80, s87, 2\n"
"v_mfma_f32_16x16x32_bf16 v[140:143], v[236:239], a[84:87], v[140:143]\n"
"s_add_u32 m0, s68, 4160\n"
"s_lshl_b32 s80, s80, 1\n"
"v_mfma_f32_16x16x32_bf16 v[132:135], v[232:235], a[64:67], v[132:135]\n"
"buffer_load_dwordx4 v252, s[40:43], s69 offen offset:0 lds\n"
"s_or_b32 s77, s77, s80\n"
"v_mfma_f32_16x16x32_bf16 v[132:135], v[236:239], a[80:83], v[132:135]\n"
"ds_read_b128 a[112:115], v245 offset:0\n"
"s_waitcnt lgkmcnt(1)\n"
"v_mfma_f32_16x16x32_bf16 v[32:35], v[224:227], a[64:67], v[32:35]\n"
"ds_read_b64_tr_b16 v[232:233], v251 offset:16776\n"
"s_and_b32 s80, s87, 1\n"
"v_mfma_f32_16x16x32_bf16 v[32:35], v[228:231], a[80:83], v[32:35]\n"
"ds_read_b64_tr_b16 v[234:235], v251 offset:17032\n"
"s_or_b32 s77, s77, s80\n"
"v_mfma_f32_16x16x32_bf16 v[40:43], v[224:227], a[68:71], v[40:43]\n"
"ds_read_b64_tr_b16 v[236:237], v251 offset:17288\n"
"s_mov_b32 s70, 12800\n"
"v_mfma_f32_16x16x32_bf16 v[40:43], v[228:231], a[84:87], v[40:43]\n"
"ds_read_b64_tr_b16 v[238:239], v251 offset:17544\n"
"s_add_u32 m0, s74, 16640\n"
"v_mfma_f32_16x16x32_bf16 v[48:51], v[224:227], a[72:75], v[48:51]\n"
"buffer_load_dwordx4 v253, s[36:39], s70 offen offset:2048 lds\n"
"s_mov_b32 s69, 1711088\n"
"v_mfma_f32_16x16x32_bf16 v[48:51], v[228:231], a[88:91], v[48:51]\n"
"s_add_u32 m0, s68, 4160\n"
"s_mul_i32 s90, s77, 14336\n"
"v_mfma_f32_16x16x32_bf16 v[56:59], v[224:227], a[76:79], v[56:59]\n"
"buffer_load_dwordx4 v252, s[40:43], s69 offen offset:1040 lds\n"
"s_lshl_b32 s77, s83, 6\n"
"v_mfma_f32_16x16x32_bf16 v[56:59], v[228:231], a[92:95], v[56:59]\n"
"ds_read_b128 a[116:119], v245 offset:2048\n"
"s_waitcnt lgkmcnt(1)\n"
"v_mfma_f32_16x16x32_bf16 v[60:63], v[232:235], a[76:79], v[60:63]\n"
"ds_read_b64_tr_b16 v[224:225], v251 offset:16800\n"
"s_add_u32 s90, s90, s77\n"
"v_mfma_f32_16x16x32_bf16 v[60:63], v[236:239], a[92:95], v[60:63]\n"
"ds_read_b64_tr_b16 v[226:227], v251 offset:17056\n"
"s_lshl2_add_u32 s77, s86, s83\n"
"v_mfma_f32_16x16x32_bf16 v[52:55], v[232:235], a[72:75], v[52:55]\n"
"ds_read_b64_tr_b16 v[228:229], v251 offset:17312\n"
"v_mfma_f32_16x16x32_bf16 v[52:55], v[236:239], a[88:91], v[52:55]\n"
"ds_read_b64_tr_b16 v[230:231], v251 offset:17568\n"
"v_mfma_f32_16x16x32_bf16 v[44:47], v[232:235], a[68:71], v[44:47]\n"
"s_cmp_eq_u32 s77, 7\n"
"s_cselect_b32 s96, 0x7f800000, 0\n"
"v_mfma_f32_16x16x32_bf16 v[44:47], v[236:239], a[84:87], v[44:47]\n"
"ds_read_b128 a[120:123], v246 offset:0\n"
"s_mov_b32 s70, 1644512\n"
"v_mfma_f32_16x16x32_bf16 v[36:39], v[232:235], a[64:67], v[36:39]\n"
"buffer_load_dwordx4 v252, s[40:43], s70 offen offset:2080 lds\n"
"v_mfma_f32_16x16x32_bf16 v[36:39], v[236:239], a[80:83], v[36:39]\n"
"ds_read_b128 a[124:127], v246 offset:2048\n"
"s_waitcnt lgkmcnt(2)\n"
"v_mfma_f32_16x16x32_bf16 a[224:227], v[224:227], a[64:67], a[224:227]\n"
"ds_read_b64_tr_b16 v[232:233], v251 offset:16808\n"
"v_mfma_f32_16x16x32_bf16 a[224:227], v[228:231], a[80:83], a[224:227]\n"
"ds_read_b64_tr_b16 v[234:235], v251 offset:17064\n"
"v_mfma_f32_16x16x32_bf16 a[232:235], v[224:227], a[68:71], a[232:235]\n"
"ds_read_b64_tr_b16 v[236:237], v251 offset:17320\n"
"s_mov_b32 s69, 18944\n"
"v_mfma_f32_16x16x32_bf16 a[232:235], v[228:231], a[84:87], a[232:235]\n"
"ds_read_b64_tr_b16 v[238:239], v251 offset:17576\n"
"s_add_u32 m0, s74, 16640\n"
"v_mfma_f32_16x16x32_bf16 a[240:243], v[224:227], a[72:75], a[240:243]\n"
"buffer_load_dwordx4 v253, s[36:39], s69 offen offset:3072 lds\n"
"s_mov_b32 s70, 1774544\n"
"v_mfma_f32_16x16x32_bf16 a[240:243], v[228:231], a[88:91], a[240:243]\n"
"s_add_u32 m0, s68, 4160\n"
"v_mfma_f32_16x16x32_bf16 a[248:251], v[224:227], a[76:79], a[248:251]\n"
"buffer_load_dwordx4 v252, s[40:43], s70 offen offset:3120 lds\n"
"v_mfma_f32_16x16x32_bf16 a[248:251], v[228:231], a[92:95], a[248:251]\n"
"s_waitcnt vmcnt(12) lgkmcnt(0)\n"
"s_barrier\n"
"v_mfma_f32_16x16x32_bf16 a[252:255], v[232:235], a[76:79], a[252:255]\n"
"ds_read_b64_tr_b16 v[224:225], v251 offset:33280\n"
"v_mfma_f32_16x16x32_bf16 a[252:255], v[236:239], a[92:95], a[252:255]\n"
"ds_read_b64_tr_b16 v[226:227], v251 offset:33536\n"
"v_mfma_f32_16x16x32_bf16 a[244:247], v[232:235], a[72:75], a[244:247]\n"
"ds_read_b64_tr_b16 v[228:229], v251 offset:33792\n"
"v_mfma_f32_16x16x32_bf16 a[244:247], v[236:239], a[88:91], a[244:247]\n"
"ds_read_b64_tr_b16 v[230:231], v251 offset:34048\n"
"v_mfma_f32_16x16x32_bf16 a[236:239], v[232:235], a[68:71], a[236:239]\n"
"s_mov_b32 s69, 1573120\n"
"v_mfma_f32_16x16x32_bf16 a[236:239], v[236:239], a[84:87], a[236:239]\n"
"s_add_u32 m0, s68, 16640\n"
"v_mfma_f32_16x16x32_bf16 a[228:231], v[232:235], a[64:67], a[228:231]\n"
"buffer_load_dwordx4 v252, s[40:43], s69 offen offset:0 lds\n"
"v_mfma_f32_16x16x32_bf16 a[228:231], v[236:239], a[80:83], a[228:231]\n"
"s_waitcnt lgkmcnt(0)\n"
"v_mfma_f32_16x16x32_bf16 v[64:67], v[224:227], a[64:67], v[64:67]\n"
"ds_read_b64_tr_b16 v[232:233], v251 offset:33288\n"
"v_mfma_f32_16x16x32_bf16 v[64:67], v[228:231], a[80:83], v[64:67]\n"
"ds_read_b64_tr_b16 v[234:235], v251 offset:33544\n"
"v_mfma_f32_16x16x32_bf16 v[72:75], v[224:227], a[68:71], v[72:75]\n"
"ds_read_b64_tr_b16 v[236:237], v251 offset:33800\n"
"s_mov_b32 s70, 640\n"
"v_mfma_f32_16x16x32_bf16 v[72:75], v[228:231], a[84:87], v[72:75]\n"
"ds_read_b64_tr_b16 v[238:239], v251 offset:34056\n"
"s_add_u32 m0, s74, 0\n"
"v_mfma_f32_16x16x32_bf16 v[80:83], v[224:227], a[72:75], v[80:83]\n"
"buffer_load_dwordx4 v253, s[36:39], s70 offen offset:0 lds\n"
"s_mov_b32 s69, 1703152\n"
"v_mfma_f32_16x16x32_bf16 v[80:83], v[228:231], a[88:91], v[80:83]\n"
"s_add_u32 m0, s68, 16640\n"
"v_mfma_f32_16x16x32_bf16 v[88:91], v[224:227], a[76:79], v[88:91]\n"
"buffer_load_dwordx4 v252, s[40:43], s69 offen offset:1040 lds\n"
"v_mfma_f32_16x16x32_bf16 v[88:91], v[228:231], a[92:95], v[88:91]\n"
"s_waitcnt lgkmcnt(0)\n"
"v_mfma_f32_16x16x32_bf16 v[92:95], v[232:235], a[76:79], v[92:95]\n"
"ds_read_b64_tr_b16 v[224:225], v251 offset:33312\n"
"v_mfma_f32_16x16x32_bf16 v[92:95], v[236:239], a[92:95], v[92:95]\n"
"ds_read_b64_tr_b16 v[226:227], v251 offset:33568\n"
"v_mfma_f32_16x16x32_bf16 v[84:87], v[232:235], a[72:75], v[84:87]\n"
"ds_read_b64_tr_b16 v[228:229], v251 offset:33824\n"
"v_mfma_f32_16x16x32_bf16 v[84:87], v[236:239], a[88:91], v[84:87]\n"
"ds_read_b64_tr_b16 v[230:231], v251 offset:34080\n"
"v_mfma_f32_16x16x32_bf16 v[76:79], v[232:235], a[68:71], v[76:79]\n"
"s_cmp_eq_u32 s77, 5\n"
"s_cselect_b32 s97, 0x7f800000, 0\n"
"v_mfma_f32_16x16x32_bf16 v[76:79], v[236:239], a[84:87], v[76:79]\n"
"s_mov_b32 s70, 1636576\n"
"s_lshl_b32 s91, s83, 10\n"
"v_mfma_f32_16x16x32_bf16 v[68:71], v[232:235], a[64:67], v[68:71]\n"
"buffer_load_dwordx4 v252, s[40:43], s70 offen offset:2080 lds\n"
"v_mfma_f32_16x16x32_bf16 v[68:71], v[236:239], a[80:83], v[68:71]\n"
"s_waitcnt lgkmcnt(0)\n"
"v_mfma_f32_16x16x32_bf16 v[192:195], v[224:227], a[64:67], v[192:195]\n"
"ds_read_b64_tr_b16 v[232:233], v251 offset:33320\n"
"v_mfma_f32_16x16x32_bf16 v[192:195], v[228:231], a[80:83], v[192:195]\n"
"ds_read_b64_tr_b16 v[234:235], v251 offset:33576\n"
"v_mfma_f32_16x16x32_bf16 v[200:203], v[224:227], a[68:71], v[200:203]\n"
"ds_read_b64_tr_b16 v[236:237], v251 offset:33832\n"
"s_mov_b32 s69, 6784\n"
"v_add_u32_e32 v250, 1, v250\n"
"v_mfma_f32_16x16x32_bf16 v[200:203], v[228:231], a[84:87], v[200:203]\n"
"ds_read_b64_tr_b16 v[238:239], v251 offset:34088\n"
"s_add_u32 m0, s74, 0\n"
"v_mfma_f32_16x16x32_bf16 v[208:211], v[224:227], a[72:75], v[208:211]\n"
"buffer_load_dwordx4 v253, s[36:39], s69 offen offset:1024 lds\n"
"s_mov_b32 s70, 1766608\n"
"v_mfma_f32_16x16x32_bf16 v[208:211], v[228:231], a[88:91], v[208:211]\n"
"s_add_u32 m0, s68, 16640\n"
"v_mfma_f32_16x16x32_bf16 v[216:219], v[224:227], a[76:79], v[216:219]\n"
"buffer_load_dwordx4 v252, s[40:43], s70 offen offset:3120 lds\n"
"v_mfma_f32_16x16x32_bf16 v[216:219], v[228:231], a[92:95], v[216:219]\n"
"s_waitcnt lgkmcnt(0)\n"
"buffer_store_dword v250, v247, s[20:23], s35 offen offset:0 sc1\n"
"v_mfma_f32_16x16x32_bf16 v[220:223], v[232:235], a[76:79], v[220:223]\n"
"ds_read_b64_tr_b16 v[224:225], v251 offset:33408\n"
"v_mfma_f32_16x16x32_bf16 v[220:223], v[236:239], a[92:95], v[220:223]\n"
"ds_read_b64_tr_b16 v[226:227], v251 offset:33664\n"
"v_mfma_f32_16x16x32_bf16 v[212:215], v[232:235], a[72:75], v[212:215]\n"
"ds_read_b64_tr_b16 v[228:229], v251 offset:33920\n"
"v_mfma_f32_16x16x32_bf16 v[212:215], v[236:239], a[88:91], v[212:215]\n"
"ds_read_b64_tr_b16 v[230:231], v251 offset:34176\n"
"v_mfma_f32_16x16x32_bf16 v[204:207], v[232:235], a[68:71], v[204:207]\n"
"s_mov_b32 s69, 1581312\n"
"v_mfma_f32_16x16x32_bf16 v[204:207], v[236:239], a[84:87], v[204:207]\n"
"s_add_u32 m0, s68, 20800\n"
"v_mfma_f32_16x16x32_bf16 v[196:199], v[232:235], a[64:67], v[196:199]\n"
"buffer_load_dwordx4 v252, s[40:43], s69 offen offset:0 lds\n"
"v_mfma_f32_16x16x32_bf16 v[196:199], v[236:239], a[80:83], v[196:199]\n"
"s_waitcnt lgkmcnt(0)\n"
"v_mfma_f32_16x16x32_bf16 v[96:99], v[224:227], a[64:67], v[96:99]\n"
"ds_read_b64_tr_b16 v[232:233], v251 offset:33416\n"
"v_mfma_f32_16x16x32_bf16 v[96:99], v[228:231], a[80:83], v[96:99]\n"
"ds_read_b64_tr_b16 v[234:235], v251 offset:33672\n"
"v_mfma_f32_16x16x32_bf16 v[104:107], v[224:227], a[68:71], v[104:107]\n"
"ds_read_b64_tr_b16 v[236:237], v251 offset:33928\n"
"s_mov_b32 s70, 12928\n"
"v_mfma_f32_16x16x32_bf16 v[104:107], v[228:231], a[84:87], v[104:107]\n"
"ds_read_b64_tr_b16 v[238:239], v251 offset:34184\n"
"s_add_u32 m0, s74, 0\n"
"v_mfma_f32_16x16x32_bf16 v[112:115], v[224:227], a[72:75], v[112:115]\n"
"buffer_load_dwordx4 v253, s[36:39], s70 offen offset:2048 lds\n"
"s_mov_b32 s69, 1711344\n"
"v_mfma_f32_16x16x32_bf16 v[112:115], v[228:231], a[88:91], v[112:115]\n"
"s_add_u32 m0, s68, 20800\n"
"v_mfma_f32_16x16x32_bf16 v[120:123], v[224:227], a[76:79], v[120:123]\n"
"buffer_load_dwordx4 v252, s[40:43], s69 offen offset:1040 lds\n"
"v_mfma_f32_16x16x32_bf16 v[120:123], v[228:231], a[92:95], v[120:123]\n"
"s_waitcnt lgkmcnt(0)\n"
"v_mfma_f32_16x16x32_bf16 v[124:127], v[232:235], a[76:79], v[124:127]\n"
"ds_read_b64_tr_b16 v[224:225], v251 offset:33440\n"
"v_mfma_f32_16x16x32_bf16 v[124:127], v[236:239], a[92:95], v[124:127]\n"
"ds_read_b64_tr_b16 v[226:227], v251 offset:33696\n"
"v_mfma_f32_16x16x32_bf16 v[116:119], v[232:235], a[72:75], v[116:119]\n"
"ds_read_b64_tr_b16 v[228:229], v251 offset:33952\n"
"v_mfma_f32_16x16x32_bf16 v[116:119], v[236:239], a[88:91], v[116:119]\n"
"ds_read_b64_tr_b16 v[230:231], v251 offset:34208\n"
"v_mfma_f32_16x16x32_bf16 v[108:111], v[232:235], a[68:71], v[108:111]\n"
"s_cmp_lt_u32 s83, 2\n"
"s_cselect_b32 s34, 0, 0x10000000\n"
"v_mfma_f32_16x16x32_bf16 v[108:111], v[236:239], a[84:87], v[108:111]\n"
"s_mov_b32 s70, 1644768\n"
"v_mfma_f32_16x16x32_bf16 v[100:103], v[232:235], a[64:67], v[100:103]\n"
"buffer_load_dwordx4 v252, s[40:43], s70 offen offset:2080 lds\n"
"v_mfma_f32_16x16x32_bf16 v[100:103], v[236:239], a[80:83], v[100:103]\n"
"s_waitcnt lgkmcnt(0)\n"
"v_mfma_f32_16x16x32_bf16 v[160:163], v[224:227], a[64:67], v[160:163]\n"
"ds_read_b64_tr_b16 v[232:233], v251 offset:33448\n"
"v_mfma_f32_16x16x32_bf16 v[160:163], v[228:231], a[80:83], v[160:163]\n"
"ds_read_b64_tr_b16 v[234:235], v251 offset:33704\n"
"v_mfma_f32_16x16x32_bf16 v[168:171], v[224:227], a[68:71], v[168:171]\n"
"ds_read_b64_tr_b16 v[236:237], v251 offset:33960\n"
"s_mov_b32 s69, 19072\n"
"v_mfma_f32_16x16x32_bf16 v[168:171], v[228:231], a[84:87], v[168:171]\n"
"ds_read_b64_tr_b16 v[238:239], v251 offset:34216\n"
"s_add_u32 m0, s74, 0\n"
"v_mfma_f32_16x16x32_bf16 v[176:179], v[224:227], a[72:75], v[176:179]\n"
"buffer_load_dwordx4 v253, s[36:39], s69 offen offset:3072 lds\n"
"s_mov_b32 s70, 1774800\n"
"v_mfma_f32_16x16x32_bf16 v[176:179], v[228:231], a[88:91], v[176:179]\n"
"s_add_u32 m0, s68, 20800\n"
"v_mfma_f32_16x16x32_bf16 v[184:187], v[224:227], a[76:79], v[184:187]\n"
"buffer_load_dwordx4 v252, s[40:43], s70 offen offset:3120 lds\n"
"v_mfma_f32_16x16x32_bf16 v[184:187], v[228:231], a[92:95], v[184:187]\n"
"s_waitcnt vmcnt(13) lgkmcnt(0)\n"
"s_barrier\n"
"v_mfma_f32_16x16x32_bf16 v[188:191], v[232:235], a[76:79], v[188:191]\n"
"ds_read_b64_tr_b16 v[224:225], v251 offset:0\n"
"v_mfma_f32_16x16x32_bf16 v[188:191], v[236:239], a[92:95], v[188:191]\n"
"ds_read_b64_tr_b16 v[226:227], v251 offset:256\n"
"v_mfma_f32_16x16x32_bf16 v[180:183], v[232:235], a[72:75], v[180:183]\n"
"ds_read_b64_tr_b16 v[228:229], v251 offset:512\n"
"v_mfma_f32_16x16x32_bf16 v[180:183], v[236:239], a[88:91], v[180:183]\n"
"ds_read_b64_tr_b16 v[230:231], v251 offset:768\n"
"v_mfma_f32_16x16x32_bf16 v[172:175], v[232:235], a[68:71], v[172:175]\n"
"s_mov_b32 s69, 2097152\n"
"v_mfma_f32_16x16x32_bf16 v[172:175], v[236:239], a[84:87], v[172:175]\n"
"s_add_u32 m0, s68, 33280\n"
"v_mfma_f32_16x16x32_bf16 v[164:167], v[232:235], a[64:67], v[164:167]\n"
"buffer_load_dwordx4 v252, s[40:43], s69 offen offset:0 lds\n"
"v_mfma_f32_16x16x32_bf16 v[164:167], v[236:239], a[80:83], v[164:167]\n"
"s_waitcnt lgkmcnt(0)\n"
"v_mfma_f32_16x16x32_bf16 v[0:3], v[224:227], a[96:99], v[0:3]\n"
"ds_read_b64_tr_b16 v[232:233], v251 offset:8\n"
"v_mfma_f32_16x16x32_bf16 v[0:3], v[228:231], a[112:115], v[0:3]\n"
"ds_read_b64_tr_b16 v[234:235], v251 offset:264\n"
"v_mfma_f32_16x16x32_bf16 v[8:11], v[224:227], a[100:103], v[8:11]\n"
"ds_read_b64_tr_b16 v[236:237], v251 offset:520\n"
"v_mfma_f32_16x16x32_bf16 v[8:11], v[228:231], a[116:119], v[8:11]\n"
"ds_read_b64_tr_b16 v[238:239], v251 offset:776\n"
"v_mfma_f32_16x16x32_bf16 v[16:19], v[224:227], a[104:107], v[16:19]\n"
"v_mfma_f32_16x16x32_bf16 v[16:19], v[228:231], a[120:123], v[16:19]\n"
"ds_read_b128 a[128:131], v243 offset:16640\n"
"s_mov_b32 s70, 2227184\n"
"v_mfma_f32_16x16x32_bf16 v[24:27], v[224:227], a[108:111], v[24:27]\n"
"buffer_load_dwordx4 v252, s[40:43], s70 offen offset:1040 lds\n"
"v_mfma_f32_16x16x32_bf16 v[24:27], v[228:231], a[124:127], v[24:27]\n"
"s_branch .Lpo2\n"
".Lpg2:\n"
"s_setpc_b64 s[78:79]\n"
".Lpo2:\n"
"ds_read_b128 a[132:135], v243 offset:18688\n"
"s_waitcnt lgkmcnt(2)\n"
"v_mfma_f32_16x16x32_bf16 v[28:31], v[232:235], a[108:111], v[28:31]\n"
"ds_read_b64_tr_b16 v[224:225], v251 offset:32\n"
"v_mfma_f32_16x16x32_bf16 v[28:31], v[236:239], a[124:127], v[28:31]\n"
"ds_read_b64_tr_b16 v[226:227], v251 offset:288\n"
"v_mfma_f32_16x16x32_bf16 v[20:23], v[232:235], a[104:107], v[20:23]\n"
"ds_read_b64_tr_b16 v[228:229], v251 offset:544\n"
"v_mfma_f32_16x16x32_bf16 v[20:23], v[236:239], a[120:123], v[20:23]\n"
"ds_read_b64_tr_b16 v[230:231], v251 offset:800\n"
"v_mfma_f32_16x16x32_bf16 v[12:15], v[232:235], a[100:103], v[12:15]\n"
"v_mfma_f32_16x16x32_bf16 v[12:15], v[236:239], a[116:119], v[12:15]\n"
"s_mov_b32 s69, 2160608\n"
"v_mfma_f32_16x16x32_bf16 v[4:7], v[232:235], a[96:99], v[4:7]\n"
"buffer_load_dwordx4 v252, s[40:43], s69 offen offset:2080 lds\n"
"v_mfma_f32_16x16x32_bf16 v[4:7], v[236:239], a[112:115], v[4:7]\n"
"ds_read_b128 a[136:139], v244 offset:16640\n"
"s_waitcnt lgkmcnt(1)\n"
"v_mfma_f32_16x16x32_bf16 v[128:131], v[224:227], a[96:99], v[128:131]\n"
"ds_read_b64_tr_b16 v[232:233], v251 offset:40\n"
"v_mfma_f32_16x16x32_bf16 v[128:131], v[228:231], a[112:115], v[128:131]\n"
"ds_read_b64_tr_b16 v[234:235], v251 offset:296\n"
"v_mfma_f32_16x16x32_bf16 v[136:139], v[224:227], a[100:103], v[136:139]\n"
"ds_read_b64_tr_b16 v[236:237], v251 offset:552\n"
"v_mfma_f32_16x16x32_bf16 v[136:139], v[228:231], a[116:119], v[136:139]\n"
"ds_read_b64_tr_b16 v[238:239], v251 offset:808\n"
"v_mfma_f32_16x16x32_bf16 v[144:147], v[224:227], a[104:107], v[144:147]\n"
"v_mfma_f32_16x16x32_bf16 v[144:147], v[228:231], a[120:123], v[144:147]\n"
"s_mov_b32 s70, 2290640\n"
"v_mfma_f32_16x16x32_bf16 v[152:155], v[224:227], a[108:111], v[152:155]\n"
"buffer_load_dwordx4 v252, s[40:43], s70 offen offset:3120 lds\n"
"v_mfma_f32_16x16x32_bf16 v[152:155], v[228:231], a[124:127], v[152:155]\n"
"ds_read_b128 a[140:143], v244 offset:18688\n"
"s_waitcnt lgkmcnt(1)\n"
"v_mfma_f32_16x16x32_bf16 v[156:159], v[232:235], a[108:111], v[156:159]\n"
"ds_read_b64_tr_b16 v[224:225], v251 offset:128\n"
"v_mfma_f32_16x16x32_bf16 v[156:159], v[236:239], a[124:127], v[156:159]\n"
"ds_read_b64_tr_b16 v[226:227], v251 offset:384\n"
"v_mfma_f32_16x16x32_bf16 v[148:151], v[232:235], a[104:107], v[148:151]\n"
"ds_read_b64_tr_b16 v[228:229], v251 offset:640\n"
"v_mfma_f32_16x16x32_bf16 v[148:151], v[236:239], a[120:123], v[148:151]\n"
"ds_read_b64_tr_b16 v[230:231], v251 offset:896\n"
"v_mfma_f32_16x16x32_bf16 v[140:143], v[232:235], a[100:103], v[140:143]\n"
"s_mov_b32 s69, 2105344\n"
"v_mfma_f32_16x16x32_bf16 v[140:143], v[236:239], a[116:119], v[140:143]\n"
"s_add_u32 m0, s68, 37440\n"
"v_mfma_f32_16x16x32_bf16 v[132:135], v[232:235], a[96:99], v[132:135]\n"
"buffer_load_dwordx4 v252, s[40:43], s69 offen offset:0 lds\n"
"v_mfma_f32_16x16x32_bf16 v[132:135], v[236:239], a[112:115], v[132:135]\n"
"ds_read_b128 a[144:147], v245 offset:16640\n"
"s_waitcnt lgkmcnt(1)\n"
"v_mfma_f32_16x16x32_bf16 v[32:35], v[224:227], a[96:99], v[32:35]\n"
"ds_read_b64_tr_b16 v[232:233], v251 offset:136\n"
"v_mfma_f32_16x16x32_bf16 v[32:35], v[228:231], a[112:115], v[32:35]\n"
"ds_read_b64_tr_b16 v[234:235], v251 offset:392\n"
"v_mfma_f32_16x16x32_bf16 v[40:43], v[224:227], a[100:103], v[40:43]\n"
"ds_read_b64_tr_b16 v[236:237], v251 offset:648\n"
"v_mfma_f32_16x16x32_bf16 v[40:43], v[228:231], a[116:119], v[40:43]\n"
"ds_read_b64_tr_b16 v[238:239], v251 offset:904\n"
"v_mfma_f32_16x16x32_bf16 v[48:51], v[224:227], a[104:107], v[48:51]\n"
"v_mfma_f32_16x16x32_bf16 v[48:51], v[228:231], a[120:123], v[48:51]\n"
"s_mov_b32 s70, 2235376\n"
"v_mfma_f32_16x16x32_bf16 v[56:59], v[224:227], a[108:111], v[56:59]\n"
"buffer_load_dwordx4 v252, s[40:43], s70 offen offset:1040 lds\n"
"v_mfma_f32_16x16x32_bf16 v[56:59], v[228:231], a[124:127], v[56:59]\n"
"ds_read_b128 a[148:151], v245 offset:18688\n"
"s_waitcnt lgkmcnt(1)\n"
"v_mfma_f32_16x16x32_bf16 v[60:63], v[232:235], a[108:111], v[60:63]\n"
"ds_read_b64_tr_b16 v[224:225], v251 offset:160\n"
"v_mfma_f32_16x16x32_bf16 v[60:63], v[236:239], a[124:127], v[60:63]\n"
"ds_read_b64_tr_b16 v[226:227], v251 offset:416\n"
"v_mfma_f32_16x16x32_bf16 v[52:55], v[232:235], a[104:107], v[52:55]\n"
"ds_read_b64_tr_b16 v[228:229], v251 offset:672\n"
"v_mfma_f32_16x16x32_bf16 v[52:55], v[236:239], a[120:123], v[52:55]\n"
"ds_read_b64_tr_b16 v[230:231], v251 offset:928\n"
"v_mfma_f32_16x16x32_bf16 v[44:47], v[232:235], a[100:103], v[44:47]\n"
"v_mfma_f32_16x16x32_bf16 v[44:47], v[236:239], a[116:119], v[44:47]\n"
"ds_read_b128 a[152:155], v246 offset:16640\n"
"s_mov_b32 s69, 2168800\n"
"v_mfma_f32_16x16x32_bf16 v[36:39], v[232:235], a[96:99], v[36:39]\n"
"buffer_load_dwordx4 v252, s[40:43], s69 offen offset:2080 lds\n"
"v_mfma_f32_16x16x32_bf16 v[36:39], v[236:239], a[112:115], v[36:39]\n"
"ds_read_b128 a[156:159], v246 offset:18688\n"
"s_waitcnt lgkmcnt(2)\n"
"v_mfma_f32_16x16x32_bf16 a[224:227], v[224:227], a[96:99], a[224:227]\n"
"ds_read_b64_tr_b16 v[232:233], v251 offset:168\n"
"v_mfma_f32_16x16x32_bf16 a[224:227], v[228:231], a[112:115], a[224:227]\n"
"ds_read_b64_tr_b16 v[234:235], v251 offset:424\n"
"v_mfma_f32_16x16x32_bf16 a[232:235], v[224:227], a[100:103], a[232:235]\n"
"ds_read_b64_tr_b16 v[236:237], v251 offset:680\n"
"v_mfma_f32_16x16x32_bf16 a[232:235], v[228:231], a[116:119], a[232:235]\n"
"ds_read_b64_tr_b16 v[238:239], v251 offset:936\n"
"v_mfma_f32_16x16x32_bf16 a[240:243], v[224:227], a[104:107], a[240:243]\n"
"v_mfma_f32_16x16x32_bf16 a[240:243], v[228:231], a[120:123], a[240:243]\n"
"s_mov_b32 s70, 2298832\n"
"v_mfma_f32_16x16x32_bf16 a[248:251], v[224:227], a[108:111], a[248:251]\n"
"buffer_load_dwordx4 v252, s[40:43], s70 offen offset:3120 lds\n"
"v_mfma_f32_16x16x32_bf16 a[248:251], v[228:231], a[124:127], a[248:251]\n"
"s_waitcnt vmcnt(8) lgkmcnt(0)\n"
"s_barrier\n"
"v_mfma_f32_16x16x32_bf16 a[252:255], v[232:235], a[108:111], a[252:255]\n"
"ds_read_b64_tr_b16 v[224:225], v251 offset:16640\n"
"v_mfma_f32_16x16x32_bf16 a[252:255], v[236:239], a[124:127], a[252:255]\n"
"ds_read_b64_tr_b16 v[226:227], v251 offset:16896\n"
"v_mfma_f32_16x16x32_bf16 a[244:247], v[232:235], a[104:107], a[244:247]\n"
"ds_read_b64_tr_b16 v[228:229], v251 offset:17152\n"
"v_mfma_f32_16x16x32_bf16 a[244:247], v[236:239], a[120:123], a[244:247]\n"
"ds_read_b64_tr_b16 v[230:231], v251 offset:17408\n"
"v_mfma_f32_16x16x32_bf16 a[236:239], v[232:235], a[100:103], a[236:239]\n"
"s_mov_b32 s69, 2621440\n"
"v_mfma_f32_16x16x32_bf16 a[236:239], v[236:239], a[116:119], a[236:239]\n"
"s_add_u32 m0, s68, 0\n"
"v_mfma_f32_16x16x32_bf16 a[228:231], v[232:235], a[96:99], a[228:231]\n"
"buffer_load_dwordx4 v252, s[40:43], s69 offen offset:0 lds\n"
"v_mfma_f32_16x16x32_bf16 a[228:231], v[236:239], a[112:115], a[228:231]\n"
"s_waitcnt lgkmcnt(0)\n"
"v_mfma_f32_16x16x32_bf16 v[64:67], v[224:227], a[96:99], v[64:67]\n"
"ds_read_b64_tr_b16 v[232:233], v251 offset:16648\n"
"v_mfma_f32_16x16x32_bf16 v[64:67], v[228:231], a[112:115], v[64:67]\n"
"ds_read_b64_tr_b16 v[234:235], v251 offset:16904\n"
"v_mfma_f32_16x16x32_bf16 v[72:75], v[224:227], a[100:103], v[72:75]\n"
"ds_read_b64_tr_b16 v[236:237], v251 offset:17160\n"
"s_mov_b32 s70, 768\n"
"v_mfma_f32_16x16x32_bf16 v[72:75], v[228:231], a[116:119], v[72:75]\n"
"ds_read_b64_tr_b16 v[238:239], v251 offset:17416\n"
"s_add_u32 m0, s74, 16640\n"
"v_mfma_f32_16x16x32_bf16 v[80:83], v[224:227], a[104:107], v[80:83]\n"
"buffer_load_dwordx4 v253, s[36:39], s70 offen offset:0 lds\n"
"s_mov_b32 s69, 2751472\n"
"v_mfma_f32_16x16x32_bf16 v[80:83], v[228:231], a[120:123], v[80:83]\n"
"ds_read_b128 a[160:163], v243 offset:0\n"
"s_add_u32 m0, s68, 0\n"
"v_mfma_f32_16x16x32_bf16 v[88:91], v[224:227], a[108:111], v[88:91]\n"
"buffer_load_dwordx4 v252, s[40:43], s69 offen offset:1040 lds\n"
"v_mfma_f32_16x16x32_bf16 v[88:91], v[228:231], a[124:127], v[88:91]\n"
"ds_read_b128 a[164:167], v243 offset:2048\n"
"s_waitcnt lgkmcnt(2)\n"
"v_mfma_f32_16x16x32_bf16 v[92:95], v[232:235], a[108:111], v[92:95]\n"
"ds_read_b64_tr_b16 v[224:225], v251 offset:16672\n"
"v_mfma_f32_16x16x32_bf16 v[92:95], v[236:239], a[124:127], v[92:95]\n"
"ds_read_b64_tr_b16 v[226:227], v251 offset:16928\n"
"v_mfma_f32_16x16x32_bf16 v[84:87], v[232:235], a[104:107], v[84:87]\n"
"ds_read_b64_tr_b16 v[228:229], v251 offset:17184\n"
"v_mfma_f32_16x16x32_bf16 v[84:87], v[236:239], a[120:123], v[84:87]\n"
"ds_read_b64_tr_b16 v[230:231], v251 offset:17440\n"
"v_mfma_f32_16x16x32_bf16 v[76:79], v[232:235], a[100:103], v[76:79]\n"
"v_mfma_f32_16x16x32_bf16 v[76:79], v[236:239], a[116:119], v[76:79]\n"
"s_mov_b32 s70, 2684896\n"
"v_mfma_f32_16x16x32_bf16 v[68:71], v[232:235], a[96:99], v[68:71]\n"
"buffer_load_dwordx4 v252, s[40:43], s70 offen offset:2080 lds\n"
"v_mfma_f32_16x16x32_bf16 v[68:71], v[236:239], a[112:115], v[68:71]\n"
"ds_read_b128 a[168:171], v244 offset:0\n"
"s_waitcnt lgkmcnt(1)\n"
"v_mfma_f32_16x16x32_bf16 v[192:195], v[224:227], a[96:99], v[192:195]\n"
"ds_read_b64_tr_b16 v[232:233], v251 offset:16680\n"
"v_mfma_f32_16x16x32_bf16 v[192:195], v[228:231], a[112:115], v[192:195]\n"
"ds_read_b64_tr_b16 v[234:235], v251 offset:16936\n"
"v_mfma_f32_16x16x32_bf16 v[200:203], v[224:227], a[100:103], v[200:203]\n"
"ds_read_b64_tr_b16 v[236:237], v251 offset:17192\n"
"s_mov_b32 s69, 6912\n"
"v_mfma_f32_16x16x32_bf16 v[200:203], v[228:231], a[116:119], v[200:203]\n"
"ds_read_b64_tr_b16 v[238:239], v251 offset:17448\n"
"s_add_u32 m0, s74, 16640\n"
"v_mfma_f32_16x16x32_bf16 v[208:211], v[224:227], a[104:107], v[208:211]\n"
"buffer_load_dwordx4 v253, s[36:39], s69 offen offset:1024 lds\n"
"s_mov_b32 s70, 2814928\n"
"v_mfma_f32_16x16x32_bf16 v[208:211], v[228:231], a[120:123], v[208:211]\n"
"s_add_u32 m0, s68, 0\n"
"v_mfma_f32_16x16x32_bf16 v[216:219], v[224:227], a[108:111], v[216:219]\n"
"buffer_load_dwordx4 v252, s[40:43], s70 offen offset:3120 lds\n"
"v_mfma_f32_16x16x32_bf16 v[216:219], v[228:231], a[124:127], v[216:219]\n"
"ds_read_b128 a[172:175], v244 offset:2048\n"
"s_waitcnt lgkmcnt(1)\n"
"v_mfma_f32_16x16x32_bf16 v[220:223], v[232:235], a[108:111], v[220:223]\n"
"ds_read_b64_tr_b16 v[224:225], v251 offset:16768\n"
"v_mfma_f32_16x16x32_bf16 v[220:223], v[236:239], a[124:127], v[220:223]\n"
"ds_read_b64_tr_b16 v[226:227], v251 offset:17024\n"
"v_mfma_f32_16x16x32_bf16 v[212:215], v[232:235], a[104:107], v[212:215]\n"
"ds_read_b64_tr_b16 v[228:229], v251 offset:17280\n"
"v_mfma_f32_16x16x32_bf16 v[212:215], v[236:239], a[120:123], v[212:215]\n"
"ds_read_b64_tr_b16 v[230:231], v251 offset:17536\n"
"v_mfma_f32_16x16x32_bf16 v[204:207], v[232:235], a[100:103], v[204:207]\n"
"s_mov_b32 s69, 2629632\n"
"v_mfma_f32_16x16x32_bf16 v[204:207], v[236:239], a[116:119], v[204:207]\n"
"s_add_u32 m0, s68, 4160\n"
"v_mfma_f32_16x16x32_bf16 v[196:199], v[232:235], a[96:99], v[196:199]\n"
"buffer_load_dwordx4 v252, s[40:43], s69 offen offset:0 lds\n"
"v_mfma_f32_16x16x32_bf16 v[196:199], v[236:239], a[112:115], v[196:199]\n"
"ds_read_b128 a[176:179], v245 offset:0\n"
"s_waitcnt lgkmcnt(1)\n"
"v_mfma_f32_16x16x32_bf16 v[96:99], v[224:227], a[96:99], v[96:99]\n"
"ds_read_b64_tr_b16 v[232:233], v251 offset:16776\n"
"v_mfma_f32_16x16x32_bf16 v[96:99], v[228:231], a[112:115], v[96:99]\n"
"ds_read_b64_tr_b16 v[234:235], v251 offset:17032\n"
"v_mfma_f32_16x16x32_bf16 v[104:107], v[224:227], a[100:103], v[104:107]\n"
"ds_read_b64_tr_b16 v[236:237], v251 offset:17288\n"
"s_mov_b32 s70, 13056\n"
"v_mfma_f32_16x16x32_bf16 v[104:107], v[228:231], a[116:119], v[104:107]\n"
"ds_read_b64_tr_b16 v[238:239], v251 offset:17544\n"
"s_add_u32 m0, s74, 16640\n"
"v_mfma_f32_16x16x32_bf16 v[112:115], v[224:227], a[104:107], v[112:115]\n"
"buffer_load_dwordx4 v253, s[36:39], s70 offen offset:2048 lds\n"
"s_mov_b32 s69, 2759664\n"
"v_mfma_f32_16x16x32_bf16 v[112:115], v[228:231], a[120:123], v[112:115]\n"
"s_add_u32 m0, s68, 4160\n"
"v_mfma_f32_16x16x32_bf16 v[120:123], v[224:227], a[108:111], v[120:123]\n"
"buffer_load_dwordx4 v252, s[40:43], s69 offen offset:1040 lds\n"
"v_mfma_f32_16x16x32_bf16 v[120:123], v[228:231], a[124:127], v[120:123]\n"
"ds_read_b128 a[180:183], v245 offset:2048\n"
"s_waitcnt lgkmcnt(1)\n"
"v_mfma_f32_16x16x32_bf16 v[124:127], v[232:235], a[108:111], v[124:127]\n"
"ds_read_b64_tr_b16 v[224:225], v251 offset:16800\n"
"v_mfma_f32_16x16x32_bf16 v[124:127], v[236:239], a[124:127], v[124:127]\n"
"ds_read_b64_tr_b16 v[226:227], v251 offset:17056\n"
"v_mfma_f32_16x16x32_bf16 v[116:119], v[232:235], a[104:107], v[116:119]\n"
"ds_read_b64_tr_b16 v[228:229], v251 offset:17312\n"
"v_mfma_f32_16x16x32_bf16 v[116:119], v[236:239], a[120:123], v[116:119]\n"
"ds_read_b64_tr_b16 v[230:231], v251 offset:17568\n"
"v_mfma_f32_16x16x32_bf16 v[108:111], v[232:235], a[100:103], v[108:111]\n"
"v_mfma_f32_16x16x32_bf16 v[108:111], v[236:239], a[116:119], v[108:111]\n"
"ds_read_b128 a[184:187], v246 offset:0\n"
"s_mov_b32 s70, 2693088\n"
"v_mfma_f32_16x16x32_bf16 v[100:103], v[232:235], a[96:99], v[100:103]\n"
"buffer_load_dwordx4 v252, s[40:43], s70 offen offset:2080 lds\n"
"v_mfma_f32_16x16x32_bf16 v[100:103], v[236:239], a[112:115], v[100:103]\n"
"ds_read_b128 a[188:191], v246 offset:2048\n"
"s_waitcnt lgkmcnt(2)\n"
"v_mfma_f32_16x16x32_bf16 v[160:163], v[224:227], a[96:99], v[160:163]\n"
"ds_read_b64_tr_b16 v[232:233], v251 offset:16808\n"
"v_mfma_f32_16x16x32_bf16 v[160:163], v[228:231], a[112:115], v[160:163]\n"
"ds_read_b64_tr_b16 v[234:235], v251 offset:17064\n"
"v_mfma_f32_16x16x32_bf16 v[168:171], v[224:227], a[100:103], v[168:171]\n"
"ds_read_b64_tr_b16 v[236:237], v251 offset:17320\n"
"s_mov_b32 s69, 19200\n"
"v_mfma_f32_16x16x32_bf16 v[168:171], v[228:231], a[116:119], v[168:171]\n"
"ds_read_b64_tr_b16 v[238:239], v251 offset:17576\n"
"s_add_u32 m0, s74, 16640\n"
"v_mfma_f32_16x16x32_bf16 v[176:179], v[224:227], a[104:107], v[176:179]\n"
"buffer_load_dwordx4 v253, s[36:39], s69 offen offset:3072 lds\n"
"s_mov_b32 s70, 2823120\n"
"v_mfma_f32_16x16x32_bf16 v[176:179], v[228:231], a[120:123], v[176:179]\n"
"s_add_u32 m0, s68, 4160\n"
"v_mfma_f32_16x16x32_bf16 v[184:187], v[224:227], a[108:111], v[184:187]\n"
"buffer_load_dwordx4 v252, s[40:43], s70 offen offset:3120 lds\n"
"v_mfma_f32_16x16x32_bf16 v[184:187], v[228:231], a[124:127], v[184:187]\n"
"s_waitcnt vmcnt(12) lgkmcnt(0)\n"
"s_barrier\n"
"v_mfma_f32_16x16x32_bf16 v[188:191], v[232:235], a[108:111], v[188:191]\n"
"ds_read_b64_tr_b16 v[224:225], v251 offset:33280\n"
"v_mfma_f32_16x16x32_bf16 v[188:191], v[236:239], a[124:127], v[188:191]\n"
"ds_read_b64_tr_b16 v[226:227], v251 offset:33536\n"
"v_mfma_f32_16x16x32_bf16 v[180:183], v[232:235], a[104:107], v[180:183]\n"
"ds_read_b64_tr_b16 v[228:229], v251 offset:33792\n"
"v_mfma_f32_16x16x32_bf16 v[180:183], v[236:239], a[120:123], v[180:183]\n"
"ds_read_b64_tr_b16 v[230:231], v251 offset:34048\n"
"v_mfma_f32_16x16x32_bf16 v[172:175], v[232:235], a[100:103], v[172:175]\n"
"s_mov_b32 s69, 3145728\n"
"v_mfma_f32_16x16x32_bf16 v[172:175], v[236:239], a[116:119], v[172:175]\n"
"s_add_u32 m0, s68, 16640\n"
"v_mfma_f32_16x16x32_bf16 v[164:167], v[232:235], a[96:99], v[164:167]\n"
"buffer_load_dwordx4 v252, s[40:43], s69 offen offset:0 lds\n"
"v_mfma_f32_16x16x32_bf16 v[164:167], v[236:239], a[112:115], v[164:167]\n"
"s_waitcnt lgkmcnt(0)\n"
"v_mfma_f32_16x16x32_bf16 v[0:3], v[224:227], a[128:131], v[0:3]\n"
"ds_read_b64_tr_b16 v[232:233], v251 offset:33288\n"
"v_add_u32_e32 v253, 16, v242\n"
"v_mfma_f32_16x16x32_bf16 v[0:3], v[228:231], a[144:147], v[0:3]\n"
"ds_read_b64_tr_b16 v[234:235], v251 offset:33544\n"
"v_lshrrev_b32_e32 v255, 7, v242\n"
"v_mfma_f32_16x16x32_bf16 v[8:11], v[224:227], a[132:135], v[8:11]\n"
"ds_read_b64_tr_b16 v[236:237], v251 offset:33800\n"
"v_mul_u32_u24_e32 v255, 14336, v255\n"
"v_mfma_f32_16x16x32_bf16 v[8:11], v[228:231], a[148:151], v[8:11]\n"
"ds_read_b64_tr_b16 v[238:239], v251 offset:34056\n"
"v_and_b32_e32 v247, 112, v242\n"
"v_mfma_f32_16x16x32_bf16 v[16:19], v[224:227], a[136:139], v[16:19]\n"
"v_add_u32_e32 v255, v247, v255\n"
"v_mfma_f32_16x16x32_bf16 v[16:19], v[228:231], a[152:155], v[16:19]\n"
"s_mov_b32 s70, 3275760\n"
"v_mfma_f32_16x16x32_bf16 v[24:27], v[224:227], a[140:143], v[24:27]\n"
"buffer_load_dwordx4 v252, s[40:43], s70 offen offset:1040 lds\n"
"v_mfma_f32_16x16x32_bf16 v[24:27], v[228:231], a[156:159], v[24:27]\n"
"s_waitcnt lgkmcnt(0)\n"
"v_mfma_f32_16x16x32_bf16 v[28:31], v[232:235], a[140:143], v[28:31]\n"
"ds_read_b64_tr_b16 v[224:225], v251 offset:33312\n"
"v_mfma_f32_16x16x32_bf16 v[28:31], v[236:239], a[156:159], v[28:31]\n"
"ds_read_b64_tr_b16 v[226:227], v251 offset:33568\n"
"v_mfma_f32_16x16x32_bf16 v[20:23], v[232:235], a[136:139], v[20:23]\n"
"ds_read_b64_tr_b16 v[228:229], v251 offset:33824\n"
"v_mfma_f32_16x16x32_bf16 v[20:23], v[236:239], a[152:155], v[20:23]\n"
"ds_read_b64_tr_b16 v[230:231], v251 offset:34080\n"
"v_mfma_f32_16x16x32_bf16 v[12:15], v[232:235], a[132:135], v[12:15]\n"
"v_mfma_f32_16x16x32_bf16 v[12:15], v[236:239], a[148:151], v[12:15]\n"
"s_mov_b32 s69, 3209184\n"
"v_mfma_f32_16x16x32_bf16 v[4:7], v[232:235], a[128:131], v[4:7]\n"
"buffer_load_dwordx4 v252, s[40:43], s69 offen offset:2080 lds\n"
"v_mfma_f32_16x16x32_bf16 v[4:7], v[236:239], a[144:147], v[4:7]\n"
"s_waitcnt lgkmcnt(0)\n"
"v_mfma_f32_16x16x32_bf16 v[128:131], v[224:227], a[128:131], v[128:131]\n"
"ds_read_b64_tr_b16 v[232:233], v251 offset:33320\n"
"v_mfma_f32_16x16x32_bf16 v[128:131], v[228:231], a[144:147], v[128:131]\n"
"ds_read_b64_tr_b16 v[234:235], v251 offset:33576\n"
"v_mfma_f32_16x16x32_bf16 v[136:139], v[224:227], a[132:135], v[136:139]\n"
"ds_read_b64_tr_b16 v[236:237], v251 offset:33832\n"
"v_mfma_f32_16x16x32_bf16 v[136:139], v[228:231], a[148:151], v[136:139]\n"
"ds_read_b64_tr_b16 v[238:239], v251 offset:34088\n"
"v_mfma_f32_16x16x32_bf16 v[144:147], v[224:227], a[136:139], v[144:147]\n"
"v_mfma_f32_16x16x32_bf16 v[144:147], v[228:231], a[152:155], v[144:147]\n"
"s_mov_b32 s70, 3339216\n"
"v_mfma_f32_16x16x32_bf16 v[152:155], v[224:227], a[140:143], v[152:155]\n"
"buffer_load_dwordx4 v252, s[40:43], s70 offen offset:3120 lds\n"
"v_mfma_f32_16x16x32_bf16 v[152:155], v[228:231], a[156:159], v[152:155]\n"
"s_waitcnt lgkmcnt(0)\n"
"v_mfma_f32_16x16x32_bf16 v[156:159], v[232:235], a[140:143], v[156:159]\n"
"ds_read_b64_tr_b16 v[224:225], v251 offset:33408\n"
"v_mfma_f32_16x16x32_bf16 v[156:159], v[236:239], a[156:159], v[156:159]\n"
"ds_read_b64_tr_b16 v[226:227], v251 offset:33664\n"
"v_mfma_f32_16x16x32_bf16 v[148:151], v[232:235], a[136:139], v[148:151]\n"
"ds_read_b64_tr_b16 v[228:229], v251 offset:33920\n"
"v_mfma_f32_16x16x32_bf16 v[148:151], v[236:239], a[152:155], v[148:151]\n"
"ds_read_b64_tr_b16 v[230:231], v251 offset:34176\n"
"v_mfma_f32_16x16x32_bf16 v[140:143], v[232:235], a[132:135], v[140:143]\n"
"s_mov_b32 s69, 3153920\n"
"v_mfma_f32_16x16x32_bf16 v[140:143], v[236:239], a[148:151], v[140:143]\n"
"s_add_u32 m0, s68, 20800\n"
"v_mfma_f32_16x16x32_bf16 v[132:135], v[232:235], a[128:131], v[132:135]\n"
"buffer_load_dwordx4 v252, s[40:43], s69 offen offset:0 lds\n"
"v_mfma_f32_16x16x32_bf16 v[132:135], v[236:239], a[144:147], v[132:135]\n"
"s_waitcnt lgkmcnt(0)\n"
"v_mfma_f32_16x16x32_bf16 v[32:35], v[224:227], a[128:131], v[32:35]\n"
"ds_read_b64_tr_b16 v[232:233], v251 offset:33416\n"
"v_mfma_f32_16x16x32_bf16 v[32:35], v[228:231], a[144:147], v[32:35]\n"
"ds_read_b64_tr_b16 v[234:235], v251 offset:33672\n"
"v_mfma_f32_16x16x32_bf16 v[40:43], v[224:227], a[132:135], v[40:43]\n"
"ds_read_b64_tr_b16 v[236:237], v251 offset:33928\n"
"v_mfma_f32_16x16x32_bf16 v[40:43], v[228:231], a[148:151], v[40:43]\n"
"ds_read_b64_tr_b16 v[238:239], v251 offset:34184\n"
"v_mfma_f32_16x16x32_bf16 v[48:51], v[224:227], a[136:139], v[48:51]\n"
"v_mfma_f32_16x16x32_bf16 v[48:51], v[228:231], a[152:155], v[48:51]\n"
"s_mov_b32 s70, 3283952\n"
"v_mfma_f32_16x16x32_bf16 v[56:59], v[224:227], a[140:143], v[56:59]\n"
"buffer_load_dwordx4 v252, s[40:43], s70 offen offset:1040 lds\n"
"v_mfma_f32_16x16x32_bf16 v[56:59], v[228:231], a[156:159], v[56:59]\n"
"s_waitcnt lgkmcnt(0)\n"
"v_mfma_f32_16x16x32_bf16 v[60:63], v[232:235], a[140:143], v[60:63]\n"
"ds_read_b64_tr_b16 v[224:225], v251 offset:33440\n"
"v_mfma_f32_16x16x32_bf16 v[60:63], v[236:239], a[156:159], v[60:63]\n"
"ds_read_b64_tr_b16 v[226:227], v251 offset:33696\n"
"v_mfma_f32_16x16x32_bf16 v[52:55], v[232:235], a[136:139], v[52:55]\n"
"ds_read_b64_tr_b16 v[228:229], v251 offset:33952\n"
"v_mfma_f32_16x16x32_bf16 v[52:55], v[236:239], a[152:155], v[52:55]\n"
"ds_read_b64_tr_b16 v[230:231], v251 offset:34208\n"
"v_mfma_f32_16x16x32_bf16 v[44:47], v[232:235], a[132:135], v[44:47]\n"
"v_mfma_f32_16x16x32_bf16 v[44:47], v[236:239], a[148:151], v[44:47]\n"
"s_mov_b32 s69, 3217376\n"
"v_mfma_f32_16x16x32_bf16 v[36:39], v[232:235], a[128:131], v[36:39]\n"
"buffer_load_dwordx4 v252, s[40:43], s69 offen offset:2080 lds\n"
"v_mfma_f32_16x16x32_bf16 v[36:39], v[236:239], a[144:147], v[36:39]\n"
"s_waitcnt lgkmcnt(0)\n"
"v_mfma_f32_16x16x32_bf16 a[224:227], v[224:227], a[128:131], a[224:227]\n"
"ds_read_b64_tr_b16 v[232:233], v251 offset:33448\n"
"v_mfma_f32_16x16x32_bf16 a[224:227], v[228:231], a[144:147], a[224:227]\n"
"ds_read_b64_tr_b16 v[234:235], v251 offset:33704\n"
"v_mfma_f32_16x16x32_bf16 a[232:235], v[224:227], a[132:135], a[232:235]\n"
"ds_read_b64_tr_b16 v[236:237], v251 offset:33960\n"
"v_mfma_f32_16x16x32_bf16 a[232:235], v[228:231], a[148:151], a[232:235]\n"
"ds_read_b64_tr_b16 v[238:239], v251 offset:34216\n"
"v_mfma_f32_16x16x32_bf16 a[240:243], v[224:227], a[136:139], a[240:243]\n"
"v_mfma_f32_16x16x32_bf16 a[240:243], v[228:231], a[152:155], a[240:243]\n"
"s_mov_b32 s70, 3347408\n"
"v_mfma_f32_16x16x32_bf16 a[248:251], v[224:227], a[140:143], a[248:251]\n"
"buffer_load_dwordx4 v252, s[40:43], s70 offen offset:3120 lds\n"
"v_mfma_f32_16x16x32_bf16 a[248:251], v[228:231], a[156:159], a[248:251]\n"
"s_waitcnt vmcnt(8) lgkmcnt(0)\n"
"s_barrier\n"
"v_mfma_f32_16x16x32_bf16 a[252:255], v[232:235], a[140:143], a[252:255]\n"
"ds_read_b64_tr_b16 v[224:225], v251 offset:0\n"
"v_mfma_f32_16x16x32_bf16 a[252:255], v[236:239], a[156:159], a[252:255]\n"
"ds_read_b64_tr_b16 v[226:227], v251 offset:256\n"
"v_mfma_f32_16x16x32_bf16 a[244:247], v[232:235], a[136:139], a[244:247]\n"
"ds_read_b64_tr_b16 v[228:229], v251 offset:512\n"
"v_mfma_f32_16x16x32_bf16 a[244:247], v[236:239], a[152:155], a[244:247]\n"
"ds_read_b64_tr_b16 v[230:231], v251 offset:768\n"
"v_mfma_f32_16x16x32_bf16 a[236:239], v[232:235], a[132:135], a[236:239]\n"
"s_mov_b32 s69, 2097408\n"
"v_mfma_f32_16x16x32_bf16 a[236:239], v[236:239], a[148:151], a[236:239]\n"
"s_add_u32 m0, s68, 49920\n"
"v_mfma_f32_16x16x32_bf16 a[228:231], v[232:235], a[128:131], a[228:231]\n"
"buffer_load_dwordx4 v252, s[40:43], s69 offen offset:0 lds\n"
"v_mfma_f32_16x16x32_bf16 a[228:231], v[236:239], a[144:147], a[228:231]\n"
"s_waitcnt lgkmcnt(0)\n"
"v_mfma_f32_16x16x32_bf16 v[0:3], v[224:227], a[160:163], v[0:3]\n"
"ds_read_b64_tr_b16 v[232:233], v251 offset:8\n"
"v_mfma_f32_16x16x32_bf16 v[0:3], v[228:231], a[176:179], v[0:3]\n"
"ds_read_b64_tr_b16 v[234:235], v251 offset:264\n"
"v_mfma_f32_16x16x32_bf16 v[8:11], v[224:227], a[164:167], v[8:11]\n"
"ds_read_b64_tr_b16 v[236:237], v251 offset:520\n"
"v_mfma_f32_16x16x32_bf16 v[8:11], v[228:231], a[180:183], v[8:11]\n"
"ds_read_b64_tr_b16 v[238:239], v251 offset:776\n"
"v_mfma_f32_16x16x32_bf16 v[16:19], v[224:227], a[168:171], v[16:19]\n"
"v_mfma_f32_16x16x32_bf16 v[16:19], v[228:231], a[184:187], v[16:19]\n"
"ds_read_b128 a[192:195], v243 offset:16640\n"
"s_mov_b32 s70, 2227440\n"
"v_mfma_f32_16x16x32_bf16 v[24:27], v[224:227], a[172:175], v[24:27]\n"
"buffer_load_dwordx4 v252, s[40:43], s70 offen offset:1040 lds\n"
"v_mfma_f32_16x16x32_bf16 v[24:27], v[228:231], a[188:191], v[24:27]\n"
"ds_read_b128 a[196:199], v243 offset:18688\n"
"s_waitcnt lgkmcnt(2)\n"
"v_mfma_f32_16x16x32_bf16 v[28:31], v[232:235], a[172:175], v[28:31]\n"
"ds_read_b64_tr_b16 v[224:225], v251 offset:32\n"
"v_mfma_f32_16x16x32_bf16 v[28:31], v[236:239], a[188:191], v[28:31]\n"
"ds_read_b64_tr_b16 v[226:227], v251 offset:288\n"
"v_mfma_f32_16x16x32_bf16 v[20:23], v[232:235], a[168:171], v[20:23]\n"
"ds_read_b64_tr_b16 v[228:229], v251 offset:544\n"
"v_mfma_f32_16x16x32_bf16 v[20:23], v[236:239], a[184:187], v[20:23]\n"
"ds_read_b64_tr_b16 v[230:231], v251 offset:800\n"
"v_mfma_f32_16x16x32_bf16 v[12:15], v[232:235], a[164:167], v[12:15]\n"
"v_mfma_f32_16x16x32_bf16 v[12:15], v[236:239], a[180:183], v[12:15]\n"
"s_mov_b32 s69, 2160864\n"
"v_mfma_f32_16x16x32_bf16 v[4:7], v[232:235], a[160:163], v[4:7]\n"
"buffer_load_dwordx4 v252, s[40:43], s69 offen offset:2080 lds\n"
"v_mfma_f32_16x16x32_bf16 v[4:7], v[236:239], a[176:179], v[4:7]\n"
"ds_read_b128 a[200:203], v244 offset:16640\n"
"s_waitcnt lgkmcnt(1)\n"
"v_mfma_f32_16x16x32_bf16 v[128:131], v[224:227], a[160:163], v[128:131]\n"
"ds_read_b64_tr_b16 v[232:233], v251 offset:40\n"
"v_mfma_f32_16x16x32_bf16 v[128:131], v[228:231], a[176:179], v[128:131]\n"
"ds_read_b64_tr_b16 v[234:235], v251 offset:296\n"
"v_mfma_f32_16x16x32_bf16 v[136:139], v[224:227], a[164:167], v[136:139]\n"
"ds_read_b64_tr_b16 v[236:237], v251 offset:552\n"
"v_mfma_f32_16x16x32_bf16 v[136:139], v[228:231], a[180:183], v[136:139]\n"
"ds_read_b64_tr_b16 v[238:239], v251 offset:808\n"
"v_mfma_f32_16x16x32_bf16 v[144:147], v[224:227], a[168:171], v[144:147]\n"
"v_mfma_f32_16x16x32_bf16 v[144:147], v[228:231], a[184:187], v[144:147]\n"
"s_mov_b32 s70, 2290896\n"
"v_mfma_f32_16x16x32_bf16 v[152:155], v[224:227], a[172:175], v[152:155]\n"
"buffer_load_dwordx4 v252, s[40:43], s70 offen offset:3120 lds\n"
"v_mfma_f32_16x16x32_bf16 v[152:155], v[228:231], a[188:191], v[152:155]\n"
"ds_read_b128 a[204:207], v244 offset:18688\n"
"s_waitcnt lgkmcnt(1)\n"
"v_mfma_f32_16x16x32_bf16 v[156:159], v[232:235], a[172:175], v[156:159]\n"
"ds_read_b64_tr_b16 v[224:225], v251 offset:128\n"
"v_mfma_f32_16x16x32_bf16 v[156:159], v[236:239], a[188:191], v[156:159]\n"
"ds_read_b64_tr_b16 v[226:227], v251 offset:384\n"
"v_mfma_f32_16x16x32_bf16 v[148:151], v[232:235], a[168:171], v[148:151]\n"
"ds_read_b64_tr_b16 v[228:229], v251 offset:640\n"
"v_mfma_f32_16x16x32_bf16 v[148:151], v[236:239], a[184:187], v[148:151]\n"
"ds_read_b64_tr_b16 v[230:231], v251 offset:896\n"
"v_mfma_f32_16x16x32_bf16 v[140:143], v[232:235], a[164:167], v[140:143]\n"
"s_mov_b32 s69, 2105600\n"
"v_mfma_f32_16x16x32_bf16 v[140:143], v[236:239], a[180:183], v[140:143]\n"
"s_add_u32 m0, s68, 54080\n"
"v_mfma_f32_16x16x32_bf16 v[132:135], v[232:235], a[160:163], v[132:135]\n"
"buffer_load_dwordx4 v252, s[40:43], s69 offen offset:0 lds\n"
"v_mfma_f32_16x16x32_bf16 v[132:135], v[236:239], a[176:179], v[132:135]\n"
"ds_read_b128 a[208:211], v245 offset:16640\n"
"s_waitcnt lgkmcnt(1)\n"
"v_mfma_f32_16x16x32_bf16 v[32:35], v[224:227], a[160:163], v[32:35]\n"
"ds_read_b64_tr_b16 v[232:233], v251 offset:136\n"
"v_mfma_f32_16x16x32_bf16 v[32:35], v[228:231], a[176:179], v[32:35]\n"
"ds_read_b64_tr_b16 v[234:235], v251 offset:392\n"
"v_mfma_f32_16x16x32_bf16 v[40:43], v[224:227], a[164:167], v[40:43]\n"
"ds_read_b64_tr_b16 v[236:237], v251 offset:648\n"
"v_mfma_f32_16x16x32_bf16 v[40:43], v[228:231], a[180:183], v[40:43]\n"
"ds_read_b64_tr_b16 v[238:239], v251 offset:904\n"
"v_mfma_f32_16x16x32_bf16 v[48:51], v[224:227], a[168:171], v[48:51]\n"
"v_mfma_f32_16x16x32_bf16 v[48:51], v[228:231], a[184:187], v[48:51]\n"
"s_mov_b32 s70, 2235632\n"
"v_mfma_f32_16x16x32_bf16 v[56:59], v[224:227], a[172:175], v[56:59]\n"
"buffer_load_dwordx4 v252, s[40:43], s70 offen offset:1040 lds\n"
"v_mfma_f32_16x16x32_bf16 v[56:59], v[228:231], a[188:191], v[56:59]\n"
"ds_read_b128 a[212:215], v245 offset:18688\n"
"s_waitcnt lgkmcnt(1)\n"
"v_mfma_f32_16x16x32_bf16 v[60:63], v[232:235], a[172:175], v[60:63]\n"
"ds_read_b64_tr_b16 v[224:225], v251 offset:160\n"
"v_mfma_f32_16x16x32_bf16 v[60:63], v[236:239], a[188:191], v[60:63]\n"
"ds_read_b64_tr_b16 v[226:227], v251 offset:416\n"
"v_mfma_f32_16x16x32_bf16 v[52:55], v[232:235], a[168:171], v[52:55]\n"
"ds_read_b64_tr_b16 v[228:229], v251 offset:672\n"
"v_mfma_f32_16x16x32_bf16 v[52:55], v[236:239], a[184:187], v[52:55]\n"
"ds_read_b64_tr_b16 v[230:231], v251 offset:928\n"
"v_mfma_f32_16x16x32_bf16 v[44:47], v[232:235], a[164:167], v[44:47]\n"
"v_mfma_f32_16x16x32_bf16 v[44:47], v[236:239], a[180:183], v[44:47]\n"
"ds_read_b128 a[216:219], v246 offset:16640\n"
"s_mov_b32 s69, 2169056\n"
"v_mfma_f32_16x16x32_bf16 v[36:39], v[232:235], a[160:163], v[36:39]\n"
"buffer_load_dwordx4 v252, s[40:43], s69 offen offset:2080 lds\n"
"v_mfma_f32_16x16x32_bf16 v[36:39], v[236:239], a[176:179], v[36:39]\n"
"ds_read_b128 a[220:223], v246 offset:18688\n"
"s_waitcnt lgkmcnt(2)\n"
"v_mfma_f32_16x16x32_bf16 a[224:227], v[224:227], a[160:163], a[224:227]\n"
"ds_read_b64_tr_b16 v[232:233], v251 offset:168\n"
"v_mfma_f32_16x16x32_bf16 a[224:227], v[228:231], a[176:179], a[224:227]\n"
"ds_read_b64_tr_b16 v[234:235], v251 offset:424\n"
"v_mfma_f32_16x16x32_bf16 a[232:235], v[224:227], a[164:167], a[232:235]\n"
"ds_read_b64_tr_b16 v[236:237], v251 offset:680\n"
"v_mfma_f32_16x16x32_bf16 a[232:235], v[228:231], a[180:183], a[232:235]\n"
"ds_read_b64_tr_b16 v[238:239], v251 offset:936\n"
"v_mfma_f32_16x16x32_bf16 a[240:243], v[224:227], a[168:171], a[240:243]\n"
"v_mfma_f32_16x16x32_bf16 a[240:243], v[228:231], a[184:187], a[240:243]\n"
"s_mov_b32 s70, 2299088\n"
"v_mfma_f32_16x16x32_bf16 a[248:251], v[224:227], a[172:175], a[248:251]\n"
"buffer_load_dwordx4 v252, s[40:43], s70 offen offset:3120 lds\n"
"v_mfma_f32_16x16x32_bf16 a[248:251], v[228:231], a[188:191], a[248:251]\n"
"s_waitcnt vmcnt(8) lgkmcnt(0)\n"
"s_barrier\n"
"v_mfma_f32_16x16x32_bf16 a[252:255], v[232:235], a[172:175], a[252:255]\n"
"ds_read_b64_tr_b16 v[224:225], v251 offset:16640\n"
"v_mfma_f32_16x16x32_bf16 a[252:255], v[236:239], a[188:191], a[252:255]\n"
"ds_read_b64_tr_b16 v[226:227], v251 offset:16896\n"
"v_mfma_f32_16x16x32_bf16 a[244:247], v[232:235], a[168:171], a[244:247]\n"
"ds_read_b64_tr_b16 v[228:229], v251 offset:17152\n"
"v_mfma_f32_16x16x32_bf16 a[244:247], v[236:239], a[184:187], a[244:247]\n"
"ds_read_b64_tr_b16 v[230:231], v251 offset:17408\n"
"v_mfma_f32_16x16x32_bf16 a[236:239], v[232:235], a[164:167], a[236:239]\n"
"s_mov_b32 s69, 2621696\n"
"v_mfma_f32_16x16x32_bf16 a[236:239], v[236:239], a[180:183], a[236:239]\n"
"s_add_u32 m0, s68, 0\n"
"v_mfma_f32_16x16x32_bf16 a[228:231], v[232:235], a[160:163], a[228:231]\n"
"buffer_load_dwordx4 v252, s[40:43], s69 offen offset:0 lds\n"
"v_mfma_f32_16x16x32_bf16 a[228:231], v[236:239], a[176:179], a[228:231]\n"
"s_waitcnt lgkmcnt(0)\n"
"v_mfma_f32_16x16x32_bf16 v[0:3], v[224:227], a[192:195], v[0:3]\n"
"ds_read_b64_tr_b16 v[232:233], v251 offset:16648\n"
"v_mfma_f32_16x16x32_bf16 v[0:3], v[228:231], a[208:211], v[0:3]\n"
"ds_read_b64_tr_b16 v[234:235], v251 offset:16904\n"
"v_mfma_f32_16x16x32_bf16 v[8:11], v[224:227], a[196:199], v[8:11]\n"
"ds_read_b64_tr_b16 v[236:237], v251 offset:17160\n"
"s_add_u32 s72, s72, 16416\n"
"v_mfma_f32_16x16x32_bf16 v[8:11], v[228:231], a[212:215], v[8:11]\n"
"ds_read_b64_tr_b16 v[238:239], v251 offset:17416\n"
"s_add_u32 m0, s68, 33280\n"
"v_mfma_f32_16x16x32_bf16 v[16:19], v[224:227], a[200:203], v[16:19]\n"
"s_branch .Lpo3\n"
".Lpg3:\n"
"s_setpc_b64 s[78:79]\n"
".Lpo3:\n"
"buffer_load_dwordx4 v253, s[44:47], s72 offen offset:0 nt lds\n"
"s_mov_b32 s70, 2751728\n"
"v_mfma_f32_16x16x32_bf16 v[16:19], v[228:231], a[216:219], v[16:19]\n"
"s_add_u32 m0, s68, 0\n"
"v_mfma_f32_16x16x32_bf16 v[24:27], v[224:227], a[204:207], v[24:27]\n"
"buffer_load_dwordx4 v252, s[40:43], s70 offen offset:1040 lds\n"
"v_mfma_f32_16x16x32_bf16 v[24:27], v[228:231], a[220:223], v[24:27]\n"
"s_waitcnt lgkmcnt(0)\n"
"v_mfma_f32_16x16x32_bf16 v[28:31], v[232:235], a[204:207], v[28:31]\n"
"ds_read_b64_tr_b16 v[224:225], v251 offset:16672\n"
"v_mfma_f32_16x16x32_bf16 v[28:31], v[236:239], a[220:223], v[28:31]\n"
"ds_read_b64_tr_b16 v[226:227], v251 offset:16928\n"
"v_mfma_f32_16x16x32_bf16 v[20:23], v[232:235], a[200:203], v[20:23]\n"
"ds_read_b64_tr_b16 v[228:229], v251 offset:17184\n"
"v_mfma_f32_16x16x32_bf16 v[20:23], v[236:239], a[216:219], v[20:23]\n"
"ds_read_b64_tr_b16 v[230:231], v251 offset:17440\n"
"s_add_u32 m0, s68, 33280\n"
"v_mfma_f32_16x16x32_bf16 v[12:15], v[232:235], a[196:199], v[12:15]\n"
"buffer_load_dwordx4 v242, s[44:47], s72 offen offset:1040 nt lds\n"
"s_mov_b32 s69, 2685152\n"
"v_mfma_f32_16x16x32_bf16 v[12:15], v[236:239], a[212:215], v[12:15]\n"
"s_add_u32 m0, s68, 0\n"
"v_mfma_f32_16x16x32_bf16 v[4:7], v[232:235], a[192:195], v[4:7]\n"
"buffer_load_dwordx4 v252, s[40:43], s69 offen offset:2080 lds\n"
"v_mfma_f32_16x16x32_bf16 v[4:7], v[236:239], a[208:211], v[4:7]\n"
"s_waitcnt lgkmcnt(0)\n"
"v_mfma_f32_16x16x32_bf16 v[128:131], v[224:227], a[192:195], v[128:131]\n"
"ds_read_b64_tr_b16 v[232:233], v251 offset:16680\n"
"v_mfma_f32_16x16x32_bf16 v[128:131], v[228:231], a[208:211], v[128:131]\n"
"ds_read_b64_tr_b16 v[234:235], v251 offset:16936\n"
"v_mfma_f32_16x16x32_bf16 v[136:139], v[224:227], a[196:199], v[136:139]\n"
"ds_read_b64_tr_b16 v[236:237], v251 offset:17192\n"
"s_sub_u32 s72, s72, 32\n"
"v_mfma_f32_16x16x32_bf16 v[136:139], v[228:231], a[212:215], v[136:139]\n"
"ds_read_b64_tr_b16 v[238:239], v251 offset:17448\n"
"s_add_u32 m0, s68, 33280\n"
"v_mfma_f32_16x16x32_bf16 v[144:147], v[224:227], a[200:203], v[144:147]\n"
"buffer_load_dwordx4 v253, s[44:47], s72 offen offset:2080 nt lds\n"
"s_mov_b32 s70, 2815184\n"
"v_mfma_f32_16x16x32_bf16 v[144:147], v[228:231], a[216:219], v[144:147]\n"
"s_add_u32 m0, s68, 0\n"
"v_mul_f32_e32 v0, s71, v0\n"
"v_mfma_f32_16x16x32_bf16 v[152:155], v[224:227], a[204:207], v[152:155]\n"
"buffer_load_dwordx4 v252, s[40:43], s70 offen offset:3120 lds\n"
"v_mul_f32_e32 v1, s71, v1\n"
"v_mfma_f32_16x16x32_bf16 v[152:155], v[228:231], a[220:223], v[152:155]\n"
"s_waitcnt lgkmcnt(0)\n"
"v_cvt_pk_bf16_f32 v0, v0, v1\n"
"v_mfma_f32_16x16x32_bf16 v[156:159], v[232:235], a[204:207], v[156:159]\n"
"ds_read_b64_tr_b16 v[224:225], v251 offset:16768\n"
"v_mul_f32_e32 v2, s71, v2\n"
"v_mfma_f32_16x16x32_bf16 v[156:159], v[236:239], a[220:223], v[156:159]\n"
"ds_read_b64_tr_b16 v[226:227], v251 offset:17024\n"
"v_mul_f32_e32 v3, s71, v3\n"
"v_mfma_f32_16x16x32_bf16 v[148:151], v[232:235], a[200:203], v[148:151]\n"
"ds_read_b64_tr_b16 v[228:229], v251 offset:17280\n"
"v_cvt_pk_bf16_f32 v1, v2, v3\n"
"v_mfma_f32_16x16x32_bf16 v[148:151], v[236:239], a[216:219], v[148:151]\n"
"ds_read_b64_tr_b16 v[230:231], v251 offset:17536\n"
"s_add_u32 m0, s68, 33280\n"
"v_mfma_f32_16x16x32_bf16 v[140:143], v[232:235], a[196:199], v[140:143]\n"
"buffer_load_dwordx4 v242, s[44:47], s72 offen offset:3120 nt lds\n"
"s_mov_b32 s69, 2629888\n"
"v_mfma_f32_16x16x32_bf16 v[140:143], v[236:239], a[212:215], v[140:143]\n"
"s_add_u32 m0, s68, 4160\n"
"v_mul_f32_e32 v4, s71, v4\n"
"v_mfma_f32_16x16x32_bf16 v[132:135], v[232:235], a[192:195], v[132:135]\n"
"buffer_load_dwordx4 v252, s[40:43], s69 offen offset:0 lds\n"
"v_mul_f32_e32 v5, s71, v5\n"
"v_mfma_f32_16x16x32_bf16 v[132:135], v[236:239], a[208:211], v[132:135]\n"
"s_waitcnt lgkmcnt(0)\n"
"v_cvt_pk_bf16_f32 v2, v4, v5\n"
"v_mfma_f32_16x16x32_bf16 v[32:35], v[224:227], a[192:195], v[32:35]\n"
"ds_read_b64_tr_b16 v[232:233], v251 offset:16776\n"
"v_mul_f32_e32 v6, s71, v6\n"
"v_mfma_f32_16x16x32_bf16 v[32:35], v[228:231], a[208:211], v[32:35]\n"
"ds_read_b64_tr_b16 v[234:235], v251 offset:17032\n"
"v_mul_f32_e32 v7, s71, v7\n"
"v_mfma_f32_16x16x32_bf16 v[40:43], v[224:227], a[196:199], v[40:43]\n"
"ds_read_b64_tr_b16 v[236:237], v251 offset:17288\n"
"s_add_u32 s72, s72, 32\n"
"v_mfma_f32_16x16x32_bf16 v[40:43], v[228:231], a[212:215], v[40:43]\n"
"ds_read_b64_tr_b16 v[238:239], v251 offset:17544\n"
"s_add_u32 m0, s68, 37440\n"
"v_mfma_f32_16x16x32_bf16 v[48:51], v[224:227], a[200:203], v[48:51]\n"
"buffer_load_dwordx4 v253, s[48:51], s72 offen offset:0 nt lds\n"
"s_mov_b32 s70, 2759920\n"
"v_mfma_f32_16x16x32_bf16 v[48:51], v[228:231], a[216:219], v[48:51]\n"
"s_add_u32 m0, s68, 4160\n"
"v_cvt_pk_bf16_f32 v3, v6, v7\n"
"v_mfma_f32_16x16x32_bf16 v[56:59], v[224:227], a[204:207], v[56:59]\n"
"buffer_load_dwordx4 v252, s[40:43], s70 offen offset:1040 lds\n"
"v_mul_f32_e32 v8, s71, v8\n"
"v_mfma_f32_16x16x32_bf16 v[56:59], v[228:231], a[220:223], v[56:59]\n"
"s_waitcnt lgkmcnt(0)\n"
"v_mul_f32_e32 v9, s71, v9\n"
"v_mfma_f32_16x16x32_bf16 v[60:63], v[232:235], a[204:207], v[60:63]\n"
"ds_read_b64_tr_b16 v[224:225], v251 offset:16800\n"
"v_cvt_pk_bf16_f32 v4, v8, v9\n"
"v_mfma_f32_16x16x32_bf16 v[60:63], v[236:239], a[220:223], v[60:63]\n"
"ds_read_b64_tr_b16 v[226:227], v251 offset:17056\n"
"v_mul_f32_e32 v10, s71, v10\n"
"v_mfma_f32_16x16x32_bf16 v[52:55], v[232:235], a[200:203], v[52:55]\n"
"ds_read_b64_tr_b16 v[228:229], v251 offset:17312\n"
"v_mul_f32_e32 v11, s71, v11\n"
"v_mfma_f32_16x16x32_bf16 v[52:55], v[236:239], a[216:219], v[52:55]\n"
"ds_read_b64_tr_b16 v[230:231], v251 offset:17568\n"
"s_add_u32 m0, s68, 37440\n"
"v_mfma_f32_16x16x32_bf16 v[44:47], v[232:235], a[196:199], v[44:47]\n"
"buffer_load_dwordx4 v242, s[48:51], s72 offen offset:1040 nt lds\n"
"s_mov_b32 s69, 2693344\n"
"v_mfma_f32_16x16x32_bf16 v[44:47], v[236:239], a[212:215], v[44:47]\n"
"s_add_u32 m0, s68, 4160\n"
"v_cvt_pk_bf16_f32 v5, v10, v11\n"
"v_mfma_f32_16x16x32_bf16 v[36:39], v[232:235], a[192:195], v[36:39]\n"
"buffer_load_dwordx4 v252, s[40:43], s69 offen offset:2080 lds\n"
"v_mul_f32_e32 v12, s71, v12\n"
"v_mfma_f32_16x16x32_bf16 v[36:39], v[236:239], a[208:211], v[36:39]\n"
"s_waitcnt lgkmcnt(0)\n"
"v_mul_f32_e32 v13, s71, v13\n"
"v_mfma_f32_16x16x32_bf16 a[224:227], v[224:227], a[192:195], a[224:227]\n"
"ds_read_b64_tr_b16 v[232:233], v251 offset:16808\n"
"v_cvt_pk_bf16_f32 v6, v12, v13\n"
"v_mfma_f32_16x16x32_bf16 a[224:227], v[228:231], a[208:211], a[224:227]\n"
"ds_read_b64_tr_b16 v[234:235], v251 offset:17064\n"
"v_mul_f32_e32 v14, s71, v14\n"
"v_mfma_f32_16x16x32_bf16 a[232:235], v[224:227], a[196:199], a[232:235]\n"
"ds_read_b64_tr_b16 v[236:237], v251 offset:17320\n"
"s_sub_u32 s72, s72, 32\n"
"v_mfma_f32_16x16x32_bf16 a[232:235], v[228:231], a[212:215], a[232:235]\n"
"ds_read_b64_tr_b16 v[238:239], v251 offset:17576\n"
"s_add_u32 m0, s68, 37440\n"
"v_mfma_f32_16x16x32_bf16 a[240:243], v[224:227], a[200:203], a[240:243]\n"
"buffer_load_dwordx4 v253, s[48:51], s72 offen offset:2080 nt lds\n"
"s_mov_b32 s70, 2823376\n"
"v_mfma_f32_16x16x32_bf16 a[240:243], v[228:231], a[216:219], a[240:243]\n"
"buffer_load_dwordx4 v242, s[48:51], s72 offen offset:3120 nt lds\n"
"s_add_u32 m0, s68, 4160\n"
"v_mfma_f32_16x16x32_bf16 a[248:251], v[224:227], a[204:207], a[248:251]\n"
"buffer_load_dwordx4 v252, s[40:43], s70 offen offset:3120 lds\n"
"v_mul_f32_e32 v15, s71, v15\n"
"v_mfma_f32_16x16x32_bf16 a[248:251], v[228:231], a[220:223], a[248:251]\n"
"s_waitcnt vmcnt(16) lgkmcnt(0)\n"
"s_barrier\n"
"v_mfma_f32_16x16x32_bf16 a[252:255], v[232:235], a[204:207], a[252:255]\n"
"ds_read_b64_tr_b16 v[224:225], v251 offset:49920\n"
"v_cvt_pk_bf16_f32 v7, v14, v15\n"
"v_mfma_f32_16x16x32_bf16 a[252:255], v[236:239], a[220:223], a[252:255]\n"
"ds_read_b64_tr_b16 v[226:227], v251 offset:50176\n"
"v_mul_f32_e32 v16, s71, v16\n"
"v_mfma_f32_16x16x32_bf16 a[244:247], v[232:235], a[200:203], a[244:247]\n"
"ds_read_b64_tr_b16 v[228:229], v251 offset:50432\n"
"v_mul_f32_e32 v17, s71, v17\n"
"v_mfma_f32_16x16x32_bf16 a[244:247], v[236:239], a[216:219], a[244:247]\n"
"ds_read_b64_tr_b16 v[230:231], v251 offset:50688\n"
"v_cvt_pk_bf16_f32 v8, v16, v17\n"
"v_mfma_f32_16x16x32_bf16 a[236:239], v[232:235], a[196:199], a[236:239]\n"
"s_mov_b32 s69, 3145984\n"
"v_mul_f32_e32 v18, s71, v18\n"
"v_mfma_f32_16x16x32_bf16 a[236:239], v[236:239], a[212:215], a[236:239]\n"
"s_add_u32 m0, s68, 16640\n"
"v_mul_f32_e32 v19, s71, v19\n"
"v_mfma_f32_16x16x32_bf16 a[228:231], v[232:235], a[192:195], a[228:231]\n"
"buffer_load_dwordx4 v252, s[40:43], s69 offen offset:0 lds\n"
"v_cvt_pk_bf16_f32 v9, v18, v19\n"
"v_mfma_f32_16x16x32_bf16 a[228:231], v[236:239], a[208:211], a[228:231]\n"
"s_waitcnt lgkmcnt(0)\n"
"v_mul_f32_e32 v20, s71, v20\n"
"v_mfma_f32_16x16x32_bf16 v[64:67], v[224:227], a[128:131], v[64:67]\n"
"ds_read_b64_tr_b16 v[232:233], v251 offset:49928\n"
"v_mul_f32_e32 v21, s71, v21\n"
"v_mfma_f32_16x16x32_bf16 v[64:67], v[228:231], a[144:147], v[64:67]\n"
"ds_read_b64_tr_b16 v[234:235], v251 offset:50184\n"
"v_cvt_pk_bf16_f32 v10, v20, v21\n"
"v_mfma_f32_16x16x32_bf16 v[72:75], v[224:227], a[132:135], v[72:75]\n"
"ds_read_b64_tr_b16 v[236:237], v251 offset:50440\n"
"v_mul_f32_e32 v22, s71, v22\n"
"v_mfma_f32_16x16x32_bf16 v[72:75], v[228:231], a[148:151], v[72:75]\n"
"ds_read_b64_tr_b16 v[238:239], v251 offset:50696\n"
"v_mul_f32_e32 v23, s71, v23\n"
"v_mfma_f32_16x16x32_bf16 v[80:83], v[224:227], a[136:139], v[80:83]\n"
"v_cvt_pk_bf16_f32 v11, v22, v23\n"
"v_mul_f32_e32 v24, s71, v24\n"
"v_mfma_f32_16x16x32_bf16 v[80:83], v[228:231], a[152:155], v[80:83]\n"
"s_mov_b32 s70, 3276016\n"
"v_mul_f32_e32 v25, s71, v25\n"
"v_mfma_f32_16x16x32_bf16 v[88:91], v[224:227], a[140:143], v[88:91]\n"
"buffer_load_dwordx4 v252, s[40:43], s70 offen offset:1040 lds\n"
"v_cvt_pk_bf16_f32 v12, v24, v25\n"
"v_mfma_f32_16x16x32_bf16 v[88:91], v[228:231], a[156:159], v[88:91]\n"
"s_waitcnt lgkmcnt(0)\n"
"v_mul_f32_e32 v26, s71, v26\n"
"v_mfma_f32_16x16x32_bf16 v[92:95], v[232:235], a[140:143], v[92:95]\n"
"ds_read_b64_tr_b16 v[224:225], v251 offset:49952\n"
"v_mul_f32_e32 v27, s71, v27\n"
"v_mfma_f32_16x16x32_bf16 v[92:95], v[236:239], a[156:159], v[92:95]\n"
"ds_read_b64_tr_b16 v[226:227], v251 offset:50208\n"
"v_cvt_pk_bf16_f32 v13, v26, v27\n"
"v_mfma_f32_16x16x32_bf16 v[84:87], v[232:235], a[136:139], v[84:87]\n"
"ds_read_b64_tr_b16 v[228:229], v251 offset:50464\n"
"v_mul_f32_e32 v28, s71, v28\n"
"v_mfma_f32_16x16x32_bf16 v[84:87], v[236:239], a[152:155], v[84:87]\n"
"ds_read_b64_tr_b16 v[230:231], v251 offset:50720\n"
"v_mul_f32_e32 v29, s71, v29\n"
"v_mfma_f32_16x16x32_bf16 v[76:79], v[232:235], a[132:135], v[76:79]\n"
"v_cvt_pk_bf16_f32 v14, v28, v29\n"
"v_mul_f32_e32 v30, s71, v30\n"
"v_mfma_f32_16x16x32_bf16 v[76:79], v[236:239], a[148:151], v[76:79]\n"
"s_mov_b32 s69, 3209440\n"
"v_mul_f32_e32 v31, s71, v31\n"
"v_mfma_f32_16x16x32_bf16 v[68:71], v[232:235], a[128:131], v[68:71]\n"
"buffer_load_dwordx4 v252, s[40:43], s69 offen offset:2080 lds\n"
"v_cvt_pk_bf16_f32 v15, v30, v31\n"
"v_mfma_f32_16x16x32_bf16 v[68:71], v[236:239], a[144:147], v[68:71]\n"
"s_waitcnt lgkmcnt(0)\n"
"v_mul_f32_e32 v128, s71, v128\n"
"v_mfma_f32_16x16x32_bf16 v[192:195], v[224:227], a[128:131], v[192:195]\n"
"ds_read_b64_tr_b16 v[232:233], v251 offset:49960\n"
"v_mul_f32_e32 v129, s71, v129\n"
"v_mfma_f32_16x16x32_bf16 v[192:195], v[228:231], a[144:147], v[192:195]\n"
"ds_read_b64_tr_b16 v[234:235], v251 offset:50216\n"
"v_cvt_pk_bf16_f32 v16, v128, v129\n"
"v_mfma_f32_16x16x32_bf16 v[200:203], v[224:227], a[132:135], v[200:203]\n"
"ds_read_b64_tr_b16 v[236:237], v251 offset:50472\n"
"v_mul_f32_e32 v130, s71, v130\n"
"v_mfma_f32_16x16x32_bf16 v[200:203], v[228:231], a[148:151], v[200:203]\n"
"ds_read_b64_tr_b16 v[238:239], v251 offset:50728\n"
"v_mul_f32_e32 v131, s71, v131\n"
"v_mfma_f32_16x16x32_bf16 v[208:211], v[224:227], a[136:139], v[208:211]\n"
"v_cvt_pk_bf16_f32 v17, v130, v131\n"
"v_mul_f32_e32 v132, s71, v132\n"
"v_mfma_f32_16x16x32_bf16 v[208:211], v[228:231], a[152:155], v[208:211]\n"
"s_mov_b32 s70, 3339472\n"
"v_mul_f32_e32 v133, s71, v133\n"
"v_mfma_f32_16x16x32_bf16 v[216:219], v[224:227], a[140:143], v[216:219]\n"
"buffer_load_dwordx4 v252, s[40:43], s70 offen offset:3120 lds\n"
"v_cvt_pk_bf16_f32 v18, v132, v133\n"
"v_mfma_f32_16x16x32_bf16 v[216:219], v[228:231], a[156:159], v[216:219]\n"
"s_waitcnt lgkmcnt(0)\n"
"v_mul_f32_e32 v134, s71, v134\n"
"v_mfma_f32_16x16x32_bf16 v[220:223], v[232:235], a[140:143], v[220:223]\n"
"ds_read_b64_tr_b16 v[224:225], v251 offset:50048\n"
"v_mul_f32_e32 v135, s71, v135\n"
"v_mfma_f32_16x16x32_bf16 v[220:223], v[236:239], a[156:159], v[220:223]\n"
"ds_read_b64_tr_b16 v[226:227], v251 offset:50304\n"
"v_cvt_pk_bf16_f32 v19, v134, v135\n"
"v_mfma_f32_16x16x32_bf16 v[212:215], v[232:235], a[136:139], v[212:215]\n"
"ds_read_b64_tr_b16 v[228:229], v251 offset:50560\n"
"v_mul_f32_e32 v136, s71, v136\n"
"v_mfma_f32_16x16x32_bf16 v[212:215], v[236:239], a[152:155], v[212:215]\n"
"ds_read_b64_tr_b16 v[230:231], v251 offset:50816\n"
"v_mul_f32_e32 v137, s71, v137\n"
"v_mfma_f32_16x16x32_bf16 v[204:207], v[232:235], a[132:135], v[204:207]\n"
"s_mov_b32 s69, 3154176\n"
"v_cvt_pk_bf16_f32 v20, v136, v137\n"
"v_mfma_f32_16x16x32_bf16 v[204:207], v[236:239], a[148:151], v[204:207]\n"
"s_add_u32 m0, s68, 20800\n"
"v_mul_f32_e32 v138, s71, v138\n"
"v_mfma_f32_16x16x32_bf16 v[196:199], v[232:235], a[128:131], v[196:199]\n"
"buffer_load_dwordx4 v252, s[40:43], s69 offen offset:0 lds\n"
"v_mul_f32_e32 v139, s71, v139\n"
"v_mfma_f32_16x16x32_bf16 v[196:199], v[236:239], a[144:147], v[196:199]\n"
"s_waitcnt lgkmcnt(0)\n"
"v_cvt_pk_bf16_f32 v21, v138, v139\n"
"v_mfma_f32_16x16x32_bf16 v[96:99], v[224:227], a[128:131], v[96:99]\n"
"ds_read_b64_tr_b16 v[232:233], v251 offset:50056\n"
"v_mul_f32_e32 v140, s71, v140\n"
"v_mfma_f32_16x16x32_bf16 v[96:99], v[228:231], a[144:147], v[96:99]\n"
"ds_read_b64_tr_b16 v[234:235], v251 offset:50312\n"
"v_mul_f32_e32 v141, s71, v141\n"
"v_mfma_f32_16x16x32_bf16 v[104:107], v[224:227], a[132:135], v[104:107]\n"
"ds_read_b64_tr_b16 v[236:237], v251 offset:50568\n"
"v_cvt_pk_bf16_f32 v22, v140, v141\n"
"v_mfma_f32_16x16x32_bf16 v[104:107], v[228:231], a[148:151], v[104:107]\n"
"ds_read_b64_tr_b16 v[238:239], v251 offset:50824\n"
"v_mul_f32_e32 v142, s71, v142\n"
"v_mfma_f32_16x16x32_bf16 v[112:115], v[224:227], a[136:139], v[112:115]\n"
"v_mul_f32_e32 v143, s71, v143\n"
"v_cvt_pk_bf16_f32 v23, v142, v143\n"
"v_mfma_f32_16x16x32_bf16 v[112:115], v[228:231], a[152:155], v[112:115]\n"
"s_mov_b32 s70, 3284208\n"
"v_mul_f32_e32 v144, s71, v144\n"
"v_mfma_f32_16x16x32_bf16 v[120:123], v[224:227], a[140:143], v[120:123]\n"
"buffer_load_dwordx4 v252, s[40:43], s70 offen offset:1040 lds\n"
"v_mul_f32_e32 v145, s71, v145\n"
"v_mfma_f32_16x16x32_bf16 v[120:123], v[228:231], a[156:159], v[120:123]\n"
"s_waitcnt lgkmcnt(0)\n"
"v_cvt_pk_bf16_f32 v24, v144, v145\n"
"v_mfma_f32_16x16x32_bf16 v[124:127], v[232:235], a[140:143], v[124:127]\n"
"ds_read_b64_tr_b16 v[224:225], v251 offset:50080\n"
"v_mul_f32_e32 v146, s71, v146\n"
"v_mfma_f32_16x16x32_bf16 v[124:127], v[236:239], a[156:159], v[124:127]\n"
"ds_read_b64_tr_b16 v[226:227], v251 offset:50336\n"
"v_mul_f32_e32 v147, s71, v147\n"
"v_mfma_f32_16x16x32_bf16 v[116:119], v[232:235], a[136:139], v[116:119]\n"
"ds_read_b64_tr_b16 v[228:229], v251 offset:50592\n"
"v_cvt_pk_bf16_f32 v25, v146, v147\n"
"v_mfma_f32_16x16x32_bf16 v[116:119], v[236:239], a[152:155], v[116:119]\n"
"ds_read_b64_tr_b16 v[230:231], v251 offset:50848\n"
"v_mul_f32_e32 v148, s71, v148\n"
"v_mfma_f32_16x16x32_bf16 v[108:111], v[232:235], a[132:135], v[108:111]\n"
"v_mul_f32_e32 v149, s71, v149\n"
"v_cvt_pk_bf16_f32 v26, v148, v149\n"
"v_mfma_f32_16x16x32_bf16 v[108:111], v[236:239], a[148:151], v[108:111]\n"
"s_mov_b32 s69, 3217632\n"
"v_mul_f32_e32 v150, s71, v150\n"
"v_mfma_f32_16x16x32_bf16 v[100:103], v[232:235], a[128:131], v[100:103]\n"
"buffer_load_dwordx4 v252, s[40:43], s69 offen offset:2080 lds\n"
"v_mul_f32_e32 v151, s71, v151\n"
"v_mfma_f32_16x16x32_bf16 v[100:103], v[236:239], a[144:147], v[100:103]\n"
"s_waitcnt lgkmcnt(0)\n"
"v_cvt_pk_bf16_f32 v27, v150, v151\n"
"v_mfma_f32_16x16x32_bf16 v[160:163], v[224:227], a[128:131], v[160:163]\n"
"ds_read_b64_tr_b16 v[232:233], v251 offset:50088\n"
"v_mul_f32_e32 v152, s71, v152\n"
"v_mfma_f32_16x16x32_bf16 v[160:163], v[228:231], a[144:147], v[160:163]\n"
"ds_read_b64_tr_b16 v[234:235], v251 offset:50344\n"
"v_mul_f32_e32 v153, s71, v153\n"
"v_mfma_f32_16x16x32_bf16 v[168:171], v[224:227], a[132:135], v[168:171]\n"
"ds_read_b64_tr_b16 v[236:237], v251 offset:50600\n"
"v_cvt_pk_bf16_f32 v28, v152, v153\n"
"v_mfma_f32_16x16x32_bf16 v[168:171], v[228:231], a[148:151], v[168:171]\n"
"ds_read_b64_tr_b16 v[238:239], v251 offset:50856\n"
"v_mul_f32_e32 v154, s71, v154\n"
"v_mfma_f32_16x16x32_bf16 v[176:179], v[224:227], a[136:139], v[176:179]\n"
"v_mul_f32_e32 v155, s71, v155\n"
"v_cvt_pk_bf16_f32 v29, v154, v155\n"
"v_mfma_f32_16x16x32_bf16 v[176:179], v[228:231], a[152:155], v[176:179]\n"
"s_mov_b32 s70, 3347664\n"
"v_mul_f32_e32 v156, s71, v156\n"
"v_mfma_f32_16x16x32_bf16 v[184:187], v[224:227], a[140:143], v[184:187]\n"
"buffer_load_dwordx4 v252, s[40:43], s70 offen offset:3120 lds\n"
"v_mul_f32_e32 v157, s71, v157\n"
"v_mfma_f32_16x16x32_bf16 v[184:187], v[228:231], a[156:159], v[184:187]\n"
"s_waitcnt vmcnt(8) lgkmcnt(0)\n"
"s_barrier\n"
"v_mfma_f32_16x16x32_bf16 v[188:191], v[232:235], a[140:143], v[188:191]\n"
"ds_read_b64_tr_b16 v[224:225], v251 offset:0\n"
"s_mov_b64 s[36:37], s[20:21]\n"
"v_mfma_f32_16x16x32_bf16 v[188:191], v[236:239], a[156:159], v[188:191]\n"
"ds_read_b64_tr_b16 v[226:227], v251 offset:256\n"
"v_cvt_pk_bf16_f32 v30, v156, v157\n"
"v_mfma_f32_16x16x32_bf16 v[180:183], v[232:235], a[136:139], v[180:183]\n"
"ds_read_b64_tr_b16 v[228:229], v251 offset:512\n"
"v_mul_f32_e32 v158, s71, v158\n"
"v_mfma_f32_16x16x32_bf16 v[180:183], v[236:239], a[152:155], v[180:183]\n"
"ds_read_b64_tr_b16 v[230:231], v251 offset:768\n"
"v_mul_f32_e32 v159, s71, v159\n"
"v_mfma_f32_16x16x32_bf16 v[172:175], v[232:235], a[132:135], v[172:175]\n"
"s_add_u32 s72, s72, 16416\n"
"v_cvt_pk_bf16_f32 v31, v158, v159\n"
"v_mfma_f32_16x16x32_bf16 v[172:175], v[236:239], a[148:151], v[172:175]\n"
"s_add_u32 m0, s68, 49920\n"
"v_mul_f32_e32 v32, s71, v32\n"
"v_mfma_f32_16x16x32_bf16 v[164:167], v[232:235], a[128:131], v[164:167]\n"
"buffer_load_dwordx4 v253, s[44:47], s72 offen offset:0 nt lds\n"
"v_mul_f32_e32 v33, s71, v33\n"
"v_mfma_f32_16x16x32_bf16 v[164:167], v[236:239], a[144:147], v[164:167]\n"
"s_waitcnt lgkmcnt(0)\n"
"v_cvt_pk_bf16_f32 v32, v32, v33\n"
"v_mfma_f32_16x16x32_bf16 v[64:67], v[224:227], a[160:163], v[64:67]\n"
"ds_read_b64_tr_b16 v[232:233], v251 offset:8\n"
"v_mul_f32_e32 v34, s71, v34\n"
"v_mfma_f32_16x16x32_bf16 v[64:67], v[228:231], a[176:179], v[64:67]\n"
"ds_read_b64_tr_b16 v[234:235], v251 offset:264\n"
"v_mul_f32_e32 v35, s71, v35\n"
"v_mfma_f32_16x16x32_bf16 v[72:75], v[224:227], a[164:167], v[72:75]\n"
"ds_read_b64_tr_b16 v[236:237], v251 offset:520\n"
"v_cvt_pk_bf16_f32 v33, v34, v35\n"
"v_mfma_f32_16x16x32_bf16 v[72:75], v[228:231], a[180:183], v[72:75]\n"
"ds_read_b64_tr_b16 v[238:239], v251 offset:776\n"
"v_mul_f32_e32 v36, s71, v36\n"
"v_mfma_f32_16x16x32_bf16 v[80:83], v[224:227], a[168:171], v[80:83]\n"
"s_cmp_eq_u32 s83, 1\n"
"s_cselect_b32 s38, 66560, 0\n"
"v_mfma_f32_16x16x32_bf16 v[80:83], v[228:231], a[184:187], v[80:83]\n"
"s_mov_b32 s39, 0x20000\n"
"s_lshl_b32 s77, s82, 7\n"
"v_mfma_f32_16x16x32_bf16 v[88:91], v[224:227], a[172:175], v[88:91]\n"
"buffer_load_dwordx4 v242, s[44:47], s72 offen offset:1040 nt lds\n"
"s_or_b32 s77, s77, s85\n"
"v_mfma_f32_16x16x32_bf16 v[88:91], v[228:231], a[188:191], v[88:91]\n"
"s_waitcnt lgkmcnt(0)\n"
"s_mul_i32 s77, s77, 1792\n"
"v_mfma_f32_16x16x32_bf16 v[92:95], v[232:235], a[172:175], v[92:95]\n"
"ds_read_b64_tr_b16 v[224:225], v251 offset:32\n"
"v_mul_f32_e32 v37, s71, v37\n"
"v_mfma_f32_16x16x32_bf16 v[92:95], v[236:239], a[188:191], v[92:95]\n"
"ds_read_b64_tr_b16 v[226:227], v251 offset:288\n"
"v_cvt_pk_bf16_f32 v34, v36, v37\n"
"v_mfma_f32_16x16x32_bf16 v[84:87], v[232:235], a[168:171], v[84:87]\n"
"ds_read_b64_tr_b16 v[228:229], v251 offset:544\n"
"v_mul_f32_e32 v38, s71, v38\n"
"v_mfma_f32_16x16x32_bf16 v[84:87], v[236:239], a[184:187], v[84:87]\n"
"ds_read_b64_tr_b16 v[230:231], v251 offset:800\n"
"v_mul_f32_e32 v39, s71, v39\n"
"v_mfma_f32_16x16x32_bf16 v[76:79], v[232:235], a[164:167], v[76:79]\n"
"s_add_u32 s60, s12, s77\n"
"s_addc_u32 s61, s13, 0\n"
"v_mfma_f32_16x16x32_bf16 v[76:79], v[236:239], a[180:183], v[76:79]\n"
"s_sub_u32 s72, s72, 32\n"
"s_and_b32 s61, s61, 0xffff\n"
"v_mfma_f32_16x16x32_bf16 v[68:71], v[232:235], a[160:163], v[68:71]\n"
"buffer_load_dwordx4 v253, s[44:47], s72 offen offset:2080 nt lds\n"
"s_sub_u32 s62, 3670016, s77\n"
"v_mfma_f32_16x16x32_bf16 v[68:71], v[236:239], a[176:179], v[68:71]\n"
"s_waitcnt lgkmcnt(0)\n"
"s_mov_b32 s63, 0x20000\n"
"v_mfma_f32_16x16x32_bf16 v[192:195], v[224:227], a[160:163], v[192:195]\n"
"ds_read_b64_tr_b16 v[232:233], v251 offset:40\n"
"s_lshl_b32 s77, s88, 1\n"
"v_mfma_f32_16x16x32_bf16 v[192:195], v[228:231], a[176:179], v[192:195]\n"
"ds_read_b64_tr_b16 v[234:235], v251 offset:296\n"
"s_or_b32 s77, s77, s86\n"
"v_mfma_f32_16x16x32_bf16 v[200:203], v[224:227], a[164:167], v[200:203]\n"
"ds_read_b64_tr_b16 v[236:237], v251 offset:552\n"
"s_lshl_b32 s77, s77, 2\n"
"v_mfma_f32_16x16x32_bf16 v[200:203], v[228:231], a[180:183], v[200:203]\n"
"ds_read_b64_tr_b16 v[238:239], v251 offset:808\n"
"s_or_b32 s77, s77, s83\n"
"v_mfma_f32_16x16x32_bf16 v[208:211], v[224:227], a[168:171], v[208:211]\n"
"s_mul_i32 s77, s77, 237568\n"
"s_mul_i32 s80, s87, 14336\n"
"v_mfma_f32_16x16x32_bf16 v[208:211], v[228:231], a[184:187], v[208:211]\n"
"s_add_u32 s77, s77, s80\n"
"v_cvt_pk_bf16_f32 v35, v38, v39\n"
"v_mfma_f32_16x16x32_bf16 v[216:219], v[224:227], a[172:175], v[216:219]\n"
"buffer_load_dwordx4 v242, s[44:47], s72 offen offset:3120 nt lds\n"
"v_mul_f32_e32 v40, s71, v40\n"
"v_mfma_f32_16x16x32_bf16 v[216:219], v[228:231], a[188:191], v[216:219]\n"
"s_waitcnt lgkmcnt(0)\n"
"v_mul_f32_e32 v41, s71, v41\n"
"v_mfma_f32_16x16x32_bf16 v[220:223], v[232:235], a[172:175], v[220:223]\n"
"ds_read_b64_tr_b16 v[224:225], v251 offset:128\n"
"v_cvt_pk_bf16_f32 v36, v40, v41\n"
"v_mfma_f32_16x16x32_bf16 v[220:223], v[236:239], a[188:191], v[220:223]\n"
"ds_read_b64_tr_b16 v[226:227], v251 offset:384\n"
"v_mul_f32_e32 v42, s71, v42\n"
"v_mfma_f32_16x16x32_bf16 v[212:215], v[232:235], a[168:171], v[212:215]\n"
"ds_read_b64_tr_b16 v[228:229], v251 offset:640\n"
"v_mul_f32_e32 v43, s71, v43\n"
"v_mfma_f32_16x16x32_bf16 v[212:215], v[236:239], a[184:187], v[212:215]\n"
"ds_read_b64_tr_b16 v[230:231], v251 offset:896\n"
"v_cvt_pk_bf16_f32 v37, v42, v43\n"
"v_mfma_f32_16x16x32_bf16 v[204:207], v[232:235], a[164:167], v[204:207]\n"
"s_add_u32 s72, s72, 32\n"
"v_mul_f32_e32 v44, s71, v44\n"
"v_mfma_f32_16x16x32_bf16 v[204:207], v[236:239], a[180:183], v[204:207]\n"
"s_add_u32 m0, s68, 54080\n"
"v_mul_f32_e32 v45, s71, v45\n"
"v_mfma_f32_16x16x32_bf16 v[196:199], v[232:235], a[160:163], v[196:199]\n"
"buffer_load_dwordx4 v253, s[48:51], s72 offen offset:0 nt lds\n"
"v_cvt_pk_bf16_f32 v38, v44, v45\n"
"v_mfma_f32_16x16x32_bf16 v[196:199], v[236:239], a[176:179], v[196:199]\n"
"s_waitcnt lgkmcnt(0)\n"
"v_mul_f32_e32 v46, s71, v46\n"
"v_mfma_f32_16x16x32_bf16 v[96:99], v[224:227], a[160:163], v[96:99]\n"
"ds_read_b64_tr_b16 v[232:233], v251 offset:136\n"
"v_mul_f32_e32 v47, s71, v47\n"
"v_mfma_f32_16x16x32_bf16 v[96:99], v[228:231], a[176:179], v[96:99]\n"
"ds_read_b64_tr_b16 v[234:235], v251 offset:392\n"
"v_cvt_pk_bf16_f32 v39, v46, v47\n"
"v_mfma_f32_16x16x32_bf16 v[104:107], v[224:227], a[164:167], v[104:107]\n"
"ds_read_b64_tr_b16 v[236:237], v251 offset:648\n"
"v_mul_f32_e32 v48, s71, v48\n"
"v_mfma_f32_16x16x32_bf16 v[104:107], v[228:231], a[180:183], v[104:107]\n"
"ds_read_b64_tr_b16 v[238:239], v251 offset:904\n"
"v_mul_f32_e32 v49, s71, v49\n"
"v_mfma_f32_16x16x32_bf16 v[112:115], v[224:227], a[168:171], v[112:115]\n"
"s_add_u32 s64, s14, s77\n"
"s_addc_u32 s65, s15, 0\n"
"v_mfma_f32_16x16x32_bf16 v[112:115], v[228:231], a[184:187], v[112:115]\n"
"s_and_b32 s65, s65, 0xffff\n"
"s_sub_u32 s66, 30408704, s77\n"
"v_mfma_f32_16x16x32_bf16 v[120:123], v[224:227], a[172:175], v[120:123]\n"
"buffer_load_dwordx4 v242, s[48:51], s72 offen offset:1040 nt lds\n"
"s_mov_b32 s67, 0x20000\n"
"v_mfma_f32_16x16x32_bf16 v[120:123], v[228:231], a[188:191], v[120:123]\n"
"s_waitcnt lgkmcnt(0)\n"
"s_lshl_b32 s77, s88, 1\n"
"v_mfma_f32_16x16x32_bf16 v[124:127], v[232:235], a[172:175], v[124:127]\n"
"ds_read_b64_tr_b16 v[224:225], v251 offset:160\n"
"s_or_b32 s77, s77, s86\n"
"v_mfma_f32_16x16x32_bf16 v[124:127], v[236:239], a[188:191], v[124:127]\n"
"ds_read_b64_tr_b16 v[226:227], v251 offset:416\n"
"s_lshl_b32 s77, s77, 3\n"
"v_mfma_f32_16x16x32_bf16 v[116:119], v[232:235], a[168:171], v[116:119]\n"
"ds_read_b64_tr_b16 v[228:229], v251 offset:672\n"
"s_or_b32 s77, s77, s87\n"
"v_mfma_f32_16x16x32_bf16 v[116:119], v[236:239], a[184:187], v[116:119]\n"
"ds_read_b64_tr_b16 v[230:231], v251 offset:928\n"
"s_and_b32 s80, s77, 15\n"
"v_mfma_f32_16x16x32_bf16 v[108:111], v[232:235], a[164:167], v[108:111]\n"
"s_cmp_eq_u32 s80, 15\n"
"s_cselect_b32 s26, 0, 114688\n"
"v_mfma_f32_16x16x32_bf16 v[108:111], v[236:239], a[180:183], v[108:111]\n"
"s_sub_u32 s72, s72, 32\n"
"s_mul_i32 s77, s77, 118784\n"
"v_mfma_f32_16x16x32_bf16 v[100:103], v[232:235], a[160:163], v[100:103]\n"
"buffer_load_dwordx4 v253, s[48:51], s72 offen offset:2080 nt lds\n"
"v_cvt_pk_bf16_f32 v40, v48, v49\n"
"v_mfma_f32_16x16x32_bf16 v[100:103], v[236:239], a[176:179], v[100:103]\n"
"s_waitcnt lgkmcnt(0)\n"
"v_mul_f32_e32 v50, s71, v50\n"
"v_mfma_f32_16x16x32_bf16 v[160:163], v[224:227], a[160:163], v[160:163]\n"
"ds_read_b64_tr_b16 v[232:233], v251 offset:168\n"
"v_mul_f32_e32 v51, s71, v51\n"
"v_mfma_f32_16x16x32_bf16 v[160:163], v[228:231], a[176:179], v[160:163]\n"
"ds_read_b64_tr_b16 v[234:235], v251 offset:424\n"
"v_cvt_pk_bf16_f32 v41, v50, v51\n"
"v_mfma_f32_16x16x32_bf16 v[168:171], v[224:227], a[164:167], v[168:171]\n"
"ds_read_b64_tr_b16 v[236:237], v251 offset:680\n"
"v_mul_f32_e32 v52, s71, v52\n"
"v_mfma_f32_16x16x32_bf16 v[168:171], v[228:231], a[180:183], v[168:171]\n"
"ds_read_b64_tr_b16 v[238:239], v251 offset:936\n"
"v_mul_f32_e32 v53, s71, v53\n"
"v_mfma_f32_16x16x32_bf16 v[176:179], v[224:227], a[168:171], v[176:179]\n"
"s_add_u32 s24, s14, s77\n"
"s_addc_u32 s25, s15, 0\n"
"v_mfma_f32_16x16x32_bf16 v[176:179], v[228:231], a[184:187], v[176:179]\n"
"s_and_b32 s25, s25, 0xffff\n"
"s_mov_b32 s27, 0x20000\n"
"v_mfma_f32_16x16x32_bf16 v[184:187], v[224:227], a[172:175], v[184:187]\n"
"buffer_load_dwordx4 v242, s[48:51], s72 offen offset:3120 nt lds\n"
"s_mul_i32 s77, s88, 215040\n"
"v_mfma_f32_16x16x32_bf16 v[184:187], v[228:231], a[188:191], v[184:187]\n"
"s_waitcnt vmcnt(8) lgkmcnt(0)\n"
"s_barrier\n"
"v_mfma_f32_16x16x32_bf16 v[188:191], v[232:235], a[172:175], v[188:191]\n"
"ds_read_b64_tr_b16 v[224:225], v251 offset:16640\n"
"v_cvt_pk_bf16_f32 v42, v52, v53\n"
"v_mfma_f32_16x16x32_bf16 v[188:191], v[236:239], a[188:191], v[188:191]\n"
"ds_read_b64_tr_b16 v[226:227], v251 offset:16896\n"
"v_mul_f32_e32 v54, s71, v54\n"
"v_mfma_f32_16x16x32_bf16 v[180:183], v[232:235], a[168:171], v[180:183]\n"
"ds_read_b64_tr_b16 v[228:229], v251 offset:17152\n"
"v_mul_f32_e32 v55, s71, v55\n"
"v_mfma_f32_16x16x32_bf16 v[180:183], v[236:239], a[184:187], v[180:183]\n"
"ds_read_b64_tr_b16 v[230:231], v251 offset:17408\n"
"v_cvt_pk_bf16_f32 v43, v54, v55\n"
"v_mfma_f32_16x16x32_bf16 v[172:175], v[232:235], a[164:167], v[172:175]\n"
"s_add_u32 s73, s73, 16416\n"
"v_mul_f32_e32 v56, s71, v56\n"
"v_mfma_f32_16x16x32_bf16 v[172:175], v[236:239], a[180:183], v[172:175]\n"
"s_add_u32 m0, s68, 0\n"
"v_mul_f32_e32 v57, s71, v57\n"
"v_mfma_f32_16x16x32_bf16 v[164:167], v[232:235], a[160:163], v[164:167]\n"
"buffer_load_dwordx4 v253, s[52:55], s73 offen offset:0 nt lds\n"
"v_cvt_pk_bf16_f32 v44, v56, v57\n"
"v_mfma_f32_16x16x32_bf16 v[164:167], v[236:239], a[176:179], v[164:167]\n"
"s_waitcnt lgkmcnt(0)\n"
"v_mul_f32_e32 v58, s71, v58\n"
"v_mfma_f32_16x16x32_bf16 v[64:67], v[224:227], a[192:195], v[64:67]\n"
"ds_read_b64_tr_b16 v[232:233], v251 offset:16648\n"
"v_mul_f32_e32 v59, s71, v59\n"
"v_mfma_f32_16x16x32_bf16 v[64:67], v[228:231], a[208:211], v[64:67]\n"
"ds_read_b64_tr_b16 v[234:235], v251 offset:16904\n"
"v_cvt_pk_bf16_f32 v45, v58, v59\n"
"v_mfma_f32_16x16x32_bf16 v[72:75], v[224:227], a[196:199], v[72:75]\n"
"ds_read_b64_tr_b16 v[236:237], v251 offset:17160\n"
"v_mul_f32_e32 v60, s71, v60\n"
"v_mfma_f32_16x16x32_bf16 v[72:75], v[228:231], a[212:215], v[72:75]\n"
"ds_read_b64_tr_b16 v[238:239], v251 offset:17416\n"
"v_mul_f32_e32 v61, s71, v61\n"
"v_mfma_f32_16x16x32_bf16 v[80:83], v[224:227], a[200:203], v[80:83]\n"
"s_add_u32 s28, s16, s77\n"
"s_addc_u32 s29, s17, 0\n"
"v_mfma_f32_16x16x32_bf16 v[80:83], v[228:231], a[216:219], v[80:83]\n"
"s_and_b32 s29, s29, 0xffff\n"
"s_mov_b32 s30, 215040\n"
"v_mfma_f32_16x16x32_bf16 v[88:91], v[224:227], a[204:207], v[88:91]\n"
"buffer_load_dwordx4 v242, s[52:55], s73 offen offset:1040 nt lds\n"
"s_mov_b32 s31, 0x20000\n"
"v_mfma_f32_16x16x32_bf16 v[88:91], v[228:231], a[220:223], v[88:91]\n"
"s_waitcnt lgkmcnt(0)\n"
"v_cvt_pk_bf16_f32 v46, v60, v61\n"
"v_mfma_f32_16x16x32_bf16 v[92:95], v[232:235], a[204:207], v[92:95]\n"
"ds_read_b64_tr_b16 v[224:225], v251 offset:16672\n"
"v_mul_f32_e32 v62, s71, v62\n"
"v_mfma_f32_16x16x32_bf16 v[92:95], v[236:239], a[220:223], v[92:95]\n"
"ds_read_b64_tr_b16 v[226:227], v251 offset:16928\n"
"v_mul_f32_e32 v63, s71, v63\n"
"v_mfma_f32_16x16x32_bf16 v[84:87], v[232:235], a[200:203], v[84:87]\n"
"ds_read_b64_tr_b16 v[228:229], v251 offset:17184\n"
"v_cvt_pk_bf16_f32 v47, v62, v63\n"
"v_mfma_f32_16x16x32_bf16 v[84:87], v[236:239], a[216:219], v[84:87]\n"
"ds_read_b64_tr_b16 v[230:231], v251 offset:17440\n"
"v_accvgpr_read_b32 v243, a224\n"
"v_mfma_f32_16x16x32_bf16 v[76:79], v[232:235], a[196:199], v[76:79]\n"
"s_branch .Lpo4\n"
".Lpg4:\n"
"s_setpc_b64 s[78:79]\n"
".Lpo4:\n"
"v_accvgpr_read_b32 v244, a225\n"
"v_mul_f32_e32 v243, s71, v243\n"
"v_mfma_f32_16x16x32_bf16 v[76:79], v[236:239], a[212:215], v[76:79]\n"
"s_sub_u32 s73, s73, 32\n"
"v_mul_f32_e32 v244, s71, v244\n"
"v_mfma_f32_16x16x32_bf16 v[68:71], v[232:235], a[192:195], v[68:71]\n"
"buffer_load_dwordx4 v253, s[52:55], s73 offen offset:2080 nt lds\n"
"v_cvt_pk_bf16_f32 v48, v243, v244\n"
"v_mfma_f32_16x16x32_bf16 v[68:71], v[236:239], a[208:211], v[68:71]\n"
"s_waitcnt lgkmcnt(0)\n"
"v_accvgpr_read_b32 v245, a226\n"
"v_mfma_f32_16x16x32_bf16 v[192:195], v[224:227], a[192:195], v[192:195]\n"
"ds_read_b64_tr_b16 v[232:233], v251 offset:16680\n"
"v_accvgpr_read_b32 v246, a227\n"
"v_mfma_f32_16x16x32_bf16 v[192:195], v[228:231], a[208:211], v[192:195]\n"
"ds_read_b64_tr_b16 v[234:235], v251 offset:16936\n"
"v_mul_f32_e32 v245, s71, v245\n"
"v_mfma_f32_16x16x32_bf16 v[200:203], v[224:227], a[196:199], v[200:203]\n"
"ds_read_b64_tr_b16 v[236:237], v251 offset:17192\n"
"v_mul_f32_e32 v246, s71, v246\n"
"v_mfma_f32_16x16x32_bf16 v[200:203], v[228:231], a[212:215], v[200:203]\n"
"ds_read_b64_tr_b16 v[238:239], v251 offset:17448\n"
"v_cvt_pk_bf16_f32 v49, v245, v246\n"
"v_mfma_f32_16x16x32_bf16 v[208:211], v[224:227], a[200:203], v[208:211]\n"
"v_accvgpr_read_b32 v243, a228\n"
"v_accvgpr_read_b32 v244, a229\n"
"v_mfma_f32_16x16x32_bf16 v[208:211], v[228:231], a[216:219], v[208:211]\n"
"v_mul_f32_e32 v243, s71, v243\n"
"v_mul_f32_e32 v244, s71, v244\n"
"v_mfma_f32_16x16x32_bf16 v[216:219], v[224:227], a[204:207], v[216:219]\n"
"buffer_load_dwordx4 v242, s[52:55], s73 offen offset:3120 nt lds\n"
"v_cvt_pk_bf16_f32 v50, v243, v244\n"
"v_mfma_f32_16x16x32_bf16 v[216:219], v[228:231], a[220:223], v[216:219]\n"
"s_waitcnt lgkmcnt(0)\n"
"v_accvgpr_read_b32 v245, a230\n"
"v_mfma_f32_16x16x32_bf16 v[220:223], v[232:235], a[204:207], v[220:223]\n"
"ds_read_b64_tr_b16 v[224:225], v251 offset:16768\n"
"v_accvgpr_read_b32 v246, a231\n"
"v_mfma_f32_16x16x32_bf16 v[220:223], v[236:239], a[220:223], v[220:223]\n"
"ds_read_b64_tr_b16 v[226:227], v251 offset:17024\n"
"v_mul_f32_e32 v245, s71, v245\n"
"v_mfma_f32_16x16x32_bf16 v[212:215], v[232:235], a[200:203], v[212:215]\n"
"ds_read_b64_tr_b16 v[228:229], v251 offset:17280\n"
"v_mul_f32_e32 v246, s71, v246\n"
"v_mfma_f32_16x16x32_bf16 v[212:215], v[236:239], a[216:219], v[212:215]\n"
"ds_read_b64_tr_b16 v[230:231], v251 offset:17536\n"
"v_cvt_pk_bf16_f32 v51, v245, v246\n"
"v_mfma_f32_16x16x32_bf16 v[204:207], v[232:235], a[196:199], v[204:207]\n"
"s_add_u32 s73, s73, 32\n"
"v_accvgpr_read_b32 v243, a232\n"
"v_mfma_f32_16x16x32_bf16 v[204:207], v[236:239], a[212:215], v[204:207]\n"
"s_add_u32 m0, s68, 4160\n"
"v_accvgpr_read_b32 v244, a233\n"
"v_mfma_f32_16x16x32_bf16 v[196:199], v[232:235], a[192:195], v[196:199]\n"
"buffer_load_dwordx4 v253, s[56:59], s73 offen offset:0 nt lds\n"
"v_mul_f32_e32 v243, s71, v243\n"
"v_mfma_f32_16x16x32_bf16 v[196:199], v[236:239], a[208:211], v[196:199]\n"
"s_waitcnt lgkmcnt(0)\n"
"v_mul_f32_e32 v244, s71, v244\n"
"v_mfma_f32_16x16x32_bf16 v[96:99], v[224:227], a[192:195], v[96:99]\n"
"ds_read_b64_tr_b16 v[232:233], v251 offset:16776\n"
"v_cvt_pk_bf16_f32 v52, v243, v244\n"
"v_mfma_f32_16x16x32_bf16 v[96:99], v[228:231], a[208:211], v[96:99]\n"
"ds_read_b64_tr_b16 v[234:235], v251 offset:17032\n"
"v_accvgpr_read_b32 v245, a234\n"
"v_mfma_f32_16x16x32_bf16 v[104:107], v[224:227], a[196:199], v[104:107]\n"
"ds_read_b64_tr_b16 v[236:237], v251 offset:17288\n"
"v_accvgpr_read_b32 v246, a235\n"
"v_mfma_f32_16x16x32_bf16 v[104:107], v[228:231], a[212:215], v[104:107]\n"
"ds_read_b64_tr_b16 v[238:239], v251 offset:17544\n"
"v_mul_f32_e32 v245, s71, v245\n"
"v_mfma_f32_16x16x32_bf16 v[112:115], v[224:227], a[200:203], v[112:115]\n"
"v_mul_f32_e32 v246, s71, v246\n"
"v_cvt_pk_bf16_f32 v53, v245, v246\n"
"v_mfma_f32_16x16x32_bf16 v[112:115], v[228:231], a[216:219], v[112:115]\n"
"v_accvgpr_read_b32 v243, a236\n"
"v_accvgpr_read_b32 v244, a237\n"
"v_mfma_f32_16x16x32_bf16 v[120:123], v[224:227], a[204:207], v[120:123]\n"
"buffer_load_dwordx4 v242, s[56:59], s73 offen offset:1040 nt lds\n"
"v_mul_f32_e32 v243, s71, v243\n"
"v_mfma_f32_16x16x32_bf16 v[120:123], v[228:231], a[220:223], v[120:123]\n"
"s_waitcnt lgkmcnt(0)\n"
"v_mul_f32_e32 v244, s71, v244\n"
"v_mfma_f32_16x16x32_bf16 v[124:127], v[232:235], a[204:207], v[124:127]\n"
"ds_read_b64_tr_b16 v[224:225], v251 offset:16800\n"
"v_cvt_pk_bf16_f32 v54, v243, v244\n"
"v_mfma_f32_16x16x32_bf16 v[124:127], v[236:239], a[220:223], v[124:127]\n"
"ds_read_b64_tr_b16 v[226:227], v251 offset:17056\n"
"v_accvgpr_read_b32 v245, a238\n"
"v_mfma_f32_16x16x32_bf16 v[116:119], v[232:235], a[200:203], v[116:119]\n"
"ds_read_b64_tr_b16 v[228:229], v251 offset:17312\n"
"v_accvgpr_read_b32 v246, a239\n"
"v_mfma_f32_16x16x32_bf16 v[116:119], v[236:239], a[216:219], v[116:119]\n"
"ds_read_b64_tr_b16 v[230:231], v251 offset:17568\n"
"v_mul_f32_e32 v245, s71, v245\n"
"v_mfma_f32_16x16x32_bf16 v[108:111], v[232:235], a[196:199], v[108:111]\n"
"v_mul_f32_e32 v246, s71, v246\n"
"v_cvt_pk_bf16_f32 v55, v245, v246\n"
"v_mfma_f32_16x16x32_bf16 v[108:111], v[236:239], a[212:215], v[108:111]\n"
"s_sub_u32 s73, s73, 32\n"
"v_accvgpr_read_b32 v243, a240\n"
"v_mfma_f32_16x16x32_bf16 v[100:103], v[232:235], a[192:195], v[100:103]\n"
"buffer_load_dwordx4 v253, s[56:59], s73 offen offset:2080 nt lds\n"
"v_accvgpr_read_b32 v244, a241\n"
"v_mfma_f32_16x16x32_bf16 v[100:103], v[236:239], a[208:211], v[100:103]\n"
"s_waitcnt lgkmcnt(0)\n"
"v_mul_f32_e32 v243, s71, v243\n"
"v_mfma_f32_16x16x32_bf16 v[160:163], v[224:227], a[192:195], v[160:163]\n"
"ds_read_b64_tr_b16 v[232:233], v251 offset:16808\n"
"v_mul_f32_e32 v244, s71, v244\n"
"v_mfma_f32_16x16x32_bf16 v[160:163], v[228:231], a[208:211], v[160:163]\n"
"ds_read_b64_tr_b16 v[234:235], v251 offset:17064\n"
"v_cvt_pk_bf16_f32 v56, v243, v244\n"
"v_mfma_f32_16x16x32_bf16 v[168:171], v[224:227], a[196:199], v[168:171]\n"
"ds_read_b64_tr_b16 v[236:237], v251 offset:17320\n"
"v_accvgpr_read_b32 v245, a242\n"
"v_mfma_f32_16x16x32_bf16 v[168:171], v[228:231], a[212:215], v[168:171]\n"
"ds_read_b64_tr_b16 v[238:239], v251 offset:17576\n"
"v_accvgpr_read_b32 v246, a243\n"
"v_mfma_f32_16x16x32_bf16 v[176:179], v[224:227], a[200:203], v[176:179]\n"
"v_mul_f32_e32 v245, s71, v245\n"
"v_mul_f32_e32 v246, s71, v246\n"
"v_mfma_f32_16x16x32_bf16 v[176:179], v[228:231], a[216:219], v[176:179]\n"
"v_cvt_pk_bf16_f32 v57, v245, v246\n"
"v_accvgpr_read_b32 v243, a244\n"
"v_mfma_f32_16x16x32_bf16 v[184:187], v[224:227], a[204:207], v[184:187]\n"
"buffer_load_dwordx4 v242, s[56:59], s73 offen offset:3120 nt lds\n"
"v_accvgpr_read_b32 v244, a245\n"
"v_mfma_f32_16x16x32_bf16 v[184:187], v[228:231], a[220:223], v[184:187]\n"
"s_waitcnt lgkmcnt(0)\n"
"s_barrier\n"
"v_mfma_f32_16x16x32_bf16 v[188:191], v[232:235], a[204:207], v[188:191]\n"
"ds_read_b128 v[224:227], v240 offset:33280\n"
"v_mul_f32_e32 v243, s71, v243\n"
"v_mfma_f32_16x16x32_bf16 v[188:191], v[236:239], a[220:223], v[188:191]\n"
"ds_read_b128 v[228:231], v240 offset:33312\n"
"v_mul_f32_e32 v244, s71, v244\n"
"v_mfma_f32_16x16x32_bf16 v[180:183], v[232:235], a[200:203], v[180:183]\n"
"v_cvt_pk_bf16_f32 v58, v243, v244\n"
"v_accvgpr_read_b32 v245, a246\n"
"v_mfma_f32_16x16x32_bf16 v[180:183], v[236:239], a[216:219], v[180:183]\n"
"v_accvgpr_read_b32 v246, a247\n"
"v_mul_f32_e32 v245, s71, v245\n"
"v_mfma_f32_16x16x32_bf16 v[172:175], v[232:235], a[196:199], v[172:175]\n"
"s_add_u32 s72, s72, 16416\n"
"v_mul_f32_e32 v246, s71, v246\n"
"v_mfma_f32_16x16x32_bf16 v[172:175], v[236:239], a[212:215], v[172:175]\n"
"s_add_u32 m0, s68, 16640\n"
"v_cvt_pk_bf16_f32 v59, v245, v246\n"
"v_mfma_f32_16x16x32_bf16 v[164:167], v[232:235], a[192:195], v[164:167]\n"
"buffer_load_dwordx4 v253, s[44:47], s72 offen offset:0 nt lds\n"
"v_accvgpr_read_b32 v243, a248\n"
"v_mfma_f32_16x16x32_bf16 v[164:167], v[236:239], a[208:211], v[164:167]\n"
"s_waitcnt lgkmcnt(0)\n"
"v_accvgpr_read_b32 v244, a249\n"
"v_mfma_f32_16x16x32_bf16 v[128:131], v[224:227], v[0:3], 0\n"
"ds_read_b128 v[232:235], v240 offset:33792\n"
"v_mul_f32_e32 v243, s71, v243\n"
"v_mfma_f32_16x16x32_bf16 v[128:131], v[228:231], v[16:19], v[128:131]\n"
"ds_read_b128 v[236:239], v240 offset:33824\n"
"v_mul_f32_e32 v244, s71, v244\n"
"v_mfma_f32_16x16x32_bf16 v[132:135], v[224:227], v[4:7], 0\n"
"v_cvt_pk_bf16_f32 v60, v243, v244\n"
"v_accvgpr_read_b32 v245, a250\n"
"v_mfma_f32_16x16x32_bf16 v[132:135], v[228:231], v[20:23], v[132:135]\n"
"v_accvgpr_read_b32 v246, a251\n"
"v_mul_f32_e32 v245, s71, v245\n"
"v_mfma_f32_16x16x32_bf16 v[136:139], v[224:227], v[8:11], 0\n"
"v_mul_f32_e32 v246, s71, v246\n"
"v_cvt_pk_bf16_f32 v61, v245, v246\n"
"v_mfma_f32_16x16x32_bf16 v[136:139], v[228:231], v[24:27], v[136:139]\n"
"v_accvgpr_read_b32 v243, a252\n"
"v_accvgpr_read_b32 v244, a253\n"
"v_mfma_f32_16x16x32_bf16 v[140:143], v[224:227], v[12:15], 0\n"
"buffer_load_dwordx4 v242, s[44:47], s72 offen offset:1040 nt lds\n"
"v_mfma_f32_16x16x32_bf16 v[140:143], v[228:231], v[28:31], v[140:143]\n"
"s_waitcnt lgkmcnt(0)\n"
"v_mul_f32_e32 v243, s71, v243\n"
"v_mfma_f32_16x16x32_bf16 v[156:159], v[232:235], v[12:15], 0\n"
"ds_read_b128 v[224:227], v240 offset:33408\n"
"v_mul_f32_e32 v244, s71, v244\n"
"v_mfma_f32_16x16x32_bf16 v[156:159], v[236:239], v[28:31], v[156:159]\n"
"ds_read_b128 v[228:231], v240 offset:33440\n"
"v_cvt_pk_bf16_f32 v62, v243, v244\n"
"v_mfma_f32_16x16x32_bf16 v[152:155], v[232:235], v[8:11], 0\n"
"v_accvgpr_read_b32 v245, a254\n"
"v_accvgpr_read_b32 v246, a255\n"
"v_mfma_f32_16x16x32_bf16 v[152:155], v[236:239], v[24:27], v[152:155]\n"
"v_mul_f32_e32 v245, s71, v245\n"
"v_mul_f32_e32 v246, s71, v246\n"
"v_mfma_f32_16x16x32_bf16 v[148:151], v[232:235], v[4:7], 0\n"
"v_cvt_pk_bf16_f32 v63, v245, v246\n"
"v_mul_f32_e32 v64, s71, v64\n"
"v_mfma_f32_16x16x32_bf16 v[148:151], v[236:239], v[20:23], v[148:151]\n"
"s_sub_u32 s72, s72, 32\n"
"v_mul_f32_e32 v65, s71, v65\n"
"v_mfma_f32_16x16x32_bf16 v[144:147], v[232:235], v[0:3], 0\n"
"buffer_load_dwordx4 v253, s[44:47], s72 offen offset:2080 nt lds\n"
"v_mfma_f32_16x16x32_bf16 v[144:147], v[236:239], v[16:19], v[144:147]\n"
"s_waitcnt lgkmcnt(0)\n"
"v_cvt_pk_bf16_f32 v64, v64, v65\n"
"v_mfma_f32_16x16x32_bf16 v[128:131], v[224:227], v[32:35], v[128:131]\n"
"ds_read_b128 v[232:235], v240 offset:33920\n"
"v_mul_f32_e32 v66, s71, v66\n"
"v_mfma_f32_16x16x32_bf16 v[128:131], v[228:231], v[48:51], v[128:131]\n"
"ds_read_b128 v[236:239], v240 offset:33952\n"
"v_mul_f32_e32 v67, s71, v67\n"
"v_mfma_f32_16x16x32_bf16 v[132:135], v[224:227], v[36:39], v[132:135]\n"
"v_cvt_pk_bf16_f32 v65, v66, v67\n"
"v_mul_f32_e32 v68, s71, v68\n"
"v_mfma_f32_16x16x32_bf16 v[132:135], v[228:231], v[52:55], v[132:135]\n"
"v_mul_f32_e32 v69, s71, v69\n"
"v_cvt_pk_bf16_f32 v66, v68, v69\n"
"v_mfma_f32_16x16x32_bf16 v[136:139], v[224:227], v[40:43], v[136:139]\n"
"v_mul_f32_e32 v70, s71, v70\n"
"v_mul_f32_e32 v71, s71, v71\n"
"v_mfma_f32_16x16x32_bf16 v[136:139], v[228:231], v[56:59], v[136:139]\n"
"v_cvt_pk_bf16_f32 v67, v70, v71\n"
"v_mul_f32_e32 v72, s71, v72\n"
"v_mfma_f32_16x16x32_bf16 v[140:143], v[224:227], v[44:47], v[140:143]\n"
"buffer_load_dwordx4 v242, s[44:47], s72 offen offset:3120 nt lds\n"
"v_mfma_f32_16x16x32_bf16 v[140:143], v[228:231], v[60:63], v[140:143]\n"
"s_waitcnt lgkmcnt(0)\n"
"v_mul_f32_e32 v73, s71, v73\n"
"v_mfma_f32_16x16x32_bf16 v[156:159], v[232:235], v[44:47], v[156:159]\n"
"ds_read_b128 v[224:227], v240 offset:33536\n"
"v_cvt_pk_bf16_f32 v68, v72, v73\n"
"v_mfma_f32_16x16x32_bf16 v[156:159], v[236:239], v[60:63], v[156:159]\n"
"ds_read_b128 v[228:231], v240 offset:33568\n"
"v_mul_f32_e32 v74, s71, v74\n"
"v_mfma_f32_16x16x32_bf16 v[152:155], v[232:235], v[40:43], v[152:155]\n"
"v_mul_f32_e32 v75, s71, v75\n"
"v_cvt_pk_bf16_f32 v69, v74, v75\n"
"v_mfma_f32_16x16x32_bf16 v[152:155], v[236:239], v[56:59], v[152:155]\n"
"v_mul_f32_e32 v76, s71, v76\n"
"v_mul_f32_e32 v77, s71, v77\n"
"v_mfma_f32_16x16x32_bf16 v[148:151], v[232:235], v[36:39], v[148:151]\n"
"s_add_u32 s72, s72, 32\n"
"v_cvt_pk_bf16_f32 v70, v76, v77\n"
"v_mfma_f32_16x16x32_bf16 v[148:151], v[236:239], v[52:55], v[148:151]\n"
"s_add_u32 m0, s68, 20800\n"
"v_mul_f32_e32 v78, s71, v78\n"
"v_mfma_f32_16x16x32_bf16 v[144:147], v[232:235], v[32:35], v[144:147]\n"
"buffer_load_dwordx4 v253, s[48:51], s72 offen offset:0 nt lds\n"
"v_mul_f32_e32 v79, s71, v79\n"
"v_pk_mul_f32 v[80:81], v[80:81], s[70:71] op_sel:[0,1] op_sel_hi:[1,1]\n"
"v_pk_mul_f32 v[82:83], v[82:83], s[70:71] op_sel:[0,1] op_sel_hi:[1,1]\n"
"v_pk_mul_f32 v[84:85], v[84:85], s[70:71] op_sel:[0,1] op_sel_hi:[1,1]\n"
"v_cvt_pk_bf16_f32 v71, v78, v79\n"
"v_cvt_pk_bf16_f32 v72, v80, v81\n"
"v_cvt_pk_bf16_f32 v73, v82, v83\n"
"v_cvt_pk_bf16_f32 v74, v84, v85\n"
"v_pk_mul_f32 v[86:87], v[86:87], s[70:71] op_sel:[0,1] op_sel_hi:[1,1]\n"
"v_pk_mul_f32 v[88:89], v[88:89], s[70:71] op_sel:[0,1] op_sel_hi:[1,1]\n"
"v_pk_mul_f32 v[90:91], v[90:91], s[70:71] op_sel:[0,1] op_sel_hi:[1,1]\n"
"v_pk_mul_f32 v[92:93], v[92:93], s[70:71] op_sel:[0,1] op_sel_hi:[1,1]\n"
"v_cvt_pk_bf16_f32 v75, v86, v87\n"
"v_cvt_pk_bf16_f32 v76, v88, v89\n"
"v_cvt_pk_bf16_f32 v77, v90, v91\n"
"v_cvt_pk_bf16_f32 v78, v92, v93\n"
"v_pk_mul_f32 v[94:95], v[94:95], s[70:71] op_sel:[0,1] op_sel_hi:[1,1]\n"
"v_pk_mul_f32 v[192:193], v[192:193], s[70:71] op_sel:[0,1] op_sel_hi:[1,1]\n"
"v_pk_mul_f32 v[194:195], v[194:195], s[70:71] op_sel:[0,1] op_sel_hi:[1,1]\n"
"v_pk_mul_f32 v[196:197], v[196:197], s[70:71] op_sel:[0,1] op_sel_hi:[1,1]\n"
"v_cvt_pk_bf16_f32 v79, v94, v95\n"
"v_cvt_pk_bf16_f32 v80, v192, v193\n"
"v_cvt_pk_bf16_f32 v81, v194, v195\n"
"v_cvt_pk_bf16_f32 v82, v196, v197\n"
"v_pk_mul_f32 v[198:199], v[198:199], s[70:71] op_sel:[0,1] op_sel_hi:[1,1]\n"
"v_pk_mul_f32 v[200:201], v[200:201], s[70:71] op_sel:[0,1] op_sel_hi:[1,1]\n"
"v_pk_mul_f32 v[202:203], v[202:203], s[70:71] op_sel:[0,1] op_sel_hi:[1,1]\n"
"v_pk_mul_f32 v[204:205], v[204:205], s[70:71] op_sel:[0,1] op_sel_hi:[1,1]\n"
"v_cvt_pk_bf16_f32 v83, v198, v199\n"
"v_cvt_pk_bf16_f32 v84, v200, v201\n"
"v_cvt_pk_bf16_f32 v85, v202, v203\n"
"v_cvt_pk_bf16_f32 v86, v204, v205\n"
"v_pk_mul_f32 v[206:207], v[206:207], s[70:71] op_sel:[0,1] op_sel_hi:[1,1]\n"
"v_pk_mul_f32 v[208:209], v[208:209], s[70:71] op_sel:[0,1] op_sel_hi:[1,1]\n"
"v_pk_mul_f32 v[210:211], v[210:211], s[70:71] op_sel:[0,1] op_sel_hi:[1,1]\n"
"v_pk_mul_f32 v[212:213], v[212:213], s[70:71] op_sel:[0,1] op_sel_hi:[1,1]\n"
"v_cvt_pk_bf16_f32 v87, v206, v207\n"
"v_cvt_pk_bf16_f32 v88, v208, v209\n"
"v_cvt_pk_bf16_f32 v89, v210, v211\n"
"v_cvt_pk_bf16_f32 v90, v212, v213\n"
"v_pk_mul_f32 v[214:215], v[214:215], s[70:71] op_sel:[0,1] op_sel_hi:[1,1]\n"
"v_pk_mul_f32 v[216:217], v[216:217], s[70:71] op_sel:[0,1] op_sel_hi:[1,1]\n"
"v_pk_mul_f32 v[218:219], v[218:219], s[70:71] op_sel:[0,1] op_sel_hi:[1,1]\n"
"v_pk_mul_f32 v[220:221], v[220:221], s[70:71] op_sel:[0,1] op_sel_hi:[1,1]\n"
"v_cvt_pk_bf16_f32 v91, v214, v215\n"
"v_cvt_pk_bf16_f32 v92, v216, v217\n"
"v_cvt_pk_bf16_f32 v93, v218, v219\n"
"v_cvt_pk_bf16_f32 v94, v220, v221\n"
"v_pk_mul_f32 v[222:223], v[222:223], s[70:71] op_sel:[0,1] op_sel_hi:[1,1]\n"
"v_cvt_pk_bf16_f32 v95, v222, v223\n"
"v_mfma_f32_16x16x32_bf16 v[144:147], v[236:239], v[48:51], v[144:147]\n"
"s_waitcnt lgkmcnt(0)\n"
"v_mul_f32_e32 v96, s71, v96\n"
"v_mfma_f32_16x16x32_bf16 v[128:131], v[224:227], v[64:67], v[128:131]\n"
"ds_read_b128 v[232:235], v240 offset:34048\n"
"v_mul_f32_e32 v97, s71, v97\n"
"v_mfma_f32_16x16x32_bf16 v[128:131], v[228:231], v[80:83], v[128:131]\n"
"ds_read_b128 v[236:239], v240 offset:34080\n"
"v_cvt_pk_bf16_f32 v96, v96, v97\n"
"v_mfma_f32_16x16x32_bf16 v[132:135], v[224:227], v[68:71], v[132:135]\n"
"v_mul_f32_e32 v98, s71, v98\n"
"v_mul_f32_e32 v99, s71, v99\n"
"v_mfma_f32_16x16x32_bf16 v[132:135], v[228:231], v[84:87], v[132:135]\n"
"v_cvt_pk_bf16_f32 v97, v98, v99\n"
"v_mul_f32_e32 v100, s71, v100\n"
"v_mfma_f32_16x16x32_bf16 v[136:139], v[224:227], v[72:75], v[136:139]\n"
"v_mul_f32_e32 v101, s71, v101\n"
"v_cvt_pk_bf16_f32 v98, v100, v101\n"
"v_mfma_f32_16x16x32_bf16 v[136:139], v[228:231], v[88:91], v[136:139]\n"
"v_mul_f32_e32 v102, s71, v102\n"
"v_mul_f32_e32 v103, s71, v103\n"
"v_mfma_f32_16x16x32_bf16 v[140:143], v[224:227], v[76:79], v[140:143]\n"
"buffer_load_dwordx4 v242, s[48:51], s72 offen offset:1040 nt lds\n"
"v_mfma_f32_16x16x32_bf16 v[140:143], v[228:231], v[92:95], v[140:143]\n"
"s_waitcnt lgkmcnt(0)\n"
"v_cvt_pk_bf16_f32 v99, v102, v103\n"
"v_mfma_f32_16x16x32_bf16 v[156:159], v[232:235], v[76:79], v[156:159]\n"
"ds_read_b128 v[224:227], v240 offset:33664\n"
"v_mul_f32_e32 v104, s71, v104\n"
"v_mfma_f32_16x16x32_bf16 v[156:159], v[236:239], v[92:95], v[156:159]\n"
"ds_read_b128 v[228:231], v240 offset:33696\n"
"v_mul_f32_e32 v105, s71, v105\n"
"v_mfma_f32_16x16x32_bf16 v[152:155], v[232:235], v[72:75], v[152:155]\n"
"v_cvt_pk_bf16_f32 v100, v104, v105\n"
"v_mul_f32_e32 v106, s71, v106\n"
"v_mfma_f32_16x16x32_bf16 v[152:155], v[236:239], v[88:91], v[152:155]\n"
"v_mul_f32_e32 v107, s71, v107\n"
"v_cvt_pk_bf16_f32 v101, v106, v107\n"
"v_mfma_f32_16x16x32_bf16 v[148:151], v[232:235], v[68:71], v[148:151]\n"
"v_mul_f32_e32 v108, s71, v108\n"
"v_mul_f32_e32 v109, s71, v109\n"
"v_mfma_f32_16x16x32_bf16 v[148:151], v[236:239], v[84:87], v[148:151]\n"
"s_sub_u32 s72, s72, 32\n"
"v_cvt_pk_bf16_f32 v102, v108, v109\n"
"v_mfma_f32_16x16x32_bf16 v[144:147], v[232:235], v[64:67], v[144:147]\n"
"buffer_load_dwordx4 v253, s[48:51], s72 offen offset:2080 nt lds\n"
"v_mul_f32_e32 v110, s71, v110\n"
"v_mul_f32_e32 v111, s71, v111\n"
"v_pk_mul_f32 v[112:113], v[112:113], s[70:71] op_sel:[0,1] op_sel_hi:[1,1]\n"
"v_pk_mul_f32 v[114:115], v[114:115], s[70:71] op_sel:[0,1] op_sel_hi:[1,1]\n"
"v_pk_mul_f32 v[116:117], v[116:117], s[70:71] op_sel:[0,1] op_sel_hi:[1,1]\n"
"v_cvt_pk_bf16_f32 v103, v110, v111\n"
"v_cvt_pk_bf16_f32 v104, v112, v113\n"
"v_cvt_pk_bf16_f32 v105, v114, v115\n"
"v_cvt_pk_bf16_f32 v106, v116, v117\n"
"v_pk_mul_f32 v[118:119], v[118:119], s[70:71] op_sel:[0,1] op_sel_hi:[1,1]\n"
"v_pk_mul_f32 v[120:121], v[120:121], s[70:71] op_sel:[0,1] op_sel_hi:[1,1]\n"
"v_pk_mul_f32 v[122:123], v[122:123], s[70:71] op_sel:[0,1] op_sel_hi:[1,1]\n"
"v_pk_mul_f32 v[124:125], v[124:125], s[70:71] op_sel:[0,1] op_sel_hi:[1,1]\n"
"v_cvt_pk_bf16_f32 v107, v118, v119\n"
"v_cvt_pk_bf16_f32 v108, v120, v121\n"
"v_cvt_pk_bf16_f32 v109, v122, v123\n"
"v_cvt_pk_bf16_f32 v110, v124, v125\n"
"v_pk_mul_f32 v[126:127], v[126:127], s[70:71] op_sel:[0,1] op_sel_hi:[1,1]\n"
"v_pk_mul_f32 v[160:161], v[160:161], s[70:71] op_sel:[0,1] op_sel_hi:[1,1]\n"
"v_pk_mul_f32 v[162:163], v[162:163], s[70:71] op_sel:[0,1] op_sel_hi:[1,1]\n"
"v_pk_mul_f32 v[164:165], v[164:165], s[70:71] op_sel:[0,1] op_sel_hi:[1,1]\n"
"v_cvt_pk_bf16_f32 v111, v126, v127\n"
"v_cvt_pk_bf16_f32 v112, v160, v161\n"
"v_cvt_pk_bf16_f32 v113, v162, v163\n"
"v_cvt_pk_bf16_f32 v114, v164, v165\n"
"v_pk_mul_f32 v[166:167], v[166:167], s[70:71] op_sel:[0,1] op_sel_hi:[1,1]\n"
"v_pk_mul_f32 v[168:169], v[168:169], s[70:71] op_sel:[0,1] op_sel_hi:[1,1]\n"
"v_pk_mul_f32 v[170:171], v[170:171], s[70:71] op_sel:[0,1] op_sel_hi:[1,1]\n"
"v_pk_mul_f32 v[172:173], v[172:173], s[70:71] op_sel:[0,1] op_sel_hi:[1,1]\n"
"v_cvt_pk_bf16_f32 v115, v166, v167\n"
"v_cvt_pk_bf16_f32 v116, v168, v169\n"
"v_cvt_pk_bf16_f32 v117, v170, v171\n"
"v_cvt_pk_bf16_f32 v118, v172, v173\n"
"v_pk_mul_f32 v[174:175], v[174:175], s[70:71] op_sel:[0,1] op_sel_hi:[1,1]\n"
"v_pk_mul_f32 v[176:177], v[176:177], s[70:71] op_sel:[0,1] op_sel_hi:[1,1]\n"
"v_pk_mul_f32 v[178:179], v[178:179], s[70:71] op_sel:[0,1] op_sel_hi:[1,1]\n"
"v_pk_mul_f32 v[180:181], v[180:181], s[70:71] op_sel:[0,1] op_sel_hi:[1,1]\n"
"v_cvt_pk_bf16_f32 v119, v174, v175\n"
"v_cvt_pk_bf16_f32 v120, v176, v177\n"
"v_cvt_pk_bf16_f32 v121, v178, v179\n"
"v_cvt_pk_bf16_f32 v122, v180, v181\n"
"v_pk_mul_f32 v[182:183], v[182:183], s[70:71] op_sel:[0,1] op_sel_hi:[1,1]\n"
"v_pk_mul_f32 v[184:185], v[184:185], s[70:71] op_sel:[0,1] op_sel_hi:[1,1]\n"
"v_pk_mul_f32 v[186:187], v[186:187], s[70:71] op_sel:[0,1] op_sel_hi:[1,1]\n"
"v_pk_mul_f32 v[188:189], v[188:189], s[70:71] op_sel:[0,1] op_sel_hi:[1,1]\n"
"v_cvt_pk_bf16_f32 v123, v182, v183\n"
"v_cvt_pk_bf16_f32 v124, v184, v185\n"
"v_cvt_pk_bf16_f32 v125, v186, v187\n"
"v_cvt_pk_bf16_f32 v126, v188, v189\n"
"v_pk_mul_f32 v[190:191], v[190:191], s[70:71] op_sel:[0,1] op_sel_hi:[1,1]\n"
"v_cvt_pk_bf16_f32 v127, v190, v191\n"
"v_mfma_f32_16x16x32_bf16 v[144:147], v[236:239], v[80:83], v[144:147]\n"
"s_waitcnt lgkmcnt(0)\n"
"v_mfma_f32_16x16x32_bf16 v[128:131], v[224:227], v[96:99], v[128:131]\n"
"ds_read_b128 v[232:235], v240 offset:34176\n"
"v_mfma_f32_16x16x32_bf16 v[128:131], v[228:231], v[112:115], v[128:131]\n"
"ds_read_b128 v[236:239], v240 offset:34208\n"
"v_mfma_f32_16x16x32_bf16 v[132:135], v[224:227], v[100:103], v[132:135]\n"
"v_mfma_f32_16x16x32_bf16 v[132:135], v[228:231], v[116:119], v[132:135]\n"
"v_mfma_f32_16x16x32_bf16 v[136:139], v[224:227], v[104:107], v[136:139]\n"
"v_mfma_f32_16x16x32_bf16 v[136:139], v[228:231], v[120:123], v[136:139]\n"
"v_mfma_f32_16x16x32_bf16 v[140:143], v[224:227], v[108:111], v[140:143]\n"
"buffer_load_dwordx4 v242, s[48:51], s72 offen offset:3120 nt lds\n"
"v_mfma_f32_16x16x32_bf16 v[140:143], v[228:231], v[124:127], v[140:143]\n"
"s_waitcnt vmcnt(16) lgkmcnt(0)\n"
"s_barrier\n"
"v_mfma_f32_16x16x32_bf16 v[156:159], v[232:235], v[108:111], v[156:159]\n"
"ds_read_b128 v[224:227], v240 offset:49920\n"
"v_mfma_f32_16x16x32_bf16 v[156:159], v[236:239], v[124:127], v[156:159]\n"
"ds_read_b128 v[228:231], v240 offset:49952\n"
"v_mfma_f32_16x16x32_bf16 v[152:155], v[232:235], v[104:107], v[152:155]\n"
"v_mfma_f32_16x16x32_bf16 v[152:155], v[236:239], v[120:123], v[152:155]\n"
"v_mfma_f32_16x16x32_bf16 v[148:151], v[232:235], v[100:103], v[148:151]\n"
"s_add_u32 s73, s73, 16416\n"
"v_mfma_f32_16x16x32_bf16 v[148:151], v[236:239], v[116:119], v[148:151]\n"
"s_add_u32 m0, s68, 33280\n"
"v_mfma_f32_16x16x32_bf16 v[144:147], v[232:235], v[96:99], v[144:147]\n"
"buffer_load_dwordx4 v253, s[52:55], s73 offen offset:0 nt lds\n"
"v_mfma_f32_16x16x32_bf16 v[144:147], v[236:239], v[112:115], v[144:147]\n"
"s_waitcnt lgkmcnt(0)\n"
"v_max3_f32 v208, v128, v129, v130\n"
"v_max3_f32 v212, v132, v133, v134\n"
"v_max3_f32 v216, v136, v137, v138\n"
"v_max3_f32 v220, v140, v141, v142\n"
"s_nop 2\n"
"v_max3_f32 v208, v208, v131, v144\n"
"v_max3_f32 v212, v212, v135, v148\n"
"v_max3_f32 v216, v216, v139, v152\n"
"v_max3_f32 v220, v220, v143, v156\n"
"v_max3_f32 v208, v208, v145, v146\n"
"v_max3_f32 v212, v212, v149, v150\n"
"v_max3_f32 v216, v216, v153, v154\n"
"v_max3_f32 v220, v220, v157, v158\n"
"v_max_f32_e32 v208, v208, v147\n"
"v_max_f32_e32 v212, v212, v151\n"
"v_max_f32_e32 v216, v216, v155\n"
"v_max_f32_e32 v220, v220, v159\n"
"v_mov_b32_e32 v209, v208\n"
"v_mov_b32_e32 v213, v212\n"
"v_mov_b32_e32 v217, v216\n"
"v_mov_b32_e32 v221, v220\n"
"v_permlane32_swap_b32_e32 v208, v209\n"
"v_permlane32_swap_b32_e32 v212, v213\n"
"v_permlane32_swap_b32_e32 v216, v217\n"
"v_permlane32_swap_b32_e32 v220, v221\n"
"v_max_f32_e32 v208, v208, v209\n"
"v_max_f32_e32 v212, v212, v213\n"
"v_max_f32_e32 v216, v216, v217\n"
"v_max_f32_e32 v220, v220, v221\n"
"v_mov_b32_e32 v209, v208\n"
"v_mov_b32_e32 v213, v212\n"
"v_mov_b32_e32 v217, v216\n"
"v_mov_b32_e32 v221, v220\n"
"v_permlane16_swap_b32_e32 v208, v209\n"
"v_permlane16_swap_b32_e32 v212, v213\n"
"v_permlane16_swap_b32_e32 v216, v217\n"
"v_permlane16_swap_b32_e32 v220, v221\n"
"v_max_f32_e32 v208, v208, v209\n"
"v_max_f32_e32 v212, v212, v213\n"
"v_max_f32_e32 v216, v216, v217\n"
"v_max_f32_e32 v220, v220, v221\n"
"v_sub_f32_e32 v209, 0, v208\n"
"v_add_f32_e32 v212, s96, v212\n"
"v_sub_f32_e32 v210, 0, v208\n"
"v_add_f32_e32 v220, s97, v220\n"
"v_sub_f32_e32 v211, 0, v208\n"
"v_sub_f32_e32 v208, 0, v208\n"
"v_sub_f32_e32 v213, 0, v212\n"
"v_sub_f32_e32 v214, 0, v212\n"
"v_sub_f32_e32 v215, 0, v212\n"
"v_sub_f32_e32 v212, 0, v212\n"
"v_sub_f32_e32 v217, 0, v216\n"
"v_sub_f32_e32 v218, 0, v216\n"
"v_sub_f32_e32 v219, 0, v216\n"
"v_sub_f32_e32 v216, 0, v216\n"
"v_sub_f32_e32 v221, 0, v220\n"
"v_sub_f32_e32 v222, 0, v220\n"
"v_sub_f32_e32 v223, 0, v220\n"
"v_sub_f32_e32 v220, 0, v220\n"
"v_add_f32_e32 v128, v208, v128\n"
"v_add_f32_e32 v129, v208, v129\n"
"v_add_f32_e32 v130, v208, v130\n"
"v_add_f32_e32 v131, v208, v131\n"
"v_add_f32_e32 v132, v212, v132\n"
"v_add_f32_e32 v133, v212, v133\n"
"v_add_f32_e32 v134, v212, v134\n"
"v_add_f32_e32 v135, v212, v135\n"
"v_add_f32_e32 v136, v216, v136\n"
"v_add_f32_e32 v137, v216, v137\n"
"v_add_f32_e32 v138, v216, v138\n"
"v_add_f32_e32 v139, v216, v139\n"
"v_add_f32_e32 v140, v220, v140\n"
"v_add_f32_e32 v141, v220, v141\n"
"v_add_f32_e32 v142, v220, v142\n"
"v_add_f32_e32 v143, v220, v143\n"
"v_add_f32_e32 v144, v208, v144\n"
"v_add_f32_e32 v145, v208, v145\n"
"v_add_f32_e32 v146, v208, v146\n"
"v_add_f32_e32 v147, v208, v147\n"
"v_add_f32_e32 v148, v212, v148\n"
"v_add_f32_e32 v149, v212, v149\n"
"v_add_f32_e32 v150, v212, v150\n"
"v_add_f32_e32 v151, v212, v151\n"
"v_add_f32_e32 v152, v216, v152\n"
"v_add_f32_e32 v153, v216, v153\n"
"v_add_f32_e32 v154, v216, v154\n"
"v_add_f32_e32 v155, v216, v155\n"
"v_add_f32_e32 v156, v220, v156\n"
"v_add_f32_e32 v157, v220, v157\n"
"v_add_f32_e32 v158, v220, v158\n"
"v_add_f32_e32 v159, v220, v159\n"
"v_lshrrev_b32_e32 v251, 2, v242\n"
"s_lshl_b32 s77, s83, 10\n"
"v_exp_f32_e32 v128, v128\n"
"v_exp_f32_e32 v129, v129\n"
"v_exp_f32_e32 v130, v130\n"
"s_add_u32 s77, s77, %3\n"
"v_exp_f32_e32 v131, v131\n"
"v_add_f32_e32 v243, v128, v129\n"
"v_exp_f32_e32 v132, v132\n"
"v_add_u32_e32 v251, s77, v251\n"
"v_exp_f32_e32 v133, v133\n"
"v_add_f32_e32 v243, v130, v243\n"
"v_exp_f32_e32 v134, v134\n"
"v_add_u32_e32 v251, 149504, v251\n"
"v_add_f32_e32 v244, v132, v133\n"
"v_add_f32_e32 v243, v131, v243\n"
"v_exp_f32_e32 v135, v135\n"
"v_exp_f32_e32 v136, v136\n"
"v_add_f32_e32 v244, v134, v244\n"
"v_exp_f32_e32 v137, v137\n"
"v_exp_f32_e32 v138, v138\n"
"v_exp_f32_e32 v139, v139\n"
"v_add_f32_e32 v244, v135, v244\n"
"v_add_f32_e32 v245, v136, v137\n"
"v_exp_f32_e32 v140, v140\n"
"v_exp_f32_e32 v141, v141\n"
"v_exp_f32_e32 v142, v142\n"
"v_add_f32_e32 v245, v138, v245\n"
"v_exp_f32_e32 v143, v143\n"
"v_add_f32_e32 v246, v140, v141\n"
"v_exp_f32_e32 v144, v144\n"
"v_add_f32_e32 v245, v139, v245\n"
"v_add_f32_e32 v246, v142, v246\n"
"v_add_f32_e32 v243, v144, v243\n"
"v_add_f32_e32 v246, v143, v246\n"
"v_mfma_f32_16x16x32_bf16 v[160:163], v[224:227], v[0:3], v[208:211]\n"
"ds_read_b128 v[232:235], v240 offset:50048\n"
"v_cvt_pk_bf16_f32 v192, v128, v129\n"
"v_mfma_f32_16x16x32_bf16 v[160:163], v[228:231], v[16:19], v[160:163]\n"
"ds_read_b128 v[236:239], v240 offset:50080\n"
"v_cvt_pk_bf16_f32 v193, v130, v131\n"
"v_mfma_f32_16x16x32_bf16 v[164:167], v[224:227], v[4:7], v[212:215]\n"
"v_exp_f32_e32 v145, v145\n"
"v_mfma_f32_16x16x32_bf16 v[164:167], v[228:231], v[20:23], v[164:167]\n"
"v_exp_f32_e32 v146, v146\n"
"v_mfma_f32_16x16x32_bf16 v[168:171], v[224:227], v[8:11], v[216:219]\n"
"v_exp_f32_e32 v147, v147\n"
"v_mfma_f32_16x16x32_bf16 v[168:171], v[228:231], v[24:27], v[168:171]\n"
"v_exp_f32_e32 v148, v148\n"
"v_mfma_f32_16x16x32_bf16 v[172:175], v[224:227], v[12:15], v[220:223]\n"
"buffer_load_dwordx4 v242, s[52:55], s73 offen offset:1040 nt lds\n"
"v_mfma_f32_16x16x32_bf16 v[172:175], v[228:231], v[28:31], v[172:175]\n"
"s_waitcnt lgkmcnt(0)\n"
"v_add_f32_e32 v243, v145, v243\n"
"v_mfma_f32_16x16x32_bf16 v[172:175], v[232:235], v[44:47], v[172:175]\n"
"ds_read_b128 v[224:227], v240 offset:50176\n"
"v_add_f32_e32 v243, v146, v243\n"
"v_mfma_f32_16x16x32_bf16 v[172:175], v[236:239], v[60:63], v[172:175]\n"
"ds_read_b128 v[228:231], v240 offset:50208\n"
"v_add_f32_e32 v243, v147, v243\n"
"v_mfma_f32_16x16x32_bf16 v[168:171], v[232:235], v[40:43], v[168:171]\n"
"v_exp_f32_e32 v149, v149\n"
"v_mfma_f32_16x16x32_bf16 v[168:171], v[236:239], v[56:59], v[168:171]\n"
"v_exp_f32_e32 v150, v150\n"
"v_mfma_f32_16x16x32_bf16 v[164:167], v[232:235], v[36:39], v[164:167]\n"
"v_exp_f32_e32 v151, v151\n"
"v_mfma_f32_16x16x32_bf16 v[164:167], v[236:239], v[52:55], v[164:167]\n"
"s_sub_u32 s73, s73, 32\n"
"v_add_f32_e32 v244, v148, v244\n"
"v_mfma_f32_16x16x32_bf16 v[160:163], v[232:235], v[32:35], v[160:163]\n"
"buffer_load_dwordx4 v253, s[52:55], s73 offen offset:2080 nt lds\n"
"v_mfma_f32_16x16x32_bf16 v[160:163], v[236:239], v[48:51], v[160:163]\n"
"s_waitcnt lgkmcnt(0)\n"
"v_add_f32_e32 v244, v149, v244\n"
"v_mfma_f32_16x16x32_bf16 v[160:163], v[224:227], v[64:67], v[160:163]\n"
"ds_read_b128 v[232:235], v240 offset:50304\n"
"v_add_f32_e32 v244, v150, v244\n"
"v_mfma_f32_16x16x32_bf16 v[160:163], v[228:231], v[80:83], v[160:163]\n"
"ds_read_b128 v[236:239], v240 offset:50336\n"
"v_add_f32_e32 v244, v151, v244\n"
"v_mfma_f32_16x16x32_bf16 v[164:167], v[224:227], v[68:71], v[164:167]\n"
"v_exp_f32_e32 v152, v152\n"
"v_mfma_f32_16x16x32_bf16 v[164:167], v[228:231], v[84:87], v[164:167]\n"
"v_exp_f32_e32 v153, v153\n"
"v_mfma_f32_16x16x32_bf16 v[168:171], v[224:227], v[72:75], v[168:171]\n"
"v_exp_f32_e32 v154, v154\n"
"v_mfma_f32_16x16x32_bf16 v[168:171], v[228:231], v[88:91], v[168:171]\n"
"v_exp_f32_e32 v155, v155\n"
"v_mfma_f32_16x16x32_bf16 v[172:175], v[224:227], v[76:79], v[172:175]\n"
"buffer_load_dwordx4 v242, s[52:55], s73 offen offset:3120 nt lds\n"
"v_mfma_f32_16x16x32_bf16 v[172:175], v[228:231], v[92:95], v[172:175]\n"
"s_waitcnt lgkmcnt(0)\n"
"v_add_f32_e32 v245, v152, v245\n"
"v_mfma_f32_16x16x32_bf16 v[172:175], v[232:235], v[108:111], v[172:175]\n"
"ds_read_b128 v[224:227], v240 offset:50432\n"
"v_add_f32_e32 v245, v153, v245\n"
"v_mfma_f32_16x16x32_bf16 v[172:175], v[236:239], v[124:127], v[172:175]\n"
"ds_read_b128 v[228:231], v240 offset:50464\n"
"v_add_f32_e32 v245, v154, v245\n"
"v_mfma_f32_16x16x32_bf16 v[168:171], v[232:235], v[104:107], v[168:171]\n"
"v_exp_f32_e32 v156, v156\n"
"v_mfma_f32_16x16x32_bf16 v[168:171], v[236:239], v[120:123], v[168:171]\n"
"v_exp_f32_e32 v157, v157\n"
"v_mfma_f32_16x16x32_bf16 v[164:167], v[232:235], v[100:103], v[164:167]\n"
"s_add_u32 s73, s73, 32\n"
"v_add_f32_e32 v245, v155, v245\n"
"v_mfma_f32_16x16x32_bf16 v[164:167], v[236:239], v[116:119], v[164:167]\n"
"s_add_u32 m0, s68, 37440\n"
"v_add_f32_e32 v246, v156, v246\n"
"v_mfma_f32_16x16x32_bf16 v[160:163], v[232:235], v[96:99], v[160:163]\n"
"buffer_load_dwordx4 v253, s[56:59], s73 offen offset:0 nt lds\n"
"v_mfma_f32_16x16x32_bf16 v[160:163], v[236:239], v[112:115], v[160:163]\n"
"s_waitcnt lgkmcnt(0)\n"
"v_add_f32_e32 v246, v157, v246\n"
"v_mfma_f32_16x16x32_bf16 v[176:179], v[224:227], v[0:3], v[208:211]\n"
"ds_read_b128 v[232:235], v240 offset:50560\n"
"s_branch .Lpo5\n"
".Lpg5:\n"
"s_setpc_b64 s[78:79]\n"
".Lpo5:\n"
"v_cvt_pk_bf16_f32 v196, v132, v133\n"
"v_mfma_f32_16x16x32_bf16 v[176:179], v[228:231], v[16:19], v[176:179]\n"
"ds_read_b128 v[236:239], v240 offset:50592\n"
"v_cvt_pk_bf16_f32 v197, v134, v135\n"
"v_mfma_f32_16x16x32_bf16 v[180:183], v[224:227], v[4:7], v[212:215]\n"
"v_exp_f32_e32 v158, v158\n"
"v_mfma_f32_16x16x32_bf16 v[180:183], v[228:231], v[20:23], v[180:183]\n"
"v_exp_f32_e32 v159, v159\n"
"v_mfma_f32_16x16x32_bf16 v[184:187], v[224:227], v[8:11], v[216:219]\n"
"v_add_f32_e32 v246, v158, v246\n"
"v_add_f32_e32 v246, v159, v246\n"
"v_mfma_f32_16x16x32_bf16 v[184:187], v[228:231], v[24:27], v[184:187]\n"
"v_cvt_pk_bf16_f32 v200, v136, v137\n"
"v_cvt_pk_bf16_f32 v201, v138, v139\n"
"v_mfma_f32_16x16x32_bf16 v[188:191], v[224:227], v[12:15], v[220:223]\n"
"buffer_load_dwordx4 v242, s[56:59], s73 offen offset:1040 nt lds\n"
"v_mfma_f32_16x16x32_bf16 v[188:191], v[228:231], v[28:31], v[188:191]\n"
"s_waitcnt lgkmcnt(0)\n"
"v_cvt_pk_bf16_f32 v204, v140, v141\n"
"v_mfma_f32_16x16x32_bf16 v[188:191], v[232:235], v[44:47], v[188:191]\n"
"ds_read_b128 v[224:227], v240 offset:50688\n"
"v_cvt_pk_bf16_f32 v205, v142, v143\n"
"v_mfma_f32_16x16x32_bf16 v[188:191], v[236:239], v[60:63], v[188:191]\n"
"ds_read_b128 v[228:231], v240 offset:50720\n"
"v_cvt_pk_bf16_f32 v194, v144, v145\n"
"v_mfma_f32_16x16x32_bf16 v[184:187], v[232:235], v[40:43], v[184:187]\n"
"v_cvt_pk_bf16_f32 v195, v146, v147\n"
"v_cvt_pk_bf16_f32 v198, v148, v149\n"
"v_mfma_f32_16x16x32_bf16 v[184:187], v[236:239], v[56:59], v[184:187]\n"
"v_cvt_pk_bf16_f32 v199, v150, v151\n"
"v_cvt_pk_bf16_f32 v202, v152, v153\n"
"v_mfma_f32_16x16x32_bf16 v[180:183], v[232:235], v[36:39], v[180:183]\n"
"v_cvt_pk_bf16_f32 v203, v154, v155\n"
"v_cvt_pk_bf16_f32 v206, v156, v157\n"
"v_mfma_f32_16x16x32_bf16 v[180:183], v[236:239], v[52:55], v[180:183]\n"
"s_sub_u32 s73, s73, 32\n"
"v_cvt_pk_bf16_f32 v207, v158, v159\n"
"v_mfma_f32_16x16x32_bf16 v[176:179], v[232:235], v[32:35], v[176:179]\n"
"buffer_load_dwordx4 v253, s[56:59], s73 offen offset:2080 nt lds\n"
"v_mfma_f32_16x16x32_bf16 v[176:179], v[236:239], v[48:51], v[176:179]\n"
"s_waitcnt lgkmcnt(0)\n"
"v_mfma_f32_16x16x32_bf16 v[176:179], v[224:227], v[64:67], v[176:179]\n"
"ds_read_b128 v[232:235], v240 offset:50816\n"
"v_mfma_f32_16x16x32_bf16 v[176:179], v[228:231], v[80:83], v[176:179]\n"
"ds_read_b128 v[236:239], v240 offset:50848\n"
"v_mfma_f32_16x16x32_bf16 v[180:183], v[224:227], v[68:71], v[180:183]\n"
"v_mfma_f32_16x16x32_bf16 v[180:183], v[228:231], v[84:87], v[180:183]\n"
"v_mfma_f32_16x16x32_bf16 v[184:187], v[224:227], v[72:75], v[184:187]\n"
"v_mfma_f32_16x16x32_bf16 v[184:187], v[228:231], v[88:91], v[184:187]\n"
"v_mfma_f32_16x16x32_bf16 v[188:191], v[224:227], v[76:79], v[188:191]\n"
"buffer_load_dwordx4 v242, s[56:59], s73 offen offset:3120 nt lds\n"
"v_mfma_f32_16x16x32_bf16 v[188:191], v[228:231], v[92:95], v[188:191]\n"
"s_waitcnt vmcnt(16) lgkmcnt(0)\n"
"s_barrier\n"
"v_mfma_f32_16x16x32_bf16 v[188:191], v[232:235], v[108:111], v[188:191]\n"
"ds_read_b64_tr_b16 v[224:225], v241 offset:0\n"
"v_mfma_f32_16x16x32_bf16 v[188:191], v[236:239], v[124:127], v[188:191]\n"
"ds_read_b64_tr_b16 v[226:227], v241 offset:512\n"
"v_mfma_f32_16x16x32_bf16 v[184:187], v[232:235], v[104:107], v[184:187]\n"
"ds_read_b64_tr_b16 v[228:229], v241 offset:32\n"
"v_mfma_f32_16x16x32_bf16 v[184:187], v[236:239], v[120:123], v[184:187]\n"
"ds_read_b64_tr_b16 v[230:231], v241 offset:544\n"
"v_mfma_f32_16x16x32_bf16 v[180:183], v[232:235], v[100:103], v[180:183]\n"
"s_add_u32 s72, s72, 16416\n"
"v_mfma_f32_16x16x32_bf16 v[180:183], v[236:239], v[116:119], v[180:183]\n"
"s_add_u32 m0, s68, 49920\n"
"v_mfma_f32_16x16x32_bf16 v[176:179], v[232:235], v[96:99], v[176:179]\n"
"buffer_load_dwordx4 v253, s[44:47], s72 offen offset:0 nt lds\n"
"v_mfma_f32_16x16x32_bf16 v[176:179], v[236:239], v[112:115], v[176:179]\n"
"s_waitcnt lgkmcnt(2)\n"
"v_mfma_f32_16x16x32_bf16 a[0:3], v[224:227], v[192:195], 0\n"
"ds_read_b64_tr_b16 v[232:233], v241 offset:64\n"
"v_max3_f32 v247, v160, v161, v162\n"
"v_mfma_f32_16x16x32_bf16 a[4:7], v[224:227], v[196:199], 0\n"
"ds_read_b64_tr_b16 v[234:235], v241 offset:576\n"
"s_waitcnt lgkmcnt(2)\n"
"v_mfma_f32_16x16x32_bf16 a[8:11], v[224:227], v[200:203], 0\n"
"ds_read_b64_tr_b16 v[236:237], v241 offset:96\n"
"v_max3_f32 v247, v247, v163, v164\n"
"v_mfma_f32_16x16x32_bf16 a[12:15], v[224:227], v[204:207], 0\n"
"ds_read_b64_tr_b16 v[238:239], v241 offset:608\n"
"v_max3_f32 v247, v247, v165, v166\n"
"v_mfma_f32_16x16x32_bf16 a[16:19], v[228:231], v[192:195], 0\n"
"ds_read_b64_tr_b16 v[224:225], v241 offset:128\n"
"v_max3_f32 v247, v247, v167, v168\n"
"v_mfma_f32_16x16x32_bf16 a[20:23], v[228:231], v[196:199], 0\n"
"ds_read_b64_tr_b16 v[226:227], v241 offset:640\n"
"v_max3_f32 v247, v247, v169, v170\n"
"v_mfma_f32_16x16x32_bf16 a[24:27], v[228:231], v[200:203], 0\n"
"buffer_load_dwordx4 v242, s[44:47], s72 offen offset:1040 nt lds\n"
"s_waitcnt lgkmcnt(4)\n"
"v_mfma_f32_16x16x32_bf16 a[28:31], v[228:231], v[204:207], 0\n"
"v_max3_f32 v247, v247, v171, v172\n"
"v_max3_f32 v247, v247, v173, v174\n"
"v_mfma_f32_16x16x32_bf16 a[32:35], v[232:235], v[192:195], 0\n"
"ds_read_b64_tr_b16 v[228:229], v241 offset:160\n"
"v_max3_f32 v247, v247, v175, v176\n"
"v_mfma_f32_16x16x32_bf16 a[36:39], v[232:235], v[196:199], 0\n"
"ds_read_b64_tr_b16 v[230:231], v241 offset:672\n"
"s_waitcnt lgkmcnt(4)\n"
"v_mfma_f32_16x16x32_bf16 a[40:43], v[232:235], v[200:203], 0\n"
"v_max3_f32 v247, v247, v177, v178\n"
"v_max3_f32 v247, v247, v179, v180\n"
"v_mfma_f32_16x16x32_bf16 a[44:47], v[232:235], v[204:207], 0\n"
"v_max3_f32 v247, v247, v181, v182\n"
"v_max3_f32 v247, v247, v183, v184\n"
"v_mfma_f32_16x16x32_bf16 a[48:51], v[236:239], v[192:195], 0\n"
"ds_read_b64_tr_b16 v[232:233], v241 offset:192\n"
"s_waitcnt lgkmcnt(3)\n"
"v_mfma_f32_16x16x32_bf16 a[52:55], v[236:239], v[196:199], 0\n"
"ds_read_b64_tr_b16 v[234:235], v241 offset:704\n"
"s_sub_u32 s72, s72, 32\n"
"v_mfma_f32_16x16x32_bf16 a[56:59], v[236:239], v[200:203], 0\n"
"buffer_load_dwordx4 v253, s[44:47], s72 offen offset:2080 nt lds\n"
"v_mfma_f32_16x16x32_bf16 a[60:63], v[236:239], v[204:207], 0\n"
"v_max3_f32 v247, v247, v185, v186\n"
"v_max3_f32 v247, v247, v187, v188\n"
"v_mfma_f32_16x16x32_bf16 a[64:67], v[224:227], v[192:195], 0\n"
"ds_read_b64_tr_b16 v[236:237], v241 offset:224\n"
"v_max3_f32 v247, v247, v189, v190\n"
"v_mfma_f32_16x16x32_bf16 a[68:71], v[224:227], v[196:199], 0\n"
"ds_read_b64_tr_b16 v[238:239], v241 offset:736\n"
"s_waitcnt lgkmcnt(4)\n"
"v_mfma_f32_16x16x32_bf16 a[72:75], v[224:227], v[200:203], 0\n"
"v_max_f32_e32 v247, v247, v191\n"
"v_cmp_lt_f32_e32 vcc, s76, v247\n"
"v_mfma_f32_16x16x32_bf16 a[76:79], v[224:227], v[204:207], 0\n"
"s_cbranch_vccnz .Lst1_16\n"
".Lrt1_16:\n"
"v_mfma_f32_16x16x32_bf16 a[80:83], v[228:231], v[192:195], 0\n"
"ds_read_b64_tr_b16 v[224:225], v241 offset:256\n"
"v_mfma_f32_16x16x32_bf16 a[84:87], v[228:231], v[196:199], 0\n"
"ds_read_b64_tr_b16 v[226:227], v241 offset:768\n"
"s_waitcnt lgkmcnt(4)\n"
"v_mfma_f32_16x16x32_bf16 a[88:91], v[228:231], v[200:203], 0\n"
"buffer_load_dwordx4 v242, s[44:47], s72 offen offset:3120 nt lds\n"
"v_mfma_f32_16x16x32_bf16 a[92:95], v[228:231], v[204:207], 0\n"
"v_exp_f32_e32 v160, v160\n"
"v_mfma_f32_16x16x32_bf16 a[96:99], v[232:235], v[192:195], 0\n"
"ds_read_b64_tr_b16 v[228:229], v241 offset:288\n"
"v_add_f32_e32 v243, v160, v243\n"
"v_mfma_f32_16x16x32_bf16 a[100:103], v[232:235], v[196:199], 0\n"
"ds_read_b64_tr_b16 v[230:231], v241 offset:800\n"
"s_waitcnt lgkmcnt(4)\n"
"v_mfma_f32_16x16x32_bf16 a[104:107], v[232:235], v[200:203], 0\n"
"v_exp_f32_e32 v161, v161\n"
"v_mfma_f32_16x16x32_bf16 a[108:111], v[232:235], v[204:207], 0\n"
"v_exp_f32_e32 v162, v162\n"
"v_mfma_f32_16x16x32_bf16 a[112:115], v[236:239], v[192:195], 0\n"
"ds_read_b64_tr_b16 v[232:233], v241 offset:320\n"
"s_add_u32 s72, s72, 32\n"
"s_waitcnt lgkmcnt(3)\n"
"v_mfma_f32_16x16x32_bf16 a[116:119], v[236:239], v[196:199], 0\n"
"ds_read_b64_tr_b16 v[234:235], v241 offset:832\n"
"s_add_u32 m0, s68, 54080\n"
"v_mfma_f32_16x16x32_bf16 a[120:123], v[236:239], v[200:203], 0\n"
"buffer_load_dwordx4 v253, s[48:51], s72 offen offset:0 nt lds\n"
"v_mfma_f32_16x16x32_bf16 a[124:127], v[236:239], v[204:207], 0\n"
"v_exp_f32_e32 v163, v163\n"
"v_mfma_f32_16x16x32_bf16 a[128:131], v[224:227], v[192:195], 0\n"
"ds_read_b64_tr_b16 v[236:237], v241 offset:352\n"
"v_add_f32_e32 v243, v161, v243\n"
"v_mfma_f32_16x16x32_bf16 a[132:135], v[224:227], v[196:199], 0\n"
"ds_read_b64_tr_b16 v[238:239], v241 offset:864\n"
"s_waitcnt lgkmcnt(4)\n"
"v_mfma_f32_16x16x32_bf16 a[136:139], v[224:227], v[200:203], 0\n"
"v_exp_f32_e32 v164, v164\n"
"v_mfma_f32_16x16x32_bf16 a[140:143], v[224:227], v[204:207], 0\n"
"v_exp_f32_e32 v165, v165\n"
"v_mfma_f32_16x16x32_bf16 a[144:147], v[228:231], v[192:195], 0\n"
"ds_read_b64_tr_b16 v[224:225], v241 offset:384\n"
"v_add_f32_e32 v243, v162, v243\n"
"v_mfma_f32_16x16x32_bf16 a[148:151], v[228:231], v[196:199], 0\n"
"ds_read_b64_tr_b16 v[226:227], v241 offset:896\n"
"s_waitcnt lgkmcnt(4)\n"
"v_mfma_f32_16x16x32_bf16 a[152:155], v[228:231], v[200:203], 0\n"
"buffer_load_dwordx4 v242, s[48:51], s72 offen offset:1040 nt lds\n"
"v_mfma_f32_16x16x32_bf16 a[156:159], v[228:231], v[204:207], 0\n"
"v_exp_f32_e32 v166, v166\n"
"v_mfma_f32_16x16x32_bf16 a[160:163], v[232:235], v[192:195], 0\n"
"ds_read_b64_tr_b16 v[228:229], v241 offset:416\n"
"v_add_f32_e32 v243, v163, v243\n"
"v_mfma_f32_16x16x32_bf16 a[164:167], v[232:235], v[196:199], 0\n"
"ds_read_b64_tr_b16 v[230:231], v241 offset:928\n"
"s_waitcnt lgkmcnt(4)\n"
"v_mfma_f32_16x16x32_bf16 a[168:171], v[232:235], v[200:203], 0\n"
"v_exp_f32_e32 v167, v167\n"
"v_mfma_f32_16x16x32_bf16 a[172:175], v[232:235], v[204:207], 0\n"
"v_exp_f32_e32 v168, v168\n"
"v_mfma_f32_16x16x32_bf16 a[176:179], v[236:239], v[192:195], 0\n"
"ds_read_b64_tr_b16 v[232:233], v241 offset:448\n"
"s_waitcnt lgkmcnt(3)\n"
"v_mfma_f32_16x16x32_bf16 a[180:183], v[236:239], v[196:199], 0\n"
"ds_read_b64_tr_b16 v[234:235], v241 offset:960\n"
"s_sub_u32 s72, s72, 32\n"
"v_mfma_f32_16x16x32_bf16 a[184:187], v[236:239], v[200:203], 0\n"
"buffer_load_dwordx4 v253, s[48:51], s72 offen offset:2080 nt lds\n"
"v_mfma_f32_16x16x32_bf16 a[188:191], v[236:239], v[204:207], 0\n"
"v_exp_f32_e32 v169, v169\n"
"v_mfma_f32_16x16x32_bf16 a[192:195], v[224:227], v[192:195], 0\n"
"ds_read_b64_tr_b16 v[236:237], v241 offset:480\n"
"v_add_f32_e32 v244, v164, v244\n"
"v_mfma_f32_16x16x32_bf16 a[196:199], v[224:227], v[196:199], 0\n"
"ds_read_b64_tr_b16 v[238:239], v241 offset:992\n"
"s_waitcnt lgkmcnt(4)\n"
"v_mfma_f32_16x16x32_bf16 a[200:203], v[224:227], v[200:203], 0\n"
"v_exp_f32_e32 v170, v170\n"
"v_mfma_f32_16x16x32_bf16 a[204:207], v[224:227], v[204:207], 0\n"
"v_exp_f32_e32 v171, v171\n"
"v_mfma_f32_16x16x32_bf16 a[208:211], v[228:231], v[192:195], 0\n"
"v_exp_f32_e32 v172, v172\n"
"v_mfma_f32_16x16x32_bf16 a[212:215], v[228:231], v[196:199], 0\n"
"v_exp_f32_e32 v173, v173\n"
"v_mfma_f32_16x16x32_bf16 a[216:219], v[228:231], v[200:203], 0\n"
"buffer_load_dwordx4 v242, s[48:51], s72 offen offset:3120 nt lds\n"
"v_mfma_f32_16x16x32_bf16 a[220:223], v[228:231], v[204:207], 0\n"
"s_waitcnt vmcnt(16) lgkmcnt(0)\n"
"s_barrier\n"
"v_mfma_f32_16x16x32_bf16 a[224:227], v[232:235], v[192:195], 0\n"
"ds_read_b128 v[224:227], v240 offset:16640\n"
"v_add_f32_e32 v244, v165, v244\n"
"v_mfma_f32_16x16x32_bf16 a[228:231], v[232:235], v[196:199], 0\n"
"ds_read_b128 v[228:231], v240 offset:16672\n"
"v_add_f32_e32 v244, v166, v244\n"
"v_mfma_f32_16x16x32_bf16 a[232:235], v[232:235], v[200:203], 0\n"
"v_exp_f32_e32 v174, v174\n"
"v_mfma_f32_16x16x32_bf16 a[236:239], v[232:235], v[204:207], 0\n"
"v_exp_f32_e32 v175, v175\n"
"v_mfma_f32_16x16x32_bf16 a[240:243], v[236:239], v[192:195], 0\n"
"ds_read_b128 v[232:235], v240 offset:16768\n"
"s_add_u32 s73, s73, 16416\n"
"v_mfma_f32_16x16x32_bf16 a[244:247], v[236:239], v[196:199], 0\n"
"s_add_u32 m0, s68, 0\n"
"v_add_f32_e32 v244, v167, v244\n"
"v_mfma_f32_16x16x32_bf16 a[248:251], v[236:239], v[200:203], 0\n"
"buffer_load_dwordx4 v253, s[52:55], s73 offen offset:0 nt lds\n"
"v_mfma_f32_16x16x32_bf16 a[252:255], v[236:239], v[204:207], 0\n"
"s_waitcnt lgkmcnt(1)\n"
"v_add_f32_e32 v245, v168, v245\n"
"v_mfma_f32_16x16x32_bf16 v[128:131], v[224:227], v[0:3], v[208:211]\n"
"s_cbranch_vccnz .Lst2_17\n"
".Lrt2_17:\n"
"v_mfma_f32_16x16x32_bf16 v[128:131], v[228:231], v[16:19], v[128:131]\n"
"ds_read_b128 v[236:239], v240 offset:16800\n"
"v_add_f32_e32 v245, v169, v245\n"
"v_mfma_f32_16x16x32_bf16 v[132:135], v[224:227], v[4:7], v[212:215]\n"
"v_exp_f32_e32 v176, v176\n"
"v_mfma_f32_16x16x32_bf16 v[132:135], v[228:231], v[20:23], v[132:135]\n"
"v_exp_f32_e32 v177, v177\n"
"v_mfma_f32_16x16x32_bf16 v[136:139], v[224:227], v[8:11], v[216:219]\n"
"v_exp_f32_e32 v178, v178\n"
"v_mfma_f32_16x16x32_bf16 v[136:139], v[228:231], v[24:27], v[136:139]\n"
"v_exp_f32_e32 v179, v179\n"
"v_mfma_f32_16x16x32_bf16 v[140:143], v[224:227], v[12:15], v[220:223]\n"
"buffer_load_dwordx4 v242, s[52:55], s73 offen offset:1040 nt lds\n"
"v_mfma_f32_16x16x32_bf16 v[140:143], v[228:231], v[28:31], v[140:143]\n"
"s_waitcnt lgkmcnt(0)\n"
"v_add_f32_e32 v245, v170, v245\n"
"v_mfma_f32_16x16x32_bf16 v[140:143], v[232:235], v[44:47], v[140:143]\n"
"ds_read_b128 v[224:227], v240 offset:16896\n"
"v_add_f32_e32 v245, v171, v245\n"
"v_mfma_f32_16x16x32_bf16 v[140:143], v[236:239], v[60:63], v[140:143]\n"
"ds_read_b128 v[228:231], v240 offset:16928\n"
"v_add_f32_e32 v246, v172, v246\n"
"v_mfma_f32_16x16x32_bf16 v[136:139], v[232:235], v[40:43], v[136:139]\n"
"v_exp_f32_e32 v180, v180\n"
"v_mfma_f32_16x16x32_bf16 v[136:139], v[236:239], v[56:59], v[136:139]\n"
"v_exp_f32_e32 v181, v181\n"
"v_mfma_f32_16x16x32_bf16 v[132:135], v[232:235], v[36:39], v[132:135]\n"
"v_exp_f32_e32 v182, v182\n"
"v_mfma_f32_16x16x32_bf16 v[132:135], v[236:239], v[52:55], v[132:135]\n"
"s_sub_u32 s73, s73, 32\n"
"v_add_f32_e32 v246, v173, v246\n"
"v_mfma_f32_16x16x32_bf16 v[128:131], v[232:235], v[32:35], v[128:131]\n"
"buffer_load_dwordx4 v253, s[52:55], s73 offen offset:2080 nt lds\n"
"v_mfma_f32_16x16x32_bf16 v[128:131], v[236:239], v[48:51], v[128:131]\n"
"s_waitcnt lgkmcnt(0)\n"
"v_add_f32_e32 v246, v174, v246\n"
"v_mfma_f32_16x16x32_bf16 v[128:131], v[224:227], v[64:67], v[128:131]\n"
"ds_read_b128 v[232:235], v240 offset:17024\n"
"v_add_f32_e32 v246, v175, v246\n"
"v_mfma_f32_16x16x32_bf16 v[128:131], v[228:231], v[80:83], v[128:131]\n"
"ds_read_b128 v[236:239], v240 offset:17056\n"
"v_add_f32_e32 v243, v176, v243\n"
"v_mfma_f32_16x16x32_bf16 v[132:135], v[224:227], v[68:71], v[132:135]\n"
"v_exp_f32_e32 v183, v183\n"
"v_mfma_f32_16x16x32_bf16 v[132:135], v[228:231], v[84:87], v[132:135]\n"
"v_exp_f32_e32 v184, v184\n"
"v_mfma_f32_16x16x32_bf16 v[136:139], v[224:227], v[72:75], v[136:139]\n"
"v_exp_f32_e32 v185, v185\n"
"v_mfma_f32_16x16x32_bf16 v[136:139], v[228:231], v[88:91], v[136:139]\n"
"v_exp_f32_e32 v186, v186\n"
"v_mfma_f32_16x16x32_bf16 v[140:143], v[224:227], v[76:79], v[140:143]\n"
"buffer_load_dwordx4 v242, s[52:55], s73 offen offset:3120 nt lds\n"
"v_mfma_f32_16x16x32_bf16 v[140:143], v[228:231], v[92:95], v[140:143]\n"
"s_waitcnt lgkmcnt(0)\n"
"v_add_f32_e32 v243, v177, v243\n"
"v_mfma_f32_16x16x32_bf16 v[140:143], v[232:235], v[108:111], v[140:143]\n"
"ds_read_b128 v[224:227], v240 offset:17152\n"
"v_add_f32_e32 v243, v178, v243\n"
"v_mfma_f32_16x16x32_bf16 v[140:143], v[236:239], v[124:127], v[140:143]\n"
"ds_read_b128 v[228:231], v240 offset:17184\n"
"v_add_f32_e32 v243, v179, v243\n"
"v_mfma_f32_16x16x32_bf16 v[136:139], v[232:235], v[104:107], v[136:139]\n"
"v_exp_f32_e32 v187, v187\n"
"v_mfma_f32_16x16x32_bf16 v[136:139], v[236:239], v[120:123], v[136:139]\n"
"v_exp_f32_e32 v188, v188\n"
"v_mfma_f32_16x16x32_bf16 v[132:135], v[232:235], v[100:103], v[132:135]\n"
"s_add_u32 s73, s73, 32\n"
"v_add_f32_e32 v244, v180, v244\n"
"v_mfma_f32_16x16x32_bf16 v[132:135], v[236:239], v[116:119], v[132:135]\n"
"s_add_u32 m0, s68, 4160\n"
"v_add_f32_e32 v244, v181, v244\n"
"v_mfma_f32_16x16x32_bf16 v[128:131], v[232:235], v[96:99], v[128:131]\n"
"buffer_load_dwordx4 v253, s[56:59], s73 offen offset:0 nt lds\n"
"v_mfma_f32_16x16x32_bf16 v[128:131], v[236:239], v[112:115], v[128:131]\n"
"s_waitcnt lgkmcnt(0)\n"
"v_add_f32_e32 v244, v182, v244\n"
"v_mfma_f32_16x16x32_bf16 v[144:147], v[224:227], v[0:3], v[208:211]\n"
"ds_read_b128 v[232:235], v240 offset:17280\n"
"v_add_f32_e32 v244, v183, v244\n"
"v_mfma_f32_16x16x32_bf16 v[144:147], v[228:231], v[16:19], v[144:147]\n"
"ds_read_b128 v[236:239], v240 offset:17312\n"
"v_add_f32_e32 v245, v184, v245\n"
"v_mfma_f32_16x16x32_bf16 v[148:151], v[224:227], v[4:7], v[212:215]\n"
"v_exp_f32_e32 v189, v189\n"
"v_mfma_f32_16x16x32_bf16 v[148:151], v[228:231], v[20:23], v[148:151]\n"
"v_exp_f32_e32 v190, v190\n"
"v_mfma_f32_16x16x32_bf16 v[152:155], v[224:227], v[8:11], v[216:219]\n"
"v_exp_f32_e32 v191, v191\n"
"v_mfma_f32_16x16x32_bf16 v[152:155], v[228:231], v[24:27], v[152:155]\n"
"v_add_f32_e32 v245, v185, v245\n"
"v_add_f32_e32 v245, v186, v245\n"
"v_mfma_f32_16x16x32_bf16 v[156:159], v[224:227], v[12:15], v[220:223]\n"
"buffer_load_dwordx4 v242, s[56:59], s73 offen offset:1040 nt lds\n"
"v_mfma_f32_16x16x32_bf16 v[156:159], v[228:231], v[28:31], v[156:159]\n"
"s_waitcnt lgkmcnt(0)\n"
"v_add_f32_e32 v245, v187, v245\n"
"v_mfma_f32_16x16x32_bf16 v[156:159], v[232:235], v[44:47], v[156:159]\n"
"ds_read_b128 v[224:227], v240 offset:17408\n"
"s_mov_b32 s75, 5\n"
"v_mfma_f32_16x16x32_bf16 v[156:159], v[236:239], v[60:63], v[156:159]\n"
"ds_read_b128 v[228:231], v240 offset:17440\n"
"v_add_f32_e32 v246, v188, v246\n"
"v_mfma_f32_16x16x32_bf16 v[152:155], v[232:235], v[40:43], v[152:155]\n"
"v_add_f32_e32 v246, v189, v246\n"
"v_add_f32_e32 v246, v190, v246\n"
"v_mfma_f32_16x16x32_bf16 v[152:155], v[236:239], v[56:59], v[152:155]\n"
"v_add_f32_e32 v246, v191, v246\n"
"v_cvt_pk_bf16_f32 v192, v160, v161\n"
"v_mfma_f32_16x16x32_bf16 v[148:151], v[232:235], v[36:39], v[148:151]\n"
"v_cvt_pk_bf16_f32 v193, v162, v163\n"
"v_cvt_pk_bf16_f32 v196, v164, v165\n"
"v_mfma_f32_16x16x32_bf16 v[148:151], v[236:239], v[52:55], v[148:151]\n"
"s_sub_u32 s73, s73, 32\n"
"v_cvt_pk_bf16_f32 v197, v166, v167\n"
"v_mfma_f32_16x16x32_bf16 v[144:147], v[232:235], v[32:35], v[144:147]\n"
"buffer_load_dwordx4 v253, s[56:59], s73 offen offset:2080 nt lds\n"
"v_mfma_f32_16x16x32_bf16 v[144:147], v[236:239], v[48:51], v[144:147]\n"
"s_waitcnt lgkmcnt(0)\n"
"v_cvt_pk_bf16_f32 v200, v168, v169\n"
"v_mfma_f32_16x16x32_bf16 v[144:147], v[224:227], v[64:67], v[144:147]\n"
"ds_read_b128 v[232:235], v240 offset:17536\n"
"v_cvt_pk_bf16_f32 v201, v170, v171\n"
"v_mfma_f32_16x16x32_bf16 v[144:147], v[228:231], v[80:83], v[144:147]\n"
"ds_read_b128 v[236:239], v240 offset:17568\n"
"v_cvt_pk_bf16_f32 v204, v172, v173\n"
"v_mfma_f32_16x16x32_bf16 v[148:151], v[224:227], v[68:71], v[148:151]\n"
"v_cvt_pk_bf16_f32 v205, v174, v175\n"
"v_cvt_pk_bf16_f32 v194, v176, v177\n"
"v_mfma_f32_16x16x32_bf16 v[148:151], v[228:231], v[84:87], v[148:151]\n"
"v_cvt_pk_bf16_f32 v195, v178, v179\n"
"v_cvt_pk_bf16_f32 v198, v180, v181\n"
"v_mfma_f32_16x16x32_bf16 v[152:155], v[224:227], v[72:75], v[152:155]\n"
"v_cvt_pk_bf16_f32 v199, v182, v183\n"
"v_cvt_pk_bf16_f32 v202, v184, v185\n"
"v_mfma_f32_16x16x32_bf16 v[152:155], v[228:231], v[88:91], v[152:155]\n"
"v_cvt_pk_bf16_f32 v203, v186, v187\n"
"v_cvt_pk_bf16_f32 v206, v188, v189\n"
"v_mfma_f32_16x16x32_bf16 v[156:159], v[224:227], v[76:79], v[156:159]\n"
"buffer_load_dwordx4 v242, s[56:59], s73 offen offset:3120 nt lds\n"
"v_mfma_f32_16x16x32_bf16 v[156:159], v[228:231], v[92:95], v[156:159]\n"
"s_waitcnt vmcnt(16) lgkmcnt(0)\n"
"s_barrier\n"
"v_mfma_f32_16x16x32_bf16 v[156:159], v[232:235], v[108:111], v[156:159]\n"
"ds_read_b64_tr_b16 v[224:225], v241 offset:33280\n"
"v_cvt_pk_bf16_f32 v207, v190, v191\n"
"v_mfma_f32_16x16x32_bf16 v[156:159], v[236:239], v[124:127], v[156:159]\n"
"ds_read_b64_tr_b16 v[226:227], v241 offset:33792\n"
"v_mfma_f32_16x16x32_bf16 v[152:155], v[232:235], v[104:107], v[152:155]\n"
"ds_read_b64_tr_b16 v[228:229], v241 offset:33312\n"
"v_mfma_f32_16x16x32_bf16 v[152:155], v[236:239], v[120:123], v[152:155]\n"
"ds_read_b64_tr_b16 v[230:231], v241 offset:33824\n"
"v_mfma_f32_16x16x32_bf16 v[148:151], v[232:235], v[100:103], v[148:151]\n"
"s_add_u32 s72, s72, 16416\n"
"v_mfma_f32_16x16x32_bf16 v[148:151], v[236:239], v[116:119], v[148:151]\n"
"s_add_u32 m0, s68, 16640\n"
"v_mfma_f32_16x16x32_bf16 v[144:147], v[232:235], v[96:99], v[144:147]\n"
"buffer_load_dwordx4 v253, s[44:47], s72 offen offset:0 nt lds\n"
"v_mfma_f32_16x16x32_bf16 v[144:147], v[236:239], v[112:115], v[144:147]\n"
"s_waitcnt lgkmcnt(2)\n"
".Lloop:\n"
"v_mfma_f32_16x16x32_bf16 a[0:3], v[224:227], v[192:195], a[0:3]\n"
"ds_read_b64_tr_b16 v[232:233], v241 offset:33344\n"
"v_max3_f32 v247, v128, v129, v130\n"
"v_mfma_f32_16x16x32_bf16 a[4:7], v[224:227], v[196:199], a[4:7]\n"
"ds_read_b64_tr_b16 v[234:235], v241 offset:33856\n"
"s_waitcnt lgkmcnt(2)\n"
"v_mfma_f32_16x16x32_bf16 a[8:11], v[224:227], v[200:203], a[8:11]\n"
"ds_read_b64_tr_b16 v[236:237], v241 offset:33376\n"
"v_max3_f32 v247, v247, v131, v132\n"
"v_mfma_f32_16x16x32_bf16 a[12:15], v[224:227], v[204:207], a[12:15]\n"
"ds_read_b64_tr_b16 v[238:239], v241 offset:33888\n"
"v_max3_f32 v247, v247, v133, v134\n"
"v_mfma_f32_16x16x32_bf16 a[16:19], v[228:231], v[192:195], a[16:19]\n"
"ds_read_b64_tr_b16 v[224:225], v241 offset:33408\n"
"v_max3_f32 v247, v247, v135, v136\n"
"v_mfma_f32_16x16x32_bf16 a[20:23], v[228:231], v[196:199], a[20:23]\n"
"ds_read_b64_tr_b16 v[226:227], v241 offset:33920\n"
"v_max3_f32 v247, v247, v137, v138\n"
"v_mfma_f32_16x16x32_bf16 a[24:27], v[228:231], v[200:203], a[24:27]\n"
"buffer_load_dwordx4 v242, s[44:47], s72 offen offset:1040 nt lds\n"
"s_waitcnt lgkmcnt(4)\n"
"v_mfma_f32_16x16x32_bf16 a[28:31], v[228:231], v[204:207], a[28:31]\n"
"v_max3_f32 v247, v247, v139, v140\n"
"v_max3_f32 v247, v247, v141, v142\n"
"v_mfma_f32_16x16x32_bf16 a[32:35], v[232:235], v[192:195], a[32:35]\n"
"ds_read_b64_tr_b16 v[228:229], v241 offset:33440\n"
"v_max3_f32 v247, v247, v143, v144\n"
"v_mfma_f32_16x16x32_bf16 a[36:39], v[232:235], v[196:199], a[36:39]\n"
"ds_read_b64_tr_b16 v[230:231], v241 offset:33952\n"
"s_waitcnt lgkmcnt(4)\n"
"v_mfma_f32_16x16x32_bf16 a[40:43], v[232:235], v[200:203], a[40:43]\n"
"v_max3_f32 v247, v247, v145, v146\n"
"v_max3_f32 v247, v247, v147, v148\n"
"v_mfma_f32_16x16x32_bf16 a[44:47], v[232:235], v[204:207], a[44:47]\n"
"v_max3_f32 v247, v247, v149, v150\n"
"v_max3_f32 v247, v247, v151, v152\n"
"v_mfma_f32_16x16x32_bf16 a[48:51], v[236:239], v[192:195], a[48:51]\n"
"ds_read_b64_tr_b16 v[232:233], v241 offset:33472\n"
"s_waitcnt lgkmcnt(3)\n"
"v_mfma_f32_16x16x32_bf16 a[52:55], v[236:239], v[196:199], a[52:55]\n"
"ds_read_b64_tr_b16 v[234:235], v241 offset:33984\n"
"s_sub_u32 s72, s72, 32\n"
"v_mfma_f32_16x16x32_bf16 a[56:59], v[236:239], v[200:203], a[56:59]\n"
"buffer_load_dwordx4 v253, s[44:47], s72 offen offset:2080 nt lds\n"
"v_mfma_f32_16x16x32_bf16 a[60:63], v[236:239], v[204:207], a[60:63]\n"
"v_max3_f32 v247, v247, v153, v154\n"
"v_max3_f32 v247, v247, v155, v156\n"
"v_mfma_f32_16x16x32_bf16 a[64:67], v[224:227], v[192:195], a[64:67]\n"
"ds_read_b64_tr_b16 v[236:237], v241 offset:33504\n"
"v_max3_f32 v247, v247, v157, v158\n"
"v_mfma_f32_16x16x32_bf16 a[68:71], v[224:227], v[196:199], a[68:71]\n"
"ds_read_b64_tr_b16 v[238:239], v241 offset:34016\n"
"s_waitcnt lgkmcnt(4)\n"
"v_mfma_f32_16x16x32_bf16 a[72:75], v[224:227], v[200:203], a[72:75]\n"
"v_max_f32_e32 v247, v247, v159\n"
"v_cmp_lt_f32_e32 vcc, s76, v247\n"
"v_mfma_f32_16x16x32_bf16 a[76:79], v[224:227], v[204:207], a[76:79]\n"
"s_cbranch_vccnz .Lst1_L0\n"
".Lrt1_L0:\n"
"v_mfma_f32_16x16x32_bf16 a[80:83], v[228:231], v[192:195], a[80:83]\n"
"ds_read_b64_tr_b16 v[224:225], v241 offset:33536\n"
"v_mfma_f32_16x16x32_bf16 a[84:87], v[228:231], v[196:199], a[84:87]\n"
"ds_read_b64_tr_b16 v[226:227], v241 offset:34048\n"
"s_waitcnt lgkmcnt(4)\n"
"v_mfma_f32_16x16x32_bf16 a[88:91], v[228:231], v[200:203], a[88:91]\n"
"buffer_load_dwordx4 v242, s[44:47], s72 offen offset:3120 nt lds\n"
"v_mfma_f32_16x16x32_bf16 a[92:95], v[228:231], v[204:207], a[92:95]\n"
"v_exp_f32_e32 v128, v128\n"
"v_mfma_f32_16x16x32_bf16 a[96:99], v[232:235], v[192:195], a[96:99]\n"
"ds_read_b64_tr_b16 v[228:229], v241 offset:33568\n"
"v_add_f32_e32 v243, v128, v243\n"
"v_mfma_f32_16x16x32_bf16 a[100:103], v[232:235], v[196:199], a[100:103]\n"
"ds_read_b64_tr_b16 v[230:231], v241 offset:34080\n"
"s_waitcnt lgkmcnt(4)\n"
"v_mfma_f32_16x16x32_bf16 a[104:107], v[232:235], v[200:203], a[104:107]\n"
"v_exp_f32_e32 v129, v129\n"
"v_mfma_f32_16x16x32_bf16 a[108:111], v[232:235], v[204:207], a[108:111]\n"
"v_exp_f32_e32 v130, v130\n"
"v_mfma_f32_16x16x32_bf16 a[112:115], v[236:239], v[192:195], a[112:115]\n"
"ds_read_b64_tr_b16 v[232:233], v241 offset:33600\n"
"s_add_u32 s72, s72, 32\n"
"s_waitcnt lgkmcnt(3)\n"
"v_mfma_f32_16x16x32_bf16 a[116:119], v[236:239], v[196:199], a[116:119]\n"
"ds_read_b64_tr_b16 v[234:235], v241 offset:34112\n"
"s_add_u32 m0, s68, 20800\n"
"v_mfma_f32_16x16x32_bf16 a[120:123], v[236:239], v[200:203], a[120:123]\n"
"buffer_load_dwordx4 v253, s[48:51], s72 offen offset:0 nt lds\n"
"v_mfma_f32_16x16x32_bf16 a[124:127], v[236:239], v[204:207], a[124:127]\n"
"v_exp_f32_e32 v131, v131\n"
"v_mfma_f32_16x16x32_bf16 a[128:131], v[224:227], v[192:195], a[128:131]\n"
"ds_read_b64_tr_b16 v[236:237], v241 offset:33632\n"
"v_add_f32_e32 v243, v129, v243\n"
"v_mfma_f32_16x16x32_bf16 a[132:135], v[224:227], v[196:199], a[132:135]\n"
"ds_read_b64_tr_b16 v[238:239], v241 offset:34144\n"
"s_waitcnt lgkmcnt(4)\n"
"v_mfma_f32_16x16x32_bf16 a[136:139], v[224:227], v[200:203], a[136:139]\n"
"v_exp_f32_e32 v132, v132\n"
"v_mfma_f32_16x16x32_bf16 a[140:143], v[224:227], v[204:207], a[140:143]\n"
"v_exp_f32_e32 v133, v133\n"
"v_mfma_f32_16x16x32_bf16 a[144:147], v[228:231], v[192:195], a[144:147]\n"
"ds_read_b64_tr_b16 v[224:225], v241 offset:33664\n"
"v_add_f32_e32 v243, v130, v243\n"
"v_mfma_f32_16x16x32_bf16 a[148:151], v[228:231], v[196:199], a[148:151]\n"
"ds_read_b64_tr_b16 v[226:227], v241 offset:34176\n"
"s_waitcnt lgkmcnt(4)\n"
"v_mfma_f32_16x16x32_bf16 a[152:155], v[228:231], v[200:203], a[152:155]\n"
"buffer_load_dwordx4 v242, s[48:51], s72 offen offset:1040 nt lds\n"
"v_mfma_f32_16x16x32_bf16 a[156:159], v[228:231], v[204:207], a[156:159]\n"
"v_exp_f32_e32 v134, v134\n"
"v_mfma_f32_16x16x32_bf16 a[160:163], v[232:235], v[192:195], a[160:163]\n"
"ds_read_b64_tr_b16 v[228:229], v241 offset:33696\n"
"v_add_f32_e32 v243, v131, v243\n"
"v_mfma_f32_16x16x32_bf16 a[164:167], v[232:235], v[196:199], a[164:167]\n"
"ds_read_b64_tr_b16 v[230:231], v241 offset:34208\n"
"s_waitcnt lgkmcnt(4)\n"
"v_mfma_f32_16x16x32_bf16 a[168:171], v[232:235], v[200:203], a[168:171]\n"
"v_exp_f32_e32 v135, v135\n"
"v_mfma_f32_16x16x32_bf16 a[172:175], v[232:235], v[204:207], a[172:175]\n"
"v_exp_f32_e32 v136, v136\n"
"v_mfma_f32_16x16x32_bf16 a[176:179], v[236:239], v[192:195], a[176:179]\n"
"ds_read_b64_tr_b16 v[232:233], v241 offset:33728\n"
"s_waitcnt lgkmcnt(3)\n"
"v_mfma_f32_16x16x32_bf16 a[180:183], v[236:239], v[196:199], a[180:183]\n"
"ds_read_b64_tr_b16 v[234:235], v241 offset:34240\n"
"s_sub_u32 s72, s72, 32\n"
"v_mfma_f32_16x16x32_bf16 a[184:187], v[236:239], v[200:203], a[184:187]\n"
"buffer_load_dwordx4 v253, s[48:51], s72 offen offset:2080 nt lds\n"
"v_mfma_f32_16x16x32_bf16 a[188:191], v[236:239], v[204:207], a[188:191]\n"
"v_exp_f32_e32 v137, v137\n"
"v_mfma_f32_16x16x32_bf16 a[192:195], v[224:227], v[192:195], a[192:195]\n"
"ds_read_b64_tr_b16 v[236:237], v241 offset:33760\n"
"v_add_f32_e32 v244, v132, v244\n"
"v_mfma_f32_16x16x32_bf16 a[196:199], v[224:227], v[196:199], a[196:199]\n"
"ds_read_b64_tr_b16 v[238:239], v241 offset:34272\n"
"s_waitcnt lgkmcnt(4)\n"
"v_mfma_f32_16x16x32_bf16 a[200:203], v[224:227], v[200:203], a[200:203]\n"
"v_exp_f32_e32 v138, v138\n"
"v_mfma_f32_16x16x32_bf16 a[204:207], v[224:227], v[204:207], a[204:207]\n"
"v_exp_f32_e32 v139, v139\n"
"v_mfma_f32_16x16x32_bf16 a[208:211], v[228:231], v[192:195], a[208:211]\n"
"v_exp_f32_e32 v140, v140\n"
"v_mfma_f32_16x16x32_bf16 a[212:215], v[228:231], v[196:199], a[212:215]\n"
"v_exp_f32_e32 v141, v141\n"
"v_mfma_f32_16x16x32_bf16 a[216:219], v[228:231], v[200:203], a[216:219]\n"
"buffer_load_dwordx4 v242, s[48:51], s72 offen offset:3120 nt lds\n"
"v_mfma_f32_16x16x32_bf16 a[220:223], v[228:231], v[204:207], a[220:223]\n"
"s_waitcnt vmcnt(16) lgkmcnt(0)\n"
"s_barrier\n"
"v_mfma_f32_16x16x32_bf16 a[224:227], v[232:235], v[192:195], a[224:227]\n"
"ds_read_b128 v[224:227], v240 offset:49920\n"
"v_add_f32_e32 v244, v133, v244\n"
"v_mfma_f32_16x16x32_bf16 a[228:231], v[232:235], v[196:199], a[228:231]\n"
"ds_read_b128 v[228:231], v240 offset:49952\n"
"v_add_f32_e32 v244, v134, v244\n"
"v_mfma_f32_16x16x32_bf16 a[232:235], v[232:235], v[200:203], a[232:235]\n"
"v_exp_f32_e32 v142, v142\n"
"v_mfma_f32_16x16x32_bf16 a[236:239], v[232:235], v[204:207], a[236:239]\n"
"v_exp_f32_e32 v143, v143\n"
"v_mfma_f32_16x16x32_bf16 a[240:243], v[236:239], v[192:195], a[240:243]\n"
"ds_read_b128 v[232:235], v240 offset:50048\n"
"s_add_u32 s73, s73, 16416\n"
"v_mfma_f32_16x16x32_bf16 a[244:247], v[236:239], v[196:199], a[244:247]\n"
"s_add_u32 m0, s68, 33280\n"
"v_add_f32_e32 v244, v135, v244\n"
"v_mfma_f32_16x16x32_bf16 a[248:251], v[236:239], v[200:203], a[248:251]\n"
"buffer_load_dwordx4 v253, s[52:55], s73 offen offset:0 nt lds\n"
"v_mfma_f32_16x16x32_bf16 a[252:255], v[236:239], v[204:207], a[252:255]\n"
"s_waitcnt lgkmcnt(1)\n"
"v_add_f32_e32 v245, v136, v245\n"
"v_mfma_f32_16x16x32_bf16 v[160:163], v[224:227], v[0:3], v[208:211]\n"
"s_cbranch_vccnz .Lst2_L1\n"
".Lrt2_L1:\n"
"v_mfma_f32_16x16x32_bf16 v[160:163], v[228:231], v[16:19], v[160:163]\n"
"ds_read_b128 v[236:239], v240 offset:50080\n"
"v_add_f32_e32 v245, v137, v245\n"
"v_mfma_f32_16x16x32_bf16 v[164:167], v[224:227], v[4:7], v[212:215]\n"
"v_exp_f32_e32 v144, v144\n"
"v_mfma_f32_16x16x32_bf16 v[164:167], v[228:231], v[20:23], v[164:167]\n"
"v_exp_f32_e32 v145, v145\n"
"v_mfma_f32_16x16x32_bf16 v[168:171], v[224:227], v[8:11], v[216:219]\n"
"v_exp_f32_e32 v146, v146\n"
"v_mfma_f32_16x16x32_bf16 v[168:171], v[228:231], v[24:27], v[168:171]\n"
"v_exp_f32_e32 v147, v147\n"
"v_mfma_f32_16x16x32_bf16 v[172:175], v[224:227], v[12:15], v[220:223]\n"
"buffer_load_dwordx4 v242, s[52:55], s73 offen offset:1040 nt lds\n"
"v_mfma_f32_16x16x32_bf16 v[172:175], v[228:231], v[28:31], v[172:175]\n"
"s_waitcnt lgkmcnt(0)\n"
"v_add_f32_e32 v245, v138, v245\n"
"v_mfma_f32_16x16x32_bf16 v[172:175], v[232:235], v[44:47], v[172:175]\n"
"ds_read_b128 v[224:227], v240 offset:50176\n"
"v_add_f32_e32 v245, v139, v245\n"
"v_mfma_f32_16x16x32_bf16 v[172:175], v[236:239], v[60:63], v[172:175]\n"
"ds_read_b128 v[228:231], v240 offset:50208\n"
"v_add_f32_e32 v246, v140, v246\n"
"v_mfma_f32_16x16x32_bf16 v[168:171], v[232:235], v[40:43], v[168:171]\n"
"v_exp_f32_e32 v148, v148\n"
"v_mfma_f32_16x16x32_bf16 v[168:171], v[236:239], v[56:59], v[168:171]\n"
"v_exp_f32_e32 v149, v149\n"
"v_mfma_f32_16x16x32_bf16 v[164:167], v[232:235], v[36:39], v[164:167]\n"
"s_branch .Lpo6\n"
".Lpg6:\n"
"s_setpc_b64 s[78:79]\n"
".Lpo6:\n"
"v_exp_f32_e32 v150, v150\n"
"v_mfma_f32_16x16x32_bf16 v[164:167], v[236:239], v[52:55], v[164:167]\n"
"s_sub_u32 s73, s73, 32\n"
"v_add_f32_e32 v246, v141, v246\n"
"v_mfma_f32_16x16x32_bf16 v[160:163], v[232:235], v[32:35], v[160:163]\n"
"buffer_load_dwordx4 v253, s[52:55], s73 offen offset:2080 nt lds\n"
"v_mfma_f32_16x16x32_bf16 v[160:163], v[236:239], v[48:51], v[160:163]\n"
"s_waitcnt lgkmcnt(0)\n"
"v_add_f32_e32 v246, v142, v246\n"
"v_mfma_f32_16x16x32_bf16 v[160:163], v[224:227], v[64:67], v[160:163]\n"
"ds_read_b128 v[232:235], v240 offset:50304\n"
"v_add_f32_e32 v246, v143, v246\n"
"v_mfma_f32_16x16x32_bf16 v[160:163], v[228:231], v[80:83], v[160:163]\n"
"ds_read_b128 v[236:239], v240 offset:50336\n"
"v_add_f32_e32 v243, v144, v243\n"
"v_mfma_f32_16x16x32_bf16 v[164:167], v[224:227], v[68:71], v[164:167]\n"
"v_exp_f32_e32 v151, v151\n"
"v_mfma_f32_16x16x32_bf16 v[164:167], v[228:231], v[84:87], v[164:167]\n"
"v_exp_f32_e32 v152, v152\n"
"v_mfma_f32_16x16x32_bf16 v[168:171], v[224:227], v[72:75], v[168:171]\n"
"v_exp_f32_e32 v153, v153\n"
"v_mfma_f32_16x16x32_bf16 v[168:171], v[228:231], v[88:91], v[168:171]\n"
"v_exp_f32_e32 v154, v154\n"
"v_mfma_f32_16x16x32_bf16 v[172:175], v[224:227], v[76:79], v[172:175]\n"
"buffer_load_dwordx4 v242, s[52:55], s73 offen offset:3120 nt lds\n"
"v_mfma_f32_16x16x32_bf16 v[172:175], v[228:231], v[92:95], v[172:175]\n"
"s_waitcnt lgkmcnt(0)\n"
"v_add_f32_e32 v243, v145, v243\n"
"v_mfma_f32_16x16x32_bf16 v[172:175], v[232:235], v[108:111], v[172:175]\n"
"ds_read_b128 v[224:227], v240 offset:50432\n"
"v_add_f32_e32 v243, v146, v243\n"
"v_mfma_f32_16x16x32_bf16 v[172:175], v[236:239], v[124:127], v[172:175]\n"
"ds_read_b128 v[228:231], v240 offset:50464\n"
"v_add_f32_e32 v243, v147, v243\n"
"v_mfma_f32_16x16x32_bf16 v[168:171], v[232:235], v[104:107], v[168:171]\n"
"v_exp_f32_e32 v155, v155\n"
"v_mfma_f32_16x16x32_bf16 v[168:171], v[236:239], v[120:123], v[168:171]\n"
"v_exp_f32_e32 v156, v156\n"
"v_mfma_f32_16x16x32_bf16 v[164:167], v[232:235], v[100:103], v[164:167]\n"
"s_add_u32 s73, s73, 32\n"
"v_add_f32_e32 v244, v148, v244\n"
"v_mfma_f32_16x16x32_bf16 v[164:167], v[236:239], v[116:119], v[164:167]\n"
"s_add_u32 m0, s68, 37440\n"
"v_add_f32_e32 v244, v149, v244\n"
"v_mfma_f32_16x16x32_bf16 v[160:163], v[232:235], v[96:99], v[160:163]\n"
"buffer_load_dwordx4 v253, s[56:59], s73 offen offset:0 nt lds\n"
"v_mfma_f32_16x16x32_bf16 v[160:163], v[236:239], v[112:115], v[160:163]\n"
"s_waitcnt lgkmcnt(0)\n"
"v_add_f32_e32 v244, v150, v244\n"
"v_mfma_f32_16x16x32_bf16 v[176:179], v[224:227], v[0:3], v[208:211]\n"
"ds_read_b128 v[232:235], v240 offset:50560\n"
"v_add_f32_e32 v244, v151, v244\n"
"v_mfma_f32_16x16x32_bf16 v[176:179], v[228:231], v[16:19], v[176:179]\n"
"ds_read_b128 v[236:239], v240 offset:50592\n"
"v_add_f32_e32 v245, v152, v245\n"
"v_mfma_f32_16x16x32_bf16 v[180:183], v[224:227], v[4:7], v[212:215]\n"
"v_exp_f32_e32 v157, v157\n"
"v_mfma_f32_16x16x32_bf16 v[180:183], v[228:231], v[20:23], v[180:183]\n"
"v_exp_f32_e32 v158, v158\n"
"v_mfma_f32_16x16x32_bf16 v[184:187], v[224:227], v[8:11], v[216:219]\n"
"v_exp_f32_e32 v159, v159\n"
"v_mfma_f32_16x16x32_bf16 v[184:187], v[228:231], v[24:27], v[184:187]\n"
"v_add_f32_e32 v245, v153, v245\n"
"v_add_f32_e32 v245, v154, v245\n"
"v_mfma_f32_16x16x32_bf16 v[188:191], v[224:227], v[12:15], v[220:223]\n"
"buffer_load_dwordx4 v242, s[56:59], s73 offen offset:1040 nt lds\n"
"v_mfma_f32_16x16x32_bf16 v[188:191], v[228:231], v[28:31], v[188:191]\n"
"s_waitcnt lgkmcnt(0)\n"
"v_add_f32_e32 v245, v155, v245\n"
"v_mfma_f32_16x16x32_bf16 v[188:191], v[232:235], v[44:47], v[188:191]\n"
"ds_read_b128 v[224:227], v240 offset:50688\n"
"v_add_f32_e32 v246, v156, v246\n"
"v_mfma_f32_16x16x32_bf16 v[188:191], v[236:239], v[60:63], v[188:191]\n"
"ds_read_b128 v[228:231], v240 offset:50720\n"
"v_add_f32_e32 v246, v157, v246\n"
"v_mfma_f32_16x16x32_bf16 v[184:187], v[232:235], v[40:43], v[184:187]\n"
"v_add_f32_e32 v246, v158, v246\n"
"v_add_f32_e32 v246, v159, v246\n"
"v_mfma_f32_16x16x32_bf16 v[184:187], v[236:239], v[56:59], v[184:187]\n"
"v_cvt_pk_bf16_f32 v192, v128, v129\n"
"v_cvt_pk_bf16_f32 v193, v130, v131\n"
"v_mfma_f32_16x16x32_bf16 v[180:183], v[232:235], v[36:39], v[180:183]\n"
"v_cvt_pk_bf16_f32 v196, v132, v133\n"
"v_cvt_pk_bf16_f32 v197, v134, v135\n"
"v_mfma_f32_16x16x32_bf16 v[180:183], v[236:239], v[52:55], v[180:183]\n"
"s_sub_u32 s73, s73, 32\n"
"v_cvt_pk_bf16_f32 v200, v136, v137\n"
"v_mfma_f32_16x16x32_bf16 v[176:179], v[232:235], v[32:35], v[176:179]\n"
"buffer_load_dwordx4 v253, s[56:59], s73 offen offset:2080 nt lds\n"
"v_mfma_f32_16x16x32_bf16 v[176:179], v[236:239], v[48:51], v[176:179]\n"
"s_waitcnt lgkmcnt(0)\n"
"v_cvt_pk_bf16_f32 v201, v138, v139\n"
"v_mfma_f32_16x16x32_bf16 v[176:179], v[224:227], v[64:67], v[176:179]\n"
"ds_read_b128 v[232:235], v240 offset:50816\n"
"v_cvt_pk_bf16_f32 v204, v140, v141\n"
"v_mfma_f32_16x16x32_bf16 v[176:179], v[228:231], v[80:83], v[176:179]\n"
"ds_read_b128 v[236:239], v240 offset:50848\n"
"v_cvt_pk_bf16_f32 v205, v142, v143\n"
"v_mfma_f32_16x16x32_bf16 v[180:183], v[224:227], v[68:71], v[180:183]\n"
"v_cvt_pk_bf16_f32 v194, v144, v145\n"
"v_cvt_pk_bf16_f32 v195, v146, v147\n"
"v_mfma_f32_16x16x32_bf16 v[180:183], v[228:231], v[84:87], v[180:183]\n"
"v_cvt_pk_bf16_f32 v198, v148, v149\n"
"v_cvt_pk_bf16_f32 v199, v150, v151\n"
"v_mfma_f32_16x16x32_bf16 v[184:187], v[224:227], v[72:75], v[184:187]\n"
"v_cvt_pk_bf16_f32 v202, v152, v153\n"
"v_cvt_pk_bf16_f32 v203, v154, v155\n"
"v_mfma_f32_16x16x32_bf16 v[184:187], v[228:231], v[88:91], v[184:187]\n"
"v_cvt_pk_bf16_f32 v206, v156, v157\n"
"v_cvt_pk_bf16_f32 v207, v158, v159\n"
"v_mfma_f32_16x16x32_bf16 v[188:191], v[224:227], v[76:79], v[188:191]\n"
"buffer_load_dwordx4 v242, s[56:59], s73 offen offset:3120 nt lds\n"
"v_mfma_f32_16x16x32_bf16 v[188:191], v[228:231], v[92:95], v[188:191]\n"
"s_waitcnt vmcnt(16) lgkmcnt(0)\n"
"s_barrier\n"
"v_mfma_f32_16x16x32_bf16 v[188:191], v[232:235], v[108:111], v[188:191]\n"
"ds_read_b64_tr_b16 v[224:225], v241 offset:0\n"
"v_mfma_f32_16x16x32_bf16 v[188:191], v[236:239], v[124:127], v[188:191]\n"
"ds_read_b64_tr_b16 v[226:227], v241 offset:512\n"
"v_mfma_f32_16x16x32_bf16 v[184:187], v[232:235], v[104:107], v[184:187]\n"
"ds_read_b64_tr_b16 v[228:229], v241 offset:32\n"
"v_mfma_f32_16x16x32_bf16 v[184:187], v[236:239], v[120:123], v[184:187]\n"
"ds_read_b64_tr_b16 v[230:231], v241 offset:544\n"
"v_mfma_f32_16x16x32_bf16 v[180:183], v[232:235], v[100:103], v[180:183]\n"
"s_add_u32 s72, s72, 16416\n"
"v_mfma_f32_16x16x32_bf16 v[180:183], v[236:239], v[116:119], v[180:183]\n"
"s_add_u32 m0, s68, 49920\n"
"v_mfma_f32_16x16x32_bf16 v[176:179], v[232:235], v[96:99], v[176:179]\n"
"buffer_load_dwordx4 v253, s[44:47], s72 offen offset:0 nt lds\n"
"v_mfma_f32_16x16x32_bf16 v[176:179], v[236:239], v[112:115], v[176:179]\n"
"s_waitcnt lgkmcnt(2)\n"
"v_mfma_f32_16x16x32_bf16 a[0:3], v[224:227], v[192:195], a[0:3]\n"
"ds_read_b64_tr_b16 v[232:233], v241 offset:64\n"
"v_max3_f32 v247, v160, v161, v162\n"
"v_mfma_f32_16x16x32_bf16 a[4:7], v[224:227], v[196:199], a[4:7]\n"
"ds_read_b64_tr_b16 v[234:235], v241 offset:576\n"
"s_waitcnt lgkmcnt(2)\n"
"v_mfma_f32_16x16x32_bf16 a[8:11], v[224:227], v[200:203], a[8:11]\n"
"ds_read_b64_tr_b16 v[236:237], v241 offset:96\n"
"v_max3_f32 v247, v247, v163, v164\n"
"v_mfma_f32_16x16x32_bf16 a[12:15], v[224:227], v[204:207], a[12:15]\n"
"ds_read_b64_tr_b16 v[238:239], v241 offset:608\n"
"v_max3_f32 v247, v247, v165, v166\n"
"v_mfma_f32_16x16x32_bf16 a[16:19], v[228:231], v[192:195], a[16:19]\n"
"ds_read_b64_tr_b16 v[224:225], v241 offset:128\n"
"v_max3_f32 v247, v247, v167, v168\n"
"v_mfma_f32_16x16x32_bf16 a[20:23], v[228:231], v[196:199], a[20:23]\n"
"ds_read_b64_tr_b16 v[226:227], v241 offset:640\n"
"v_max3_f32 v247, v247, v169, v170\n"
"v_mfma_f32_16x16x32_bf16 a[24:27], v[228:231], v[200:203], a[24:27]\n"
"buffer_load_dwordx4 v242, s[44:47], s72 offen offset:1040 nt lds\n"
"s_waitcnt lgkmcnt(4)\n"
"v_mfma_f32_16x16x32_bf16 a[28:31], v[228:231], v[204:207], a[28:31]\n"
"v_max3_f32 v247, v247, v171, v172\n"
"v_max3_f32 v247, v247, v173, v174\n"
"v_mfma_f32_16x16x32_bf16 a[32:35], v[232:235], v[192:195], a[32:35]\n"
"ds_read_b64_tr_b16 v[228:229], v241 offset:160\n"
"v_max3_f32 v247, v247, v175, v176\n"
"v_mfma_f32_16x16x32_bf16 a[36:39], v[232:235], v[196:199], a[36:39]\n"
"ds_read_b64_tr_b16 v[230:231], v241 offset:672\n"
"s_waitcnt lgkmcnt(4)\n"
"v_mfma_f32_16x16x32_bf16 a[40:43], v[232:235], v[200:203], a[40:43]\n"
"v_max3_f32 v247, v247, v177, v178\n"
"v_max3_f32 v247, v247, v179, v180\n"
"v_mfma_f32_16x16x32_bf16 a[44:47], v[232:235], v[204:207], a[44:47]\n"
"v_max3_f32 v247, v247, v181, v182\n"
"v_max3_f32 v247, v247, v183, v184\n"
"v_mfma_f32_16x16x32_bf16 a[48:51], v[236:239], v[192:195], a[48:51]\n"
"ds_read_b64_tr_b16 v[232:233], v241 offset:192\n"
"s_waitcnt lgkmcnt(3)\n"
"v_mfma_f32_16x16x32_bf16 a[52:55], v[236:239], v[196:199], a[52:55]\n"
"ds_read_b64_tr_b16 v[234:235], v241 offset:704\n"
"s_sub_u32 s72, s72, 32\n"
"v_mfma_f32_16x16x32_bf16 a[56:59], v[236:239], v[200:203], a[56:59]\n"
"buffer_load_dwordx4 v253, s[44:47], s72 offen offset:2080 nt lds\n"
"v_mfma_f32_16x16x32_bf16 a[60:63], v[236:239], v[204:207], a[60:63]\n"
"v_max3_f32 v247, v247, v185, v186\n"
"v_max3_f32 v247, v247, v187, v188\n"
"v_mfma_f32_16x16x32_bf16 a[64:67], v[224:227], v[192:195], a[64:67]\n"
"ds_read_b64_tr_b16 v[236:237], v241 offset:224\n"
"v_max3_f32 v247, v247, v189, v190\n"
"v_mfma_f32_16x16x32_bf16 a[68:71], v[224:227], v[196:199], a[68:71]\n"
"ds_read_b64_tr_b16 v[238:239], v241 offset:736\n"
"s_waitcnt lgkmcnt(4)\n"
"v_mfma_f32_16x16x32_bf16 a[72:75], v[224:227], v[200:203], a[72:75]\n"
"v_max_f32_e32 v247, v247, v191\n"
"v_cmp_lt_f32_e32 vcc, s76, v247\n"
"v_mfma_f32_16x16x32_bf16 a[76:79], v[224:227], v[204:207], a[76:79]\n"
"s_cbranch_vccnz .Lst1_L2\n"
".Lrt1_L2:\n"
"v_mfma_f32_16x16x32_bf16 a[80:83], v[228:231], v[192:195], a[80:83]\n"
"ds_read_b64_tr_b16 v[224:225], v241 offset:256\n"
"v_mfma_f32_16x16x32_bf16 a[84:87], v[228:231], v[196:199], a[84:87]\n"
"ds_read_b64_tr_b16 v[226:227], v241 offset:768\n"
"s_waitcnt lgkmcnt(4)\n"
"v_mfma_f32_16x16x32_bf16 a[88:91], v[228:231], v[200:203], a[88:91]\n"
"buffer_load_dwordx4 v242, s[44:47], s72 offen offset:3120 nt lds\n"
"v_mfma_f32_16x16x32_bf16 a[92:95], v[228:231], v[204:207], a[92:95]\n"
"v_exp_f32_e32 v160, v160\n"
"v_mfma_f32_16x16x32_bf16 a[96:99], v[232:235], v[192:195], a[96:99]\n"
"ds_read_b64_tr_b16 v[228:229], v241 offset:288\n"
"v_add_f32_e32 v243, v160, v243\n"
"v_mfma_f32_16x16x32_bf16 a[100:103], v[232:235], v[196:199], a[100:103]\n"
"ds_read_b64_tr_b16 v[230:231], v241 offset:800\n"
"s_waitcnt lgkmcnt(4)\n"
"v_mfma_f32_16x16x32_bf16 a[104:107], v[232:235], v[200:203], a[104:107]\n"
"v_exp_f32_e32 v161, v161\n"
"v_mfma_f32_16x16x32_bf16 a[108:111], v[232:235], v[204:207], a[108:111]\n"
"v_exp_f32_e32 v162, v162\n"
"v_mfma_f32_16x16x32_bf16 a[112:115], v[236:239], v[192:195], a[112:115]\n"
"ds_read_b64_tr_b16 v[232:233], v241 offset:320\n"
"s_add_u32 s72, s72, 32\n"
"s_waitcnt lgkmcnt(3)\n"
"v_mfma_f32_16x16x32_bf16 a[116:119], v[236:239], v[196:199], a[116:119]\n"
"ds_read_b64_tr_b16 v[234:235], v241 offset:832\n"
"s_add_u32 m0, s68, 54080\n"
"v_mfma_f32_16x16x32_bf16 a[120:123], v[236:239], v[200:203], a[120:123]\n"
"buffer_load_dwordx4 v253, s[48:51], s72 offen offset:0 nt lds\n"
"v_mfma_f32_16x16x32_bf16 a[124:127], v[236:239], v[204:207], a[124:127]\n"
"v_exp_f32_e32 v163, v163\n"
"v_mfma_f32_16x16x32_bf16 a[128:131], v[224:227], v[192:195], a[128:131]\n"
"ds_read_b64_tr_b16 v[236:237], v241 offset:352\n"
"v_add_f32_e32 v243, v161, v243\n"
"v_mfma_f32_16x16x32_bf16 a[132:135], v[224:227], v[196:199], a[132:135]\n"
"ds_read_b64_tr_b16 v[238:239], v241 offset:864\n"
"s_waitcnt lgkmcnt(4)\n"
"v_mfma_f32_16x16x32_bf16 a[136:139], v[224:227], v[200:203], a[136:139]\n"
"v_exp_f32_e32 v164, v164\n"
"v_mfma_f32_16x16x32_bf16 a[140:143], v[224:227], v[204:207], a[140:143]\n"
"v_exp_f32_e32 v165, v165\n"
"v_mfma_f32_16x16x32_bf16 a[144:147], v[228:231], v[192:195], a[144:147]\n"
"ds_read_b64_tr_b16 v[224:225], v241 offset:384\n"
"v_add_f32_e32 v243, v162, v243\n"
"v_mfma_f32_16x16x32_bf16 a[148:151], v[228:231], v[196:199], a[148:151]\n"
"ds_read_b64_tr_b16 v[226:227], v241 offset:896\n"
"s_waitcnt lgkmcnt(4)\n"
"v_mfma_f32_16x16x32_bf16 a[152:155], v[228:231], v[200:203], a[152:155]\n"
"buffer_load_dwordx4 v242, s[48:51], s72 offen offset:1040 nt lds\n"
"v_mfma_f32_16x16x32_bf16 a[156:159], v[228:231], v[204:207], a[156:159]\n"
"v_exp_f32_e32 v166, v166\n"
"v_mfma_f32_16x16x32_bf16 a[160:163], v[232:235], v[192:195], a[160:163]\n"
"ds_read_b64_tr_b16 v[228:229], v241 offset:416\n"
"v_add_f32_e32 v243, v163, v243\n"
"v_mfma_f32_16x16x32_bf16 a[164:167], v[232:235], v[196:199], a[164:167]\n"
"ds_read_b64_tr_b16 v[230:231], v241 offset:928\n"
"s_waitcnt lgkmcnt(4)\n"
"v_mfma_f32_16x16x32_bf16 a[168:171], v[232:235], v[200:203], a[168:171]\n"
"v_exp_f32_e32 v167, v167\n"
"v_mfma_f32_16x16x32_bf16 a[172:175], v[232:235], v[204:207], a[172:175]\n"
"v_exp_f32_e32 v168, v168\n"
"v_mfma_f32_16x16x32_bf16 a[176:179], v[236:239], v[192:195], a[176:179]\n"
"ds_read_b64_tr_b16 v[232:233], v241 offset:448\n"
"s_waitcnt lgkmcnt(3)\n"
"v_mfma_f32_16x16x32_bf16 a[180:183], v[236:239], v[196:199], a[180:183]\n"
"ds_read_b64_tr_b16 v[234:235], v241 offset:960\n"
"s_sub_u32 s72, s72, 32\n"
"v_mfma_f32_16x16x32_bf16 a[184:187], v[236:239], v[200:203], a[184:187]\n"
"buffer_load_dwordx4 v253, s[48:51], s72 offen offset:2080 nt lds\n"
"v_mfma_f32_16x16x32_bf16 a[188:191], v[236:239], v[204:207], a[188:191]\n"
"v_exp_f32_e32 v169, v169\n"
"v_mfma_f32_16x16x32_bf16 a[192:195], v[224:227], v[192:195], a[192:195]\n"
"ds_read_b64_tr_b16 v[236:237], v241 offset:480\n"
"v_add_f32_e32 v244, v164, v244\n"
"v_mfma_f32_16x16x32_bf16 a[196:199], v[224:227], v[196:199], a[196:199]\n"
"ds_read_b64_tr_b16 v[238:239], v241 offset:992\n"
"s_waitcnt lgkmcnt(4)\n"
"v_mfma_f32_16x16x32_bf16 a[200:203], v[224:227], v[200:203], a[200:203]\n"
"v_exp_f32_e32 v170, v170\n"
"v_mfma_f32_16x16x32_bf16 a[204:207], v[224:227], v[204:207], a[204:207]\n"
"v_exp_f32_e32 v171, v171\n"
"v_mfma_f32_16x16x32_bf16 a[208:211], v[228:231], v[192:195], a[208:211]\n"
"v_exp_f32_e32 v172, v172\n"
"v_mfma_f32_16x16x32_bf16 a[212:215], v[228:231], v[196:199], a[212:215]\n"
"v_exp_f32_e32 v173, v173\n"
"v_mfma_f32_16x16x32_bf16 a[216:219], v[228:231], v[200:203], a[216:219]\n"
"buffer_load_dwordx4 v242, s[48:51], s72 offen offset:3120 nt lds\n"
"v_mfma_f32_16x16x32_bf16 a[220:223], v[228:231], v[204:207], a[220:223]\n"
"s_waitcnt vmcnt(16) lgkmcnt(0)\n"
"s_barrier\n"
"v_mfma_f32_16x16x32_bf16 a[224:227], v[232:235], v[192:195], a[224:227]\n"
"ds_read_b128 v[224:227], v240 offset:16640\n"
"v_add_f32_e32 v244, v165, v244\n"
"v_mfma_f32_16x16x32_bf16 a[228:231], v[232:235], v[196:199], a[228:231]\n"
"ds_read_b128 v[228:231], v240 offset:16672\n"
"v_add_f32_e32 v244, v166, v244\n"
"v_mfma_f32_16x16x32_bf16 a[232:235], v[232:235], v[200:203], a[232:235]\n"
"v_exp_f32_e32 v174, v174\n"
"v_mfma_f32_16x16x32_bf16 a[236:239], v[232:235], v[204:207], a[236:239]\n"
"v_exp_f32_e32 v175, v175\n"
"v_mfma_f32_16x16x32_bf16 a[240:243], v[236:239], v[192:195], a[240:243]\n"
"ds_read_b128 v[232:235], v240 offset:16768\n"
"s_add_u32 s73, s73, 16416\n"
"v_mfma_f32_16x16x32_bf16 a[244:247], v[236:239], v[196:199], a[244:247]\n"
"s_add_u32 m0, s68, 0\n"
"v_add_f32_e32 v244, v167, v244\n"
"v_mfma_f32_16x16x32_bf16 a[248:251], v[236:239], v[200:203], a[248:251]\n"
"buffer_load_dwordx4 v253, s[52:55], s73 offen offset:0 nt lds\n"
"v_mfma_f32_16x16x32_bf16 a[252:255], v[236:239], v[204:207], a[252:255]\n"
"s_waitcnt lgkmcnt(1)\n"
"v_add_f32_e32 v245, v168, v245\n"
"v_mfma_f32_16x16x32_bf16 v[128:131], v[224:227], v[0:3], v[208:211]\n"
"s_cbranch_vccnz .Lst2_L3\n"
".Lrt2_L3:\n"
"v_mfma_f32_16x16x32_bf16 v[128:131], v[228:231], v[16:19], v[128:131]\n"
"ds_read_b128 v[236:239], v240 offset:16800\n"
"v_add_f32_e32 v245, v169, v245\n"
"v_mfma_f32_16x16x32_bf16 v[132:135], v[224:227], v[4:7], v[212:215]\n"
"v_exp_f32_e32 v176, v176\n"
"v_mfma_f32_16x16x32_bf16 v[132:135], v[228:231], v[20:23], v[132:135]\n"
"v_exp_f32_e32 v177, v177\n"
"v_mfma_f32_16x16x32_bf16 v[136:139], v[224:227], v[8:11], v[216:219]\n"
"v_exp_f32_e32 v178, v178\n"
"v_mfma_f32_16x16x32_bf16 v[136:139], v[228:231], v[24:27], v[136:139]\n"
"v_exp_f32_e32 v179, v179\n"
"v_mfma_f32_16x16x32_bf16 v[140:143], v[224:227], v[12:15], v[220:223]\n"
"buffer_load_dwordx4 v242, s[52:55], s73 offen offset:1040 nt lds\n"
"v_mfma_f32_16x16x32_bf16 v[140:143], v[228:231], v[28:31], v[140:143]\n"
"s_waitcnt lgkmcnt(0)\n"
"v_add_f32_e32 v245, v170, v245\n"
"v_mfma_f32_16x16x32_bf16 v[140:143], v[232:235], v[44:47], v[140:143]\n"
"ds_read_b128 v[224:227], v240 offset:16896\n"
"v_add_f32_e32 v245, v171, v245\n"
"v_mfma_f32_16x16x32_bf16 v[140:143], v[236:239], v[60:63], v[140:143]\n"
"ds_read_b128 v[228:231], v240 offset:16928\n"
"v_add_f32_e32 v246, v172, v246\n"
"v_mfma_f32_16x16x32_bf16 v[136:139], v[232:235], v[40:43], v[136:139]\n"
"v_exp_f32_e32 v180, v180\n"
"v_mfma_f32_16x16x32_bf16 v[136:139], v[236:239], v[56:59], v[136:139]\n"
"v_exp_f32_e32 v181, v181\n"
"v_mfma_f32_16x16x32_bf16 v[132:135], v[232:235], v[36:39], v[132:135]\n"
"v_exp_f32_e32 v182, v182\n"
"v_mfma_f32_16x16x32_bf16 v[132:135], v[236:239], v[52:55], v[132:135]\n"
"s_sub_u32 s73, s73, 32\n"
"v_add_f32_e32 v246, v173, v246\n"
"v_mfma_f32_16x16x32_bf16 v[128:131], v[232:235], v[32:35], v[128:131]\n"
"buffer_load_dwordx4 v253, s[52:55], s73 offen offset:2080 nt lds\n"
"v_mfma_f32_16x16x32_bf16 v[128:131], v[236:239], v[48:51], v[128:131]\n"
"s_waitcnt lgkmcnt(0)\n"
"v_add_f32_e32 v246, v174, v246\n"
"v_mfma_f32_16x16x32_bf16 v[128:131], v[224:227], v[64:67], v[128:131]\n"
"ds_read_b128 v[232:235], v240 offset:17024\n"
"v_add_f32_e32 v246, v175, v246\n"
"v_mfma_f32_16x16x32_bf16 v[128:131], v[228:231], v[80:83], v[128:131]\n"
"ds_read_b128 v[236:239], v240 offset:17056\n"
"v_add_f32_e32 v243, v176, v243\n"
"v_mfma_f32_16x16x32_bf16 v[132:135], v[224:227], v[68:71], v[132:135]\n"
"v_exp_f32_e32 v183, v183\n"
"v_mfma_f32_16x16x32_bf16 v[132:135], v[228:231], v[84:87], v[132:135]\n"
"v_exp_f32_e32 v184, v184\n"
"v_mfma_f32_16x16x32_bf16 v[136:139], v[224:227], v[72:75], v[136:139]\n"
"v_exp_f32_e32 v185, v185\n"
"v_mfma_f32_16x16x32_bf16 v[136:139], v[228:231], v[88:91], v[136:139]\n"
"v_exp_f32_e32 v186, v186\n"
"v_mfma_f32_16x16x32_bf16 v[140:143], v[224:227], v[76:79], v[140:143]\n"
"buffer_load_dwordx4 v242, s[52:55], s73 offen offset:3120 nt lds\n"
"v_mfma_f32_16x16x32_bf16 v[140:143], v[228:231], v[92:95], v[140:143]\n"
"s_waitcnt lgkmcnt(0)\n"
"v_add_f32_e32 v243, v177, v243\n"
"v_mfma_f32_16x16x32_bf16 v[140:143], v[232:235], v[108:111], v[140:143]\n"
"ds_read_b128 v[224:227], v240 offset:17152\n"
"v_add_f32_e32 v243, v178, v243\n"
"v_mfma_f32_16x16x32_bf16 v[140:143], v[236:239], v[124:127], v[140:143]\n"
"ds_read_b128 v[228:231], v240 offset:17184\n"
"v_add_f32_e32 v243, v179, v243\n"
"v_mfma_f32_16x16x32_bf16 v[136:139], v[232:235], v[104:107], v[136:139]\n"
"v_exp_f32_e32 v187, v187\n"
"v_mfma_f32_16x16x32_bf16 v[136:139], v[236:239], v[120:123], v[136:139]\n"
"v_exp_f32_e32 v188, v188\n"
"v_mfma_f32_16x16x32_bf16 v[132:135], v[232:235], v[100:103], v[132:135]\n"
"s_add_u32 s73, s73, 32\n"
"v_add_f32_e32 v244, v180, v244\n"
"v_mfma_f32_16x16x32_bf16 v[132:135], v[236:239], v[116:119], v[132:135]\n"
"s_add_u32 m0, s68, 4160\n"
"v_add_f32_e32 v244, v181, v244\n"
"v_mfma_f32_16x16x32_bf16 v[128:131], v[232:235], v[96:99], v[128:131]\n"
"buffer_load_dwordx4 v253, s[56:59], s73 offen offset:0 nt lds\n"
"v_mfma_f32_16x16x32_bf16 v[128:131], v[236:239], v[112:115], v[128:131]\n"
"s_waitcnt lgkmcnt(0)\n"
"v_add_f32_e32 v244, v182, v244\n"
"v_mfma_f32_16x16x32_bf16 v[144:147], v[224:227], v[0:3], v[208:211]\n"
"ds_read_b128 v[232:235], v240 offset:17280\n"
"v_add_f32_e32 v244, v183, v244\n"
"v_mfma_f32_16x16x32_bf16 v[144:147], v[228:231], v[16:19], v[144:147]\n"
"ds_read_b128 v[236:239], v240 offset:17312\n"
"v_add_f32_e32 v245, v184, v245\n"
"v_mfma_f32_16x16x32_bf16 v[148:151], v[224:227], v[4:7], v[212:215]\n"
"v_exp_f32_e32 v189, v189\n"
"v_mfma_f32_16x16x32_bf16 v[148:151], v[228:231], v[20:23], v[148:151]\n"
"v_exp_f32_e32 v190, v190\n"
"v_mfma_f32_16x16x32_bf16 v[152:155], v[224:227], v[8:11], v[216:219]\n"
"v_exp_f32_e32 v191, v191\n"
"v_mfma_f32_16x16x32_bf16 v[152:155], v[228:231], v[24:27], v[152:155]\n"
"v_add_f32_e32 v245, v185, v245\n"
"v_add_f32_e32 v245, v186, v245\n"
"v_mfma_f32_16x16x32_bf16 v[156:159], v[224:227], v[12:15], v[220:223]\n"
"buffer_load_dwordx4 v242, s[56:59], s73 offen offset:1040 nt lds\n"
"v_mfma_f32_16x16x32_bf16 v[156:159], v[228:231], v[28:31], v[156:159]\n"
"s_waitcnt lgkmcnt(0)\n"
"v_add_f32_e32 v245, v187, v245\n"
"v_mfma_f32_16x16x32_bf16 v[156:159], v[232:235], v[44:47], v[156:159]\n"
"ds_read_b128 v[224:227], v240 offset:17408\n"
"v_add_f32_e32 v246, v188, v246\n"
"v_mfma_f32_16x16x32_bf16 v[156:159], v[236:239], v[60:63], v[156:159]\n"
"ds_read_b128 v[228:231], v240 offset:17440\n"
"v_add_f32_e32 v246, v189, v246\n"
"v_mfma_f32_16x16x32_bf16 v[152:155], v[232:235], v[40:43], v[152:155]\n"
"v_add_f32_e32 v246, v190, v246\n"
"v_add_f32_e32 v246, v191, v246\n"
"v_mfma_f32_16x16x32_bf16 v[152:155], v[236:239], v[56:59], v[152:155]\n"
"v_cvt_pk_bf16_f32 v192, v160, v161\n"
"v_cvt_pk_bf16_f32 v193, v162, v163\n"
"v_mfma_f32_16x16x32_bf16 v[148:151], v[232:235], v[36:39], v[148:151]\n"
"v_cvt_pk_bf16_f32 v196, v164, v165\n"
"v_cvt_pk_bf16_f32 v197, v166, v167\n"
"v_mfma_f32_16x16x32_bf16 v[148:151], v[236:239], v[52:55], v[148:151]\n"
"s_sub_u32 s73, s73, 32\n"
"v_cvt_pk_bf16_f32 v200, v168, v169\n"
"v_mfma_f32_16x16x32_bf16 v[144:147], v[232:235], v[32:35], v[144:147]\n"
"buffer_load_dwordx4 v253, s[56:59], s73 offen offset:2080 nt lds\n"
"v_mfma_f32_16x16x32_bf16 v[144:147], v[236:239], v[48:51], v[144:147]\n"
"s_waitcnt lgkmcnt(0)\n"
"v_cvt_pk_bf16_f32 v201, v170, v171\n"
"v_mfma_f32_16x16x32_bf16 v[144:147], v[224:227], v[64:67], v[144:147]\n"
"ds_read_b128 v[232:235], v240 offset:17536\n"
"v_cvt_pk_bf16_f32 v204, v172, v173\n"
"v_mfma_f32_16x16x32_bf16 v[144:147], v[228:231], v[80:83], v[144:147]\n"
"ds_read_b128 v[236:239], v240 offset:17568\n"
"v_cvt_pk_bf16_f32 v205, v174, v175\n"
"v_mfma_f32_16x16x32_bf16 v[148:151], v[224:227], v[68:71], v[148:151]\n"
"v_cvt_pk_bf16_f32 v194, v176, v177\n"
"v_cvt_pk_bf16_f32 v195, v178, v179\n"
"v_mfma_f32_16x16x32_bf16 v[148:151], v[228:231], v[84:87], v[148:151]\n"
"v_cvt_pk_bf16_f32 v198, v180, v181\n"
"v_cvt_pk_bf16_f32 v199, v182, v183\n"
"v_mfma_f32_16x16x32_bf16 v[152:155], v[224:227], v[72:75], v[152:155]\n"
"v_cvt_pk_bf16_f32 v202, v184, v185\n"
"v_cvt_pk_bf16_f32 v203, v186, v187\n"
"v_mfma_f32_16x16x32_bf16 v[152:155], v[228:231], v[88:91], v[152:155]\n"
"v_cvt_pk_bf16_f32 v206, v188, v189\n"
"v_cvt_pk_bf16_f32 v207, v190, v191\n"
"v_mfma_f32_16x16x32_bf16 v[156:159], v[224:227], v[76:79], v[156:159]\n"
"buffer_load_dwordx4 v242, s[56:59], s73 offen offset:3120 nt lds\n"
"v_mfma_f32_16x16x32_bf16 v[156:159], v[228:231], v[92:95], v[156:159]\n"
"s_waitcnt vmcnt(16) lgkmcnt(0)\n"
"s_barrier\n"
"v_mfma_f32_16x16x32_bf16 v[156:159], v[232:235], v[108:111], v[156:159]\n"
"ds_read_b64_tr_b16 v[224:225], v241 offset:33280\n"
"v_mfma_f32_16x16x32_bf16 v[156:159], v[236:239], v[124:127], v[156:159]\n"
"ds_read_b64_tr_b16 v[226:227], v241 offset:33792\n"
"v_mfma_f32_16x16x32_bf16 v[152:155], v[232:235], v[104:107], v[152:155]\n"
"ds_read_b64_tr_b16 v[228:229], v241 offset:33312\n"
"v_mfma_f32_16x16x32_bf16 v[152:155], v[236:239], v[120:123], v[152:155]\n"
"ds_read_b64_tr_b16 v[230:231], v241 offset:33824\n"
"v_mfma_f32_16x16x32_bf16 v[148:151], v[232:235], v[100:103], v[148:151]\n"
"s_add_u32 s72, s72, 16416\n"
"v_mfma_f32_16x16x32_bf16 v[148:151], v[236:239], v[116:119], v[148:151]\n"
"s_add_u32 m0, s68, 16640\n"
"v_mfma_f32_16x16x32_bf16 v[144:147], v[232:235], v[96:99], v[144:147]\n"
"buffer_load_dwordx4 v253, s[44:47], s72 offen offset:0 nt lds\n"
"v_mfma_f32_16x16x32_bf16 v[144:147], v[236:239], v[112:115], v[144:147]\n"
"s_waitcnt lgkmcnt(2)\n"
"s_sub_u32 s75, s75, 1\n"
"s_cbranch_scc0 .Lloop\n"
"v_mfma_f32_16x16x32_bf16 a[0:3], v[224:227], v[192:195], a[0:3]\n"
"ds_read_b64_tr_b16 v[232:233], v241 offset:33344\n"
"v_max3_f32 v247, v128, v129, v130\n"
"v_mfma_f32_16x16x32_bf16 a[4:7], v[224:227], v[196:199], a[4:7]\n"
"ds_read_b64_tr_b16 v[234:235], v241 offset:33856\n"
"s_waitcnt lgkmcnt(2)\n"
"v_mfma_f32_16x16x32_bf16 a[8:11], v[224:227], v[200:203], a[8:11]\n"
"ds_read_b64_tr_b16 v[236:237], v241 offset:33376\n"
"v_max3_f32 v247, v247, v131, v132\n"
"v_mfma_f32_16x16x32_bf16 a[12:15], v[224:227], v[204:207], a[12:15]\n"
"ds_read_b64_tr_b16 v[238:239], v241 offset:33888\n"
"v_max3_f32 v247, v247, v133, v134\n"
"v_mfma_f32_16x16x32_bf16 a[16:19], v[228:231], v[192:195], a[16:19]\n"
"ds_read_b64_tr_b16 v[224:225], v241 offset:33408\n"
"v_max3_f32 v247, v247, v135, v136\n"
"v_mfma_f32_16x16x32_bf16 a[20:23], v[228:231], v[196:199], a[20:23]\n"
"ds_read_b64_tr_b16 v[226:227], v241 offset:33920\n"
"v_max3_f32 v247, v247, v137, v138\n"
"v_mfma_f32_16x16x32_bf16 a[24:27], v[228:231], v[200:203], a[24:27]\n"
"s_waitcnt lgkmcnt(4)\n"
"v_max3_f32 v247, v247, v139, v140\n"
"v_mfma_f32_16x16x32_bf16 a[28:31], v[228:231], v[204:207], a[28:31]\n"
"v_max3_f32 v247, v247, v141, v142\n"
"v_max3_f32 v247, v247, v143, v144\n"
"v_mfma_f32_16x16x32_bf16 a[32:35], v[232:235], v[192:195], a[32:35]\n"
"ds_read_b64_tr_b16 v[228:229], v241 offset:33440\n"
"v_max3_f32 v247, v247, v145, v146\n"
"v_mfma_f32_16x16x32_bf16 a[36:39], v[232:235], v[196:199], a[36:39]\n"
"ds_read_b64_tr_b16 v[230:231], v241 offset:33952\n"
"s_waitcnt lgkmcnt(4)\n"
"v_mfma_f32_16x16x32_bf16 a[40:43], v[232:235], v[200:203], a[40:43]\n"
"v_max3_f32 v247, v247, v147, v148\n"
"v_max3_f32 v247, v247, v149, v150\n"
"v_mfma_f32_16x16x32_bf16 a[44:47], v[232:235], v[204:207], a[44:47]\n"
"v_max3_f32 v247, v247, v151, v152\n"
"v_max3_f32 v247, v247, v153, v154\n"
"v_mfma_f32_16x16x32_bf16 a[48:51], v[236:239], v[192:195], a[48:51]\n"
"ds_read_b64_tr_b16 v[232:233], v241 offset:33472\n"
"v_max3_f32 v247, v247, v155, v156\n"
"v_mfma_f32_16x16x32_bf16 a[52:55], v[236:239], v[196:199], a[52:55]\n"
"ds_read_b64_tr_b16 v[234:235], v241 offset:33984\n"
"s_waitcnt lgkmcnt(4)\n"
"v_mfma_f32_16x16x32_bf16 a[56:59], v[236:239], v[200:203], a[56:59]\n"
"v_max3_f32 v247, v247, v157, v158\n"
"v_max_f32_e32 v247, v247, v159\n"
"v_mfma_f32_16x16x32_bf16 a[60:63], v[236:239], v[204:207], a[60:63]\n"
"v_cmp_lt_f32_e32 vcc, s76, v247\n"
"v_mfma_f32_16x16x32_bf16 a[64:67], v[224:227], v[192:195], a[64:67]\n"
"ds_read_b64_tr_b16 v[236:237], v241 offset:33504\n"
"v_mfma_f32_16x16x32_bf16 a[68:71], v[224:227], v[196:199], a[68:71]\n"
"ds_read_b64_tr_b16 v[238:239], v241 offset:34016\n"
"s_waitcnt lgkmcnt(4)\n"
"v_mfma_f32_16x16x32_bf16 a[72:75], v[224:227], v[200:203], a[72:75]\n"
"s_cbranch_vccnz .Lst1_42\n"
".Lrt1_42:\n"
"v_mfma_f32_16x16x32_bf16 a[76:79], v[224:227], v[204:207], a[76:79]\n"
"v_exp_f32_e32 v128, v128\n"
"v_mfma_f32_16x16x32_bf16 a[80:83], v[228:231], v[192:195], a[80:83]\n"
"ds_read_b64_tr_b16 v[224:225], v241 offset:33536\n"
"v_add_f32_e32 v243, v128, v243\n"
"v_mfma_f32_16x16x32_bf16 a[84:87], v[228:231], v[196:199], a[84:87]\n"
"ds_read_b64_tr_b16 v[226:227], v241 offset:34048\n"
"s_waitcnt lgkmcnt(4)\n"
"v_mfma_f32_16x16x32_bf16 a[88:91], v[228:231], v[200:203], a[88:91]\n"
"v_exp_f32_e32 v129, v129\n"
"v_mfma_f32_16x16x32_bf16 a[92:95], v[228:231], v[204:207], a[92:95]\n"
"v_exp_f32_e32 v130, v130\n"
"v_mfma_f32_16x16x32_bf16 a[96:99], v[232:235], v[192:195], a[96:99]\n"
"ds_read_b64_tr_b16 v[228:229], v241 offset:33568\n"
"v_add_f32_e32 v243, v129, v243\n"
"v_mfma_f32_16x16x32_bf16 a[100:103], v[232:235], v[196:199], a[100:103]\n"
"ds_read_b64_tr_b16 v[230:231], v241 offset:34080\n"
"s_waitcnt lgkmcnt(4)\n"
"v_mfma_f32_16x16x32_bf16 a[104:107], v[232:235], v[200:203], a[104:107]\n"
"v_exp_f32_e32 v131, v131\n"
"v_mfma_f32_16x16x32_bf16 a[108:111], v[232:235], v[204:207], a[108:111]\n"
"v_exp_f32_e32 v132, v132\n"
"v_mfma_f32_16x16x32_bf16 a[112:115], v[236:239], v[192:195], a[112:115]\n"
"ds_read_b64_tr_b16 v[232:233], v241 offset:33600\n"
"v_add_f32_e32 v243, v130, v243\n"
"v_mfma_f32_16x16x32_bf16 a[116:119], v[236:239], v[196:199], a[116:119]\n"
"ds_read_b64_tr_b16 v[234:235], v241 offset:34112\n"
"s_waitcnt lgkmcnt(4)\n"
"v_mfma_f32_16x16x32_bf16 a[120:123], v[236:239], v[200:203], a[120:123]\n"
"v_exp_f32_e32 v133, v133\n"
"v_mfma_f32_16x16x32_bf16 a[124:127], v[236:239], v[204:207], a[124:127]\n"
"v_exp_f32_e32 v134, v134\n"
"v_mfma_f32_16x16x32_bf16 a[128:131], v[224:227], v[192:195], a[128:131]\n"
"ds_read_b64_tr_b16 v[236:237], v241 offset:33632\n"
"v_add_f32_e32 v243, v131, v243\n"
"v_mfma_f32_16x16x32_bf16 a[132:135], v[224:227], v[196:199], a[132:135]\n"
"ds_read_b64_tr_b16 v[238:239], v241 offset:34144\n"
"s_waitcnt lgkmcnt(4)\n"
"v_mfma_f32_16x16x32_bf16 a[136:139], v[224:227], v[200:203], a[136:139]\n"
"v_exp_f32_e32 v135, v135\n"
"v_mfma_f32_16x16x32_bf16 a[140:143], v[224:227], v[204:207], a[140:143]\n"
"v_exp_f32_e32 v136, v136\n"
"v_mfma_f32_16x16x32_bf16 a[144:147], v[228:231], v[192:195], a[144:147]\n"
"ds_read_b64_tr_b16 v[224:225], v241 offset:33664\n"
"v_add_f32_e32 v244, v132, v244\n"
"v_mfma_f32_16x16x32_bf16 a[148:151], v[228:231], v[196:199], a[148:151]\n"
"ds_read_b64_tr_b16 v[226:227], v241 offset:34176\n"
"s_waitcnt lgkmcnt(4)\n"
"v_mfma_f32_16x16x32_bf16 a[152:155], v[228:231], v[200:203], a[152:155]\n"
"v_exp_f32_e32 v137, v137\n"
"v_mfma_f32_16x16x32_bf16 a[156:159], v[228:231], v[204:207], a[156:159]\n"
"v_exp_f32_e32 v138, v138\n"
"v_mfma_f32_16x16x32_bf16 a[160:163], v[232:235], v[192:195], a[160:163]\n"
"ds_read_b64_tr_b16 v[228:229], v241 offset:33696\n"
"v_add_f32_e32 v244, v133, v244\n"
"v_mfma_f32_16x16x32_bf16 a[164:167], v[232:235], v[196:199], a[164:167]\n"
"ds_read_b64_tr_b16 v[230:231], v241 offset:34208\n"
"s_waitcnt lgkmcnt(4)\n"
"v_mfma_f32_16x16x32_bf16 a[168:171], v[232:235], v[200:203], a[168:171]\n"
"v_exp_f32_e32 v139, v139\n"
"v_mfma_f32_16x16x32_bf16 a[172:175], v[232:235], v[204:207], a[172:175]\n"
"v_exp_f32_e32 v140, v140\n"
"v_mfma_f32_16x16x32_bf16 a[176:179], v[236:239], v[192:195], a[176:179]\n"
"ds_read_b64_tr_b16 v[232:233], v241 offset:33728\n"
"v_add_f32_e32 v244, v134, v244\n"
"v_mfma_f32_16x16x32_bf16 a[180:183], v[236:239], v[196:199], a[180:183]\n"
"ds_read_b64_tr_b16 v[234:235], v241 offset:34240\n"
"s_waitcnt lgkmcnt(4)\n"
"v_mfma_f32_16x16x32_bf16 a[184:187], v[236:239], v[200:203], a[184:187]\n"
"v_exp_f32_e32 v141, v141\n"
"v_mfma_f32_16x16x32_bf16 a[188:191], v[236:239], v[204:207], a[188:191]\n"
"v_exp_f32_e32 v142, v142\n"
"v_mfma_f32_16x16x32_bf16 a[192:195], v[224:227], v[192:195], a[192:195]\n"
"ds_read_b64_tr_b16 v[236:237], v241 offset:33760\n"
"v_add_f32_e32 v244, v135, v244\n"
"v_mfma_f32_16x16x32_bf16 a[196:199], v[224:227], v[196:199], a[196:199]\n"
"ds_read_b64_tr_b16 v[238:239], v241 offset:34272\n"
"s_waitcnt lgkmcnt(4)\n"
"v_mfma_f32_16x16x32_bf16 a[200:203], v[224:227], v[200:203], a[200:203]\n"
"v_exp_f32_e32 v143, v143\n"
"v_mfma_f32_16x16x32_bf16 a[204:207], v[224:227], v[204:207], a[204:207]\n"
"v_exp_f32_e32 v144, v144\n"
"v_mfma_f32_16x16x32_bf16 a[208:211], v[228:231], v[192:195], a[208:211]\n"
"v_exp_f32_e32 v145, v145\n"
"v_mfma_f32_16x16x32_bf16 a[212:215], v[228:231], v[196:199], a[212:215]\n"
"v_exp_f32_e32 v146, v146\n"
"v_mfma_f32_16x16x32_bf16 a[216:219], v[228:231], v[200:203], a[216:219]\n"
"v_exp_f32_e32 v147, v147\n"
"v_mfma_f32_16x16x32_bf16 a[220:223], v[228:231], v[204:207], a[220:223]\n"
"s_branch .Lpo7\n"
".Lpg7:\n"
"s_setpc_b64 s[78:79]\n"
".Lpo7:\n"
"s_waitcnt vmcnt(9) lgkmcnt(0)\n"
"s_barrier\n"
"v_mfma_f32_16x16x32_bf16 a[224:227], v[232:235], v[192:195], a[224:227]\n"
"ds_read_b128 v[224:227], v240 offset:49920\n"
"v_add_f32_e32 v245, v136, v245\n"
"v_mfma_f32_16x16x32_bf16 a[228:231], v[232:235], v[196:199], a[228:231]\n"
"ds_read_b128 v[228:231], v240 offset:49952\n"
"v_add_f32_e32 v245, v137, v245\n"
"v_mfma_f32_16x16x32_bf16 a[232:235], v[232:235], v[200:203], a[232:235]\n"
"v_exp_f32_e32 v148, v148\n"
"v_mfma_f32_16x16x32_bf16 a[236:239], v[232:235], v[204:207], a[236:239]\n"
"v_exp_f32_e32 v149, v149\n"
"v_mfma_f32_16x16x32_bf16 a[240:243], v[236:239], v[192:195], a[240:243]\n"
"ds_read_b128 v[232:235], v240 offset:50048\n"
"s_add_u32 s73, s73, 16416\n"
"v_mfma_f32_16x16x32_bf16 a[244:247], v[236:239], v[196:199], a[244:247]\n"
"s_add_u32 m0, s68, 33280\n"
"v_add_f32_e32 v245, v138, v245\n"
"v_mfma_f32_16x16x32_bf16 a[248:251], v[236:239], v[200:203], a[248:251]\n"
"buffer_load_dwordx4 v253, s[52:55], s73 offen offset:0 nt lds\n"
"v_mfma_f32_16x16x32_bf16 a[252:255], v[236:239], v[204:207], a[252:255]\n"
"s_waitcnt lgkmcnt(1)\n"
"v_add_f32_e32 v245, v139, v245\n"
"v_mfma_f32_16x16x32_bf16 v[160:163], v[224:227], v[0:3], v[208:211]\n"
"s_cbranch_vccnz .Lst2_43\n"
".Lrt2_43:\n"
"v_mfma_f32_16x16x32_bf16 v[160:163], v[228:231], v[16:19], v[160:163]\n"
"ds_read_b128 v[236:239], v240 offset:50080\n"
"v_add_f32_e32 v246, v140, v246\n"
"v_mfma_f32_16x16x32_bf16 v[164:167], v[224:227], v[4:7], v[212:215]\n"
"v_exp_f32_e32 v150, v150\n"
"v_mfma_f32_16x16x32_bf16 v[164:167], v[228:231], v[20:23], v[164:167]\n"
"v_exp_f32_e32 v151, v151\n"
"v_mfma_f32_16x16x32_bf16 v[168:171], v[224:227], v[8:11], v[216:219]\n"
"v_exp_f32_e32 v152, v152\n"
"v_mfma_f32_16x16x32_bf16 v[168:171], v[228:231], v[24:27], v[168:171]\n"
"v_exp_f32_e32 v153, v153\n"
"v_mfma_f32_16x16x32_bf16 v[172:175], v[224:227], v[12:15], v[220:223]\n"
"buffer_load_dwordx4 v242, s[52:55], s73 offen offset:1040 nt lds\n"
"v_mfma_f32_16x16x32_bf16 v[172:175], v[228:231], v[28:31], v[172:175]\n"
"s_waitcnt lgkmcnt(0)\n"
"v_add_f32_e32 v246, v141, v246\n"
"v_mfma_f32_16x16x32_bf16 v[172:175], v[232:235], v[44:47], v[172:175]\n"
"ds_read_b128 v[224:227], v240 offset:50176\n"
"v_add_f32_e32 v246, v142, v246\n"
"v_mfma_f32_16x16x32_bf16 v[172:175], v[236:239], v[60:63], v[172:175]\n"
"ds_read_b128 v[228:231], v240 offset:50208\n"
"v_add_f32_e32 v246, v143, v246\n"
"v_mfma_f32_16x16x32_bf16 v[168:171], v[232:235], v[40:43], v[168:171]\n"
"v_exp_f32_e32 v154, v154\n"
"v_mfma_f32_16x16x32_bf16 v[168:171], v[236:239], v[56:59], v[168:171]\n"
"v_exp_f32_e32 v155, v155\n"
"v_mfma_f32_16x16x32_bf16 v[164:167], v[232:235], v[36:39], v[164:167]\n"
"v_exp_f32_e32 v156, v156\n"
"v_mfma_f32_16x16x32_bf16 v[164:167], v[236:239], v[52:55], v[164:167]\n"
"s_sub_u32 s73, s73, 32\n"
"v_add_f32_e32 v243, v144, v243\n"
"v_mfma_f32_16x16x32_bf16 v[160:163], v[232:235], v[32:35], v[160:163]\n"
"buffer_load_dwordx4 v253, s[52:55], s73 offen offset:2080 nt lds\n"
"v_mfma_f32_16x16x32_bf16 v[160:163], v[236:239], v[48:51], v[160:163]\n"
"s_waitcnt lgkmcnt(0)\n"
"v_add_f32_e32 v243, v145, v243\n"
"v_mfma_f32_16x16x32_bf16 v[160:163], v[224:227], v[64:67], v[160:163]\n"
"ds_read_b128 v[232:235], v240 offset:50304\n"
"v_add_f32_e32 v243, v146, v243\n"
"v_mfma_f32_16x16x32_bf16 v[160:163], v[228:231], v[80:83], v[160:163]\n"
"ds_read_b128 v[236:239], v240 offset:50336\n"
"v_add_f32_e32 v243, v147, v243\n"
"v_mfma_f32_16x16x32_bf16 v[164:167], v[224:227], v[68:71], v[164:167]\n"
"v_exp_f32_e32 v157, v157\n"
"v_mfma_f32_16x16x32_bf16 v[164:167], v[228:231], v[84:87], v[164:167]\n"
"v_exp_f32_e32 v158, v158\n"
"v_mfma_f32_16x16x32_bf16 v[168:171], v[224:227], v[72:75], v[168:171]\n"
"v_exp_f32_e32 v159, v159\n"
"v_mfma_f32_16x16x32_bf16 v[168:171], v[228:231], v[88:91], v[168:171]\n"
"v_add_f32_e32 v244, v148, v244\n"
"v_add_f32_e32 v244, v149, v244\n"
"v_mfma_f32_16x16x32_bf16 v[172:175], v[224:227], v[76:79], v[172:175]\n"
"buffer_load_dwordx4 v242, s[52:55], s73 offen offset:3120 nt lds\n"
"v_mfma_f32_16x16x32_bf16 v[172:175], v[228:231], v[92:95], v[172:175]\n"
"s_waitcnt lgkmcnt(0)\n"
"v_add_f32_e32 v244, v150, v244\n"
"v_mfma_f32_16x16x32_bf16 v[172:175], v[232:235], v[108:111], v[172:175]\n"
"ds_read_b128 v[224:227], v240 offset:50432\n"
"v_add_f32_e32 v244, v151, v244\n"
"v_mfma_f32_16x16x32_bf16 v[172:175], v[236:239], v[124:127], v[172:175]\n"
"ds_read_b128 v[228:231], v240 offset:50464\n"
"v_add_f32_e32 v245, v152, v245\n"
"v_mfma_f32_16x16x32_bf16 v[168:171], v[232:235], v[104:107], v[168:171]\n"
"v_add_f32_e32 v245, v153, v245\n"
"v_add_f32_e32 v245, v154, v245\n"
"v_mfma_f32_16x16x32_bf16 v[168:171], v[236:239], v[120:123], v[168:171]\n"
"v_add_f32_e32 v245, v155, v245\n"
"v_add_f32_e32 v246, v156, v246\n"
"v_mfma_f32_16x16x32_bf16 v[164:167], v[232:235], v[100:103], v[164:167]\n"
"s_add_u32 s73, s73, 32\n"
"v_add_f32_e32 v246, v157, v246\n"
"v_mfma_f32_16x16x32_bf16 v[164:167], v[236:239], v[116:119], v[164:167]\n"
"s_add_u32 m0, s68, 37440\n"
"v_add_f32_e32 v246, v158, v246\n"
"v_mfma_f32_16x16x32_bf16 v[160:163], v[232:235], v[96:99], v[160:163]\n"
"buffer_load_dwordx4 v253, s[56:59], s73 offen offset:0 nt lds\n"
"v_mfma_f32_16x16x32_bf16 v[160:163], v[236:239], v[112:115], v[160:163]\n"
"s_waitcnt lgkmcnt(0)\n"
"v_add_f32_e32 v246, v159, v246\n"
"v_mfma_f32_16x16x32_bf16 v[176:179], v[224:227], v[0:3], v[208:211]\n"
"ds_read_b128 v[232:235], v240 offset:50560\n"
"v_cvt_pk_bf16_f32 v192, v128, v129\n"
"v_mfma_f32_16x16x32_bf16 v[176:179], v[228:231], v[16:19], v[176:179]\n"
"ds_read_b128 v[236:239], v240 offset:50592\n"
"v_cvt_pk_bf16_f32 v193, v130, v131\n"
"v_mfma_f32_16x16x32_bf16 v[180:183], v[224:227], v[4:7], v[212:215]\n"
"v_cvt_pk_bf16_f32 v196, v132, v133\n"
"v_cvt_pk_bf16_f32 v197, v134, v135\n"
"v_mfma_f32_16x16x32_bf16 v[180:183], v[228:231], v[20:23], v[180:183]\n"
"v_cvt_pk_bf16_f32 v200, v136, v137\n"
"v_cvt_pk_bf16_f32 v201, v138, v139\n"
"v_mfma_f32_16x16x32_bf16 v[184:187], v[224:227], v[8:11], v[216:219]\n"
"v_cvt_pk_bf16_f32 v204, v140, v141\n"
"v_cvt_pk_bf16_f32 v205, v142, v143\n"
"v_mfma_f32_16x16x32_bf16 v[184:187], v[228:231], v[24:27], v[184:187]\n"
"v_cvt_pk_bf16_f32 v194, v144, v145\n"
"v_cvt_pk_bf16_f32 v195, v146, v147\n"
"v_mfma_f32_16x16x32_bf16 v[188:191], v[224:227], v[12:15], v[220:223]\n"
"buffer_load_dwordx4 v242, s[56:59], s73 offen offset:1040 nt lds\n"
"v_mfma_f32_16x16x32_bf16 v[188:191], v[228:231], v[28:31], v[188:191]\n"
"s_waitcnt lgkmcnt(0)\n"
"v_cvt_pk_bf16_f32 v198, v148, v149\n"
"v_mfma_f32_16x16x32_bf16 v[188:191], v[232:235], v[44:47], v[188:191]\n"
"ds_read_b128 v[224:227], v240 offset:50688\n"
"v_cvt_pk_bf16_f32 v199, v150, v151\n"
"v_mfma_f32_16x16x32_bf16 v[188:191], v[236:239], v[60:63], v[188:191]\n"
"ds_read_b128 v[228:231], v240 offset:50720\n"
"v_cvt_pk_bf16_f32 v202, v152, v153\n"
"v_mfma_f32_16x16x32_bf16 v[184:187], v[232:235], v[40:43], v[184:187]\n"
"v_cvt_pk_bf16_f32 v203, v154, v155\n"
"v_cvt_pk_bf16_f32 v206, v156, v157\n"
"v_mfma_f32_16x16x32_bf16 v[184:187], v[236:239], v[56:59], v[184:187]\n"
"v_cvt_pk_bf16_f32 v207, v158, v159\n"
"v_max3_f32 v247, v160, v161, v162\n"
"v_mfma_f32_16x16x32_bf16 v[180:183], v[232:235], v[36:39], v[180:183]\n"
"v_max3_f32 v247, v247, v163, v164\n"
"v_max3_f32 v247, v247, v165, v166\n"
"v_mfma_f32_16x16x32_bf16 v[180:183], v[236:239], v[52:55], v[180:183]\n"
"s_sub_u32 s73, s73, 32\n"
"v_max3_f32 v247, v247, v167, v168\n"
"v_mfma_f32_16x16x32_bf16 v[176:179], v[232:235], v[32:35], v[176:179]\n"
"buffer_load_dwordx4 v253, s[56:59], s73 offen offset:2080 nt lds\n"
"v_mfma_f32_16x16x32_bf16 v[176:179], v[236:239], v[48:51], v[176:179]\n"
"s_waitcnt lgkmcnt(0)\n"
"v_max3_f32 v247, v247, v169, v170\n"
"v_mfma_f32_16x16x32_bf16 v[176:179], v[224:227], v[64:67], v[176:179]\n"
"ds_read_b128 v[232:235], v240 offset:50816\n"
"v_max3_f32 v247, v247, v171, v172\n"
"v_mfma_f32_16x16x32_bf16 v[176:179], v[228:231], v[80:83], v[176:179]\n"
"ds_read_b128 v[236:239], v240 offset:50848\n"
"v_max3_f32 v247, v247, v173, v174\n"
"v_mfma_f32_16x16x32_bf16 v[180:183], v[224:227], v[68:71], v[180:183]\n"
"v_mfma_f32_16x16x32_bf16 v[180:183], v[228:231], v[84:87], v[180:183]\n"
"v_mfma_f32_16x16x32_bf16 v[184:187], v[224:227], v[72:75], v[184:187]\n"
"v_mfma_f32_16x16x32_bf16 v[184:187], v[228:231], v[88:91], v[184:187]\n"
"v_mfma_f32_16x16x32_bf16 v[188:191], v[224:227], v[76:79], v[188:191]\n"
"buffer_load_dwordx4 v242, s[56:59], s73 offen offset:3120 nt lds\n"
"v_mfma_f32_16x16x32_bf16 v[188:191], v[228:231], v[92:95], v[188:191]\n"
"s_waitcnt vmcnt(9) lgkmcnt(0)\n"
"s_barrier\n"
"v_mfma_f32_16x16x32_bf16 v[188:191], v[232:235], v[108:111], v[188:191]\n"
"ds_read_b64_tr_b16 v[224:225], v241 offset:0\n"
"v_mfma_f32_16x16x32_bf16 v[188:191], v[236:239], v[124:127], v[188:191]\n"
"ds_read_b64_tr_b16 v[226:227], v241 offset:512\n"
"v_mfma_f32_16x16x32_bf16 v[184:187], v[232:235], v[104:107], v[184:187]\n"
"ds_read_b64_tr_b16 v[228:229], v241 offset:32\n"
"v_mfma_f32_16x16x32_bf16 v[184:187], v[236:239], v[120:123], v[184:187]\n"
"ds_read_b64_tr_b16 v[230:231], v241 offset:544\n"
"v_mfma_f32_16x16x32_bf16 v[180:183], v[232:235], v[100:103], v[180:183]\n"
"s_mov_b32 s69, 0\n"
"v_mfma_f32_16x16x32_bf16 v[180:183], v[236:239], v[116:119], v[180:183]\n"
"s_add_u32 m0, s68, 49920\n"
"v_mfma_f32_16x16x32_bf16 v[176:179], v[232:235], v[96:99], v[176:179]\n"
"buffer_load_dwordx4 v255, s[60:63], s69 offen offset:0 lds\n"
"v_mfma_f32_16x16x32_bf16 v[176:179], v[236:239], v[112:115], v[176:179]\n"
"s_waitcnt lgkmcnt(2)\n"
"v_mfma_f32_16x16x32_bf16 a[0:3], v[224:227], v[192:195], a[0:3]\n"
"ds_read_b64_tr_b16 v[232:233], v241 offset:64\n"
"v_mfma_f32_16x16x32_bf16 a[4:7], v[224:227], v[196:199], a[4:7]\n"
"ds_read_b64_tr_b16 v[234:235], v241 offset:576\n"
"s_waitcnt lgkmcnt(2)\n"
"v_mfma_f32_16x16x32_bf16 a[8:11], v[224:227], v[200:203], a[8:11]\n"
"ds_read_b64_tr_b16 v[236:237], v241 offset:96\n"
"v_mfma_f32_16x16x32_bf16 a[12:15], v[224:227], v[204:207], a[12:15]\n"
"ds_read_b64_tr_b16 v[238:239], v241 offset:608\n"
"v_mfma_f32_16x16x32_bf16 a[16:19], v[228:231], v[192:195], a[16:19]\n"
"ds_read_b64_tr_b16 v[224:225], v241 offset:128\n"
"v_max3_f32 v247, v247, v175, v176\n"
"v_mfma_f32_16x16x32_bf16 a[20:23], v[228:231], v[196:199], a[20:23]\n"
"ds_read_b64_tr_b16 v[226:227], v241 offset:640\n"
"s_mov_b32 s70, 6128\n"
"v_mfma_f32_16x16x32_bf16 a[24:27], v[228:231], v[200:203], a[24:27]\n"
"buffer_load_dwordx4 v255, s[60:63], s70 offen offset:1040 lds\n"
"s_waitcnt lgkmcnt(4)\n"
"v_mfma_f32_16x16x32_bf16 a[28:31], v[228:231], v[204:207], a[28:31]\n"
"v_max3_f32 v247, v247, v177, v178\n"
"v_max3_f32 v247, v247, v179, v180\n"
"v_mfma_f32_16x16x32_bf16 a[32:35], v[232:235], v[192:195], a[32:35]\n"
"ds_read_b64_tr_b16 v[228:229], v241 offset:160\n"
"v_max3_f32 v247, v247, v181, v182\n"
"v_mfma_f32_16x16x32_bf16 a[36:39], v[232:235], v[196:199], a[36:39]\n"
"ds_read_b64_tr_b16 v[230:231], v241 offset:672\n"
"s_waitcnt lgkmcnt(4)\n"
"v_mfma_f32_16x16x32_bf16 a[40:43], v[232:235], v[200:203], a[40:43]\n"
"v_max3_f32 v247, v247, v183, v184\n"
"v_max3_f32 v247, v247, v185, v186\n"
"v_mfma_f32_16x16x32_bf16 a[44:47], v[232:235], v[204:207], a[44:47]\n"
"v_max3_f32 v247, v247, v187, v188\n"
"v_max3_f32 v247, v247, v189, v190\n"
"v_mfma_f32_16x16x32_bf16 a[48:51], v[236:239], v[192:195], a[48:51]\n"
"ds_read_b64_tr_b16 v[232:233], v241 offset:192\n"
"s_waitcnt lgkmcnt(3)\n"
"v_mfma_f32_16x16x32_bf16 a[52:55], v[236:239], v[196:199], a[52:55]\n"
"ds_read_b64_tr_b16 v[234:235], v241 offset:704\n"
"s_mov_b32 s69, 1504\n"
"v_mfma_f32_16x16x32_bf16 a[56:59], v[236:239], v[200:203], a[56:59]\n"
"buffer_load_dwordx4 v255, s[60:63], s69 offen offset:2080 lds\n"
"v_mfma_f32_16x16x32_bf16 a[60:63], v[236:239], v[204:207], a[60:63]\n"
"v_max_f32_e32 v247, v247, v191\n"
"v_cmp_lt_f32_e32 vcc, s76, v247\n"
"v_mfma_f32_16x16x32_bf16 a[64:67], v[224:227], v[192:195], a[64:67]\n"
"ds_read_b64_tr_b16 v[236:237], v241 offset:224\n"
"v_mfma_f32_16x16x32_bf16 a[68:71], v[224:227], v[196:199], a[68:71]\n"
"ds_read_b64_tr_b16 v[238:239], v241 offset:736\n"
"s_waitcnt lgkmcnt(4)\n"
"v_mfma_f32_16x16x32_bf16 a[72:75], v[224:227], v[200:203], a[72:75]\n"
"s_cbranch_vccnz .Lst1_44\n"
".Lrt1_44:\n"
"v_mfma_f32_16x16x32_bf16 a[76:79], v[224:227], v[204:207], a[76:79]\n"
"v_exp_f32_e32 v160, v160\n"
"v_mfma_f32_16x16x32_bf16 a[80:83], v[228:231], v[192:195], a[80:83]\n"
"ds_read_b64_tr_b16 v[224:225], v241 offset:256\n"
"s_waitcnt lgkmcnt(3)\n"
"v_mfma_f32_16x16x32_bf16 a[84:87], v[228:231], v[196:199], a[84:87]\n"
"ds_read_b64_tr_b16 v[226:227], v241 offset:768\n"
"s_mov_b32 s70, 7632\n"
"v_mfma_f32_16x16x32_bf16 a[88:91], v[228:231], v[200:203], a[88:91]\n"
"buffer_load_dwordx4 v255, s[60:63], s70 offen offset:3120 lds\n"
"v_mfma_f32_16x16x32_bf16 a[92:95], v[228:231], v[204:207], a[92:95]\n"
"v_exp_f32_e32 v161, v161\n"
"v_mfma_f32_16x16x32_bf16 a[96:99], v[232:235], v[192:195], a[96:99]\n"
"ds_read_b64_tr_b16 v[228:229], v241 offset:288\n"
"v_add_f32_e32 v243, v160, v243\n"
"v_mfma_f32_16x16x32_bf16 a[100:103], v[232:235], v[196:199], a[100:103]\n"
"ds_read_b64_tr_b16 v[230:231], v241 offset:800\n"
"s_waitcnt lgkmcnt(4)\n"
"v_mfma_f32_16x16x32_bf16 a[104:107], v[232:235], v[200:203], a[104:107]\n"
"v_exp_f32_e32 v162, v162\n"
"v_mfma_f32_16x16x32_bf16 a[108:111], v[232:235], v[204:207], a[108:111]\n"
"v_exp_f32_e32 v163, v163\n"
"v_mfma_f32_16x16x32_bf16 a[112:115], v[236:239], v[192:195], a[112:115]\n"
"ds_read_b64_tr_b16 v[232:233], v241 offset:320\n"
"s_mov_b32 s69, 896\n"
"s_waitcnt lgkmcnt(3)\n"
"v_mfma_f32_16x16x32_bf16 a[116:119], v[236:239], v[196:199], a[116:119]\n"
"ds_read_b64_tr_b16 v[234:235], v241 offset:832\n"
"s_add_u32 m0, s68, 54080\n"
"v_mfma_f32_16x16x32_bf16 a[120:123], v[236:239], v[200:203], a[120:123]\n"
"buffer_load_dwordx4 v255, s[60:63], s69 offen offset:0 lds\n"
"v_mfma_f32_16x16x32_bf16 a[124:127], v[236:239], v[204:207], a[124:127]\n"
"v_exp_f32_e32 v164, v164\n"
"v_mfma_f32_16x16x32_bf16 a[128:131], v[224:227], v[192:195], a[128:131]\n"
"ds_read_b64_tr_b16 v[236:237], v241 offset:352\n"
"v_add_f32_e32 v243, v161, v243\n"
"v_mfma_f32_16x16x32_bf16 a[132:135], v[224:227], v[196:199], a[132:135]\n"
"ds_read_b64_tr_b16 v[238:239], v241 offset:864\n"
"s_waitcnt lgkmcnt(4)\n"
"v_mfma_f32_16x16x32_bf16 a[136:139], v[224:227], v[200:203], a[136:139]\n"
"v_exp_f32_e32 v165, v165\n"
"v_mfma_f32_16x16x32_bf16 a[140:143], v[224:227], v[204:207], a[140:143]\n"
"v_exp_f32_e32 v166, v166\n"
"v_mfma_f32_16x16x32_bf16 a[144:147], v[228:231], v[192:195], a[144:147]\n"
"ds_read_b64_tr_b16 v[224:225], v241 offset:384\n"
"s_waitcnt lgkmcnt(3)\n"
"v_mfma_f32_16x16x32_bf16 a[148:151], v[228:231], v[196:199], a[148:151]\n"
"ds_read_b64_tr_b16 v[226:227], v241 offset:896\n"
"s_mov_b32 s70, 7024\n"
"v_mfma_f32_16x16x32_bf16 a[152:155], v[228:231], v[200:203], a[152:155]\n"
"buffer_load_dwordx4 v255, s[60:63], s70 offen offset:1040 lds\n"
"v_mfma_f32_16x16x32_bf16 a[156:159], v[228:231], v[204:207], a[156:159]\n"
"v_exp_f32_e32 v167, v167\n"
"v_mfma_f32_16x16x32_bf16 a[160:163], v[232:235], v[192:195], a[160:163]\n"
"ds_read_b64_tr_b16 v[228:229], v241 offset:416\n"
"v_add_f32_e32 v243, v162, v243\n"
"v_mfma_f32_16x16x32_bf16 a[164:167], v[232:235], v[196:199], a[164:167]\n"
"ds_read_b64_tr_b16 v[230:231], v241 offset:928\n"
"s_waitcnt lgkmcnt(4)\n"
"v_mfma_f32_16x16x32_bf16 a[168:171], v[232:235], v[200:203], a[168:171]\n"
"v_exp_f32_e32 v168, v168\n"
"v_mfma_f32_16x16x32_bf16 a[172:175], v[232:235], v[204:207], a[172:175]\n"
"v_exp_f32_e32 v169, v169\n"
"v_mfma_f32_16x16x32_bf16 a[176:179], v[236:239], v[192:195], a[176:179]\n"
"ds_read_b64_tr_b16 v[232:233], v241 offset:448\n"
"s_waitcnt lgkmcnt(3)\n"
"v_mfma_f32_16x16x32_bf16 a[180:183], v[236:239], v[196:199], a[180:183]\n"
"ds_read_b64_tr_b16 v[234:235], v241 offset:960\n"
"s_mov_b32 s69, 2400\n"
"v_mfma_f32_16x16x32_bf16 a[184:187], v[236:239], v[200:203], a[184:187]\n"
"buffer_load_dwordx4 v255, s[60:63], s69 offen offset:2080 lds\n"
"v_mfma_f32_16x16x32_bf16 a[188:191], v[236:239], v[204:207], a[188:191]\n"
"v_exp_f32_e32 v170, v170\n"
"v_mfma_f32_16x16x32_bf16 a[192:195], v[224:227], v[192:195], a[192:195]\n"
"ds_read_b64_tr_b16 v[236:237], v241 offset:480\n"
"v_add_f32_e32 v243, v163, v243\n"
"v_mfma_f32_16x16x32_bf16 a[196:199], v[224:227], v[196:199], a[196:199]\n"
"ds_read_b64_tr_b16 v[238:239], v241 offset:992\n"
"s_waitcnt lgkmcnt(4)\n"
"v_mfma_f32_16x16x32_bf16 a[200:203], v[224:227], v[200:203], a[200:203]\n"
"v_exp_f32_e32 v171, v171\n"
"v_mfma_f32_16x16x32_bf16 a[204:207], v[224:227], v[204:207], a[204:207]\n"
"v_exp_f32_e32 v172, v172\n"
"v_mfma_f32_16x16x32_bf16 a[208:211], v[228:231], v[192:195], a[208:211]\n"
"v_exp_f32_e32 v173, v173\n"
"v_mfma_f32_16x16x32_bf16 a[212:215], v[228:231], v[196:199], a[212:215]\n"
"s_mov_b32 s70, 8528\n"
"v_add_f32_e32 v244, v164, v244\n"
"v_mfma_f32_16x16x32_bf16 a[216:219], v[228:231], v[200:203], a[216:219]\n"
"buffer_load_dwordx4 v255, s[60:63], s70 offen offset:3120 lds\n"
"v_mfma_f32_16x16x32_bf16 a[220:223], v[228:231], v[204:207], a[220:223]\n"
"s_waitcnt vmcnt(8) lgkmcnt(0)\n"
"s_barrier\n"
"v_mfma_f32_16x16x32_bf16 a[224:227], v[232:235], v[192:195], a[224:227]\n"
"ds_read_b64_tr_b16 v[224:225], v241 offset:33280\n"
"v_add_f32_e32 v244, v165, v244\n"
"v_mfma_f32_16x16x32_bf16 a[228:231], v[232:235], v[196:199], a[228:231]\n"
"ds_read_b64_tr_b16 v[226:227], v241 offset:33792\n"
"v_add_f32_e32 v244, v166, v244\n"
"v_mfma_f32_16x16x32_bf16 a[232:235], v[232:235], v[200:203], a[232:235]\n"
"ds_read_b64_tr_b16 v[228:229], v241 offset:33312\n"
"v_add_f32_e32 v244, v167, v244\n"
"v_mfma_f32_16x16x32_bf16 a[236:239], v[232:235], v[204:207], a[236:239]\n"
"ds_read_b64_tr_b16 v[230:231], v241 offset:33824\n"
"v_add_f32_e32 v245, v168, v245\n"
"v_mfma_f32_16x16x32_bf16 a[240:243], v[236:239], v[192:195], a[240:243]\n"
"ds_read_b64_tr_b16 v[232:233], v241 offset:33344\n"
"s_mov_b32 s69, 114688\n"
"v_mfma_f32_16x16x32_bf16 a[244:247], v[236:239], v[196:199], a[244:247]\n"
"ds_read_b64_tr_b16 v[234:235], v241 offset:33856\n"
"s_add_u32 m0, s68, 16640\n"
"v_mfma_f32_16x16x32_bf16 a[248:251], v[236:239], v[200:203], a[248:251]\n"
"buffer_load_dwordx4 v255, s[60:63], s69 offen offset:0 lds\n"
"v_mfma_f32_16x16x32_bf16 a[252:255], v[236:239], v[204:207], a[252:255]\n"
"s_waitcnt lgkmcnt(4)\n"
"v_add_f32_e32 v245, v169, v245\n"
"s_cbranch_vccnz .Lst2_45\n"
".Lrt2_45:\n"
"v_add_f32_e32 v245, v170, v245\n"
"v_add_f32_e32 v246, v172, v246\n"
"v_exp_f32_e32 v174, v174\n"
"v_exp_f32_e32 v175, v175\n"
"v_add_f32_e32 v245, v171, v245\n"
"v_add_f32_e32 v246, v173, v246\n"
"v_exp_f32_e32 v176, v176\n"
"v_exp_f32_e32 v177, v177\n"
"v_exp_f32_e32 v178, v178\n"
"v_add_f32_e32 v246, v174, v246\n"
"v_add_f32_e32 v243, v176, v243\n"
"v_exp_f32_e32 v179, v179\n"
"v_exp_f32_e32 v180, v180\n"
"v_add_f32_e32 v246, v175, v246\n"
"v_add_f32_e32 v243, v177, v243\n"
"v_exp_f32_e32 v181, v181\n"
"v_add_f32_e32 v244, v180, v244\n"
"v_exp_f32_e32 v182, v182\n"
"v_add_f32_e32 v243, v178, v243\n"
"v_exp_f32_e32 v183, v183\n"
"v_add_f32_e32 v244, v181, v244\n"
"v_exp_f32_e32 v184, v184\n"
"v_add_f32_e32 v243, v179, v243\n"
"v_exp_f32_e32 v185, v185\n"
"v_add_f32_e32 v244, v182, v244\n"
"v_add_f32_e32 v245, v184, v245\n"
"v_exp_f32_e32 v186, v186\n"
"v_exp_f32_e32 v187, v187\n"
"v_add_f32_e32 v244, v183, v244\n"
"v_add_f32_e32 v245, v185, v245\n"
"v_exp_f32_e32 v188, v188\n"
"v_exp_f32_e32 v189, v189\n"
"v_exp_f32_e32 v190, v190\n"
"v_add_f32_e32 v245, v186, v245\n"
"v_add_f32_e32 v246, v188, v246\n"
"v_exp_f32_e32 v191, v191\n"
"v_cvt_pk_bf16_f32 v208, v160, v161\n"
"v_add_f32_e32 v245, v187, v245\n"
"v_add_f32_e32 v246, v189, v246\n"
"v_cvt_pk_bf16_f32 v209, v162, v163\n"
"v_cvt_pk_bf16_f32 v212, v164, v165\n"
"v_cvt_pk_bf16_f32 v213, v166, v167\n"
"v_add_f32_e32 v246, v190, v246\n"
"v_cvt_pk_bf16_f32 v216, v168, v169\n"
"v_cvt_pk_bf16_f32 v217, v170, v171\n"
"v_cvt_pk_bf16_f32 v220, v172, v173\n"
"v_add_f32_e32 v246, v191, v246\n"
"v_cvt_pk_bf16_f32 v221, v174, v175\n"
"v_cvt_pk_bf16_f32 v210, v176, v177\n"
"v_cvt_pk_bf16_f32 v211, v178, v179\n"
"v_cvt_pk_bf16_f32 v214, v180, v181\n"
"v_cvt_pk_bf16_f32 v215, v182, v183\n"
"v_cvt_pk_bf16_f32 v218, v184, v185\n"
"v_cvt_pk_bf16_f32 v219, v186, v187\n"
"v_cvt_pk_bf16_f32 v222, v188, v189\n"
"v_cvt_pk_bf16_f32 v223, v190, v191\n"
"v_mfma_f32_16x16x32_bf16 a[0:3], v[224:227], v[208:211], a[0:3]\n"
"ds_read_b64_tr_b16 v[236:237], v241 offset:33376\n"
"v_mov_b32_e32 v128, v243\n"
"v_mfma_f32_16x16x32_bf16 a[4:7], v[224:227], v[212:215], a[4:7]\n"
"ds_read_b64_tr_b16 v[238:239], v241 offset:33888\n"
"s_waitcnt lgkmcnt(4)\n"
"v_mfma_f32_16x16x32_bf16 a[8:11], v[224:227], v[216:219], a[8:11]\n"
"s_mul_i32 s77, s85, 33280\n"
"s_add_u32 s77, s77, s89\n"
"v_mfma_f32_16x16x32_bf16 a[12:15], v[224:227], v[220:223], a[12:15]\n"
"v_permlane32_swap_b32_e32 v243, v128\n"
"v_mfma_f32_16x16x32_bf16 a[16:19], v[228:231], v[208:211], a[16:19]\n"
"ds_read_b64_tr_b16 v[224:225], v241 offset:33408\n"
"s_waitcnt lgkmcnt(3)\n"
"v_mfma_f32_16x16x32_bf16 a[20:23], v[228:231], v[212:215], a[20:23]\n"
"ds_read_b64_tr_b16 v[226:227], v241 offset:33920\n"
"s_mov_b32 s70, 120816\n"
"v_mfma_f32_16x16x32_bf16 a[24:27], v[228:231], v[216:219], a[24:27]\n"
"buffer_load_dwordx4 v255, s[60:63], s70 offen offset:1040 lds\n"
"v_mfma_f32_16x16x32_bf16 a[28:31], v[228:231], v[220:223], a[28:31]\n"
"v_mov_b32_e32 v129, v244\n"
"v_mov_b32_e32 v130, v245\n"
"v_mfma_f32_16x16x32_bf16 a[32:35], v[232:235], v[208:211], a[32:35]\n"
"ds_read_b64_tr_b16 v[228:229], v241 offset:33440\n"
"v_mov_b32_e32 v131, v246\n"
"v_mfma_f32_16x16x32_bf16 a[36:39], v[232:235], v[212:215], a[36:39]\n"
"ds_read_b64_tr_b16 v[230:231], v241 offset:33952\n"
"s_waitcnt lgkmcnt(4)\n"
"v_mfma_f32_16x16x32_bf16 a[40:43], v[232:235], v[216:219], a[40:43]\n"
"v_permlane32_swap_b32_e32 v244, v129\n"
"v_mfma_f32_16x16x32_bf16 a[44:47], v[232:235], v[220:223], a[44:47]\n"
"v_permlane32_swap_b32_e32 v245, v130\n"
"v_mfma_f32_16x16x32_bf16 a[48:51], v[236:239], v[208:211], a[48:51]\n"
"ds_read_b64_tr_b16 v[232:233], v241 offset:33472\n"
"s_waitcnt lgkmcnt(3)\n"
"v_mfma_f32_16x16x32_bf16 a[52:55], v[236:239], v[212:215], a[52:55]\n"
"ds_read_b64_tr_b16 v[234:235], v241 offset:33984\n"
"s_mov_b32 s69, 116192\n"
"v_mfma_f32_16x16x32_bf16 a[56:59], v[236:239], v[216:219], a[56:59]\n"
"buffer_load_dwordx4 v255, s[60:63], s69 offen offset:2080 lds\n"
"v_mfma_f32_16x16x32_bf16 a[60:63], v[236:239], v[220:223], a[60:63]\n"
"v_permlane32_swap_b32_e32 v246, v131\n"
"v_mfma_f32_16x16x32_bf16 a[64:67], v[224:227], v[208:211], a[64:67]\n"
"ds_read_b64_tr_b16 v[236:237], v241 offset:33504\n"
"v_add_f32_e32 v243, v243, v128\n"
"v_mfma_f32_16x16x32_bf16 a[68:71], v[224:227], v[212:215], a[68:71]\n"
"ds_read_b64_tr_b16 v[238:239], v241 offset:34016\n"
"s_waitcnt lgkmcnt(4)\n"
"v_mfma_f32_16x16x32_bf16 a[72:75], v[224:227], v[216:219], a[72:75]\n"
"v_add_f32_e32 v244, v244, v129\n"
"v_add_f32_e32 v245, v245, v130\n"
"v_mfma_f32_16x16x32_bf16 a[76:79], v[224:227], v[220:223], a[76:79]\n"
"v_add_f32_e32 v246, v246, v131\n"
"v_mov_b32_e32 v128, v243\n"
"v_mfma_f32_16x16x32_bf16 a[80:83], v[228:231], v[208:211], a[80:83]\n"
"ds_read_b64_tr_b16 v[224:225], v241 offset:33536\n"
"s_waitcnt lgkmcnt(3)\n"
"v_mfma_f32_16x16x32_bf16 a[84:87], v[228:231], v[212:215], a[84:87]\n"
"ds_read_b64_tr_b16 v[226:227], v241 offset:34048\n"
"s_mov_b32 s70, 122320\n"
"v_mfma_f32_16x16x32_bf16 a[88:91], v[228:231], v[216:219], a[88:91]\n"
"buffer_load_dwordx4 v255, s[60:63], s70 offen offset:3120 lds\n"
"v_mfma_f32_16x16x32_bf16 a[92:95], v[228:231], v[220:223], a[92:95]\n"
"v_permlane16_swap_b32_e32 v243, v128\n"
"v_mfma_f32_16x16x32_bf16 a[96:99], v[232:235], v[208:211], a[96:99]\n"
"ds_read_b64_tr_b16 v[228:229], v241 offset:33568\n"
"v_mov_b32_e32 v129, v244\n"
"v_mfma_f32_16x16x32_bf16 a[100:103], v[232:235], v[212:215], a[100:103]\n"
"ds_read_b64_tr_b16 v[230:231], v241 offset:34080\n"
"s_waitcnt lgkmcnt(4)\n"
"v_mfma_f32_16x16x32_bf16 a[104:107], v[232:235], v[216:219], a[104:107]\n"
"v_permlane16_swap_b32_e32 v244, v129\n"
"v_mfma_f32_16x16x32_bf16 a[108:111], v[232:235], v[220:223], a[108:111]\n"
"v_mov_b32_e32 v130, v245\n"
"v_mov_b32_e32 v131, v246\n"
"v_mfma_f32_16x16x32_bf16 a[112:115], v[236:239], v[208:211], a[112:115]\n"
"ds_read_b64_tr_b16 v[232:233], v241 offset:33600\n"
"s_mov_b32 s69, 115584\n"
"s_waitcnt lgkmcnt(3)\n"
"v_mfma_f32_16x16x32_bf16 a[116:119], v[236:239], v[212:215], a[116:119]\n"
"ds_read_b64_tr_b16 v[234:235], v241 offset:34112\n"
"s_add_u32 m0, s68, 20800\n"
"v_mfma_f32_16x16x32_bf16 a[120:123], v[236:239], v[216:219], a[120:123]\n"
"buffer_load_dwordx4 v255, s[60:63], s69 offen offset:0 lds\n"
"v_mfma_f32_16x16x32_bf16 a[124:127], v[236:239], v[220:223], a[124:127]\n"
"v_permlane16_swap_b32_e32 v245, v130\n"
"v_mfma_f32_16x16x32_bf16 a[128:131], v[224:227], v[208:211], a[128:131]\n"
"ds_read_b64_tr_b16 v[236:237], v241 offset:33632\n"
"v_add_f32_e32 v243, v243, v128\n"
"v_mfma_f32_16x16x32_bf16 a[132:135], v[224:227], v[212:215], a[132:135]\n"
"ds_read_b64_tr_b16 v[238:239], v241 offset:34144\n"
"s_waitcnt lgkmcnt(4)\n"
"v_mfma_f32_16x16x32_bf16 a[136:139], v[224:227], v[216:219], a[136:139]\n"
"v_permlane16_swap_b32_e32 v246, v131\n"
"v_mfma_f32_16x16x32_bf16 a[140:143], v[224:227], v[220:223], a[140:143]\n"
"v_rcp_f32_e32 v243, v243\n"
"v_mfma_f32_16x16x32_bf16 a[144:147], v[228:231], v[208:211], a[144:147]\n"
"ds_read_b64_tr_b16 v[224:225], v241 offset:33664\n"
"s_waitcnt lgkmcnt(3)\n"
"v_mfma_f32_16x16x32_bf16 a[148:151], v[228:231], v[212:215], a[148:151]\n"
"ds_read_b64_tr_b16 v[226:227], v241 offset:34176\n"
"s_mov_b32 s70, 121712\n"
"v_mfma_f32_16x16x32_bf16 a[152:155], v[228:231], v[216:219], a[152:155]\n"
"buffer_load_dwordx4 v255, s[60:63], s70 offen offset:1040 lds\n"
"v_mfma_f32_16x16x32_bf16 a[156:159], v[228:231], v[220:223], a[156:159]\n"
"v_add_f32_e32 v244, v244, v129\n"
"v_add_f32_e32 v245, v245, v130\n"
"v_mfma_f32_16x16x32_bf16 a[160:163], v[232:235], v[208:211], a[160:163]\n"
"ds_read_b64_tr_b16 v[228:229], v241 offset:33696\n"
"v_add_f32_e32 v246, v246, v131\n"
"v_mfma_f32_16x16x32_bf16 a[164:167], v[232:235], v[212:215], a[164:167]\n"
"ds_read_b64_tr_b16 v[230:231], v241 offset:34208\n"
"s_waitcnt lgkmcnt(4)\n"
"v_mfma_f32_16x16x32_bf16 a[168:171], v[232:235], v[216:219], a[168:171]\n"
"v_rcp_f32_e32 v245, v245\n"
"v_mfma_f32_16x16x32_bf16 a[172:175], v[232:235], v[220:223], a[172:175]\n"
"v_max_f32_e32 v244, 0x0da24260, v244\n"
"v_max_f32_e32 v246, 0x0da24260, v246\n"
"v_mfma_f32_16x16x32_bf16 a[176:179], v[236:239], v[208:211], a[176:179]\n"
"ds_read_b64_tr_b16 v[232:233], v241 offset:33728\n"
"s_waitcnt lgkmcnt(3)\n"
"v_mfma_f32_16x16x32_bf16 a[180:183], v[236:239], v[212:215], a[180:183]\n"
"ds_read_b64_tr_b16 v[234:235], v241 offset:34240\n"
"s_mov_b32 s69, 117088\n"
"v_mfma_f32_16x16x32_bf16 a[184:187], v[236:239], v[216:219], a[184:187]\n"
"buffer_load_dwordx4 v255, s[60:63], s69 offen offset:2080 lds\n"
"v_mfma_f32_16x16x32_bf16 a[188:191], v[236:239], v[220:223], a[188:191]\n"
"v_rcp_f32_e32 v244, v244\n"
"v_mfma_f32_16x16x32_bf16 a[192:195], v[224:227], v[208:211], a[192:195]\n"
"ds_read_b64_tr_b16 v[236:237], v241 offset:33760\n"
"v_add_u32_e32 v252, s77, v242\n"
"v_mfma_f32_16x16x32_bf16 a[196:199], v[224:227], v[212:215], a[196:199]\n"
"ds_read_b64_tr_b16 v[238:239], v241 offset:34272\n"
"s_waitcnt lgkmcnt(4)\n"
"v_mfma_f32_16x16x32_bf16 a[200:203], v[224:227], v[216:219], a[200:203]\n"
"v_rcp_f32_e32 v246, v246\n"
"v_mfma_f32_16x16x32_bf16 a[204:207], v[224:227], v[220:223], a[204:207]\n"
"v_sub_u32_e32 v253, v254, v241\n"
"v_add_u32_e32 v253, v253, v252\n"
"v_mfma_f32_16x16x32_bf16 a[208:211], v[228:231], v[208:211], a[208:211]\n"
"v_accvgpr_read_b32 v132, a8\n"
"v_accvgpr_read_b32 v133, a9\n"
"v_mfma_f32_16x16x32_bf16 a[212:215], v[228:231], v[212:215], a[212:215]\n"
"s_mov_b32 s70, 123216\n"
"v_mul_f32_e32 v132, v245, v132\n"
"v_mfma_f32_16x16x32_bf16 a[216:219], v[228:231], v[216:219], a[216:219]\n"
"buffer_load_dwordx4 v255, s[60:63], s70 offen offset:3120 lds\n"
"v_mfma_f32_16x16x32_bf16 a[220:223], v[228:231], v[220:223], a[220:223]\n"
"s_waitcnt vmcnt(8) lgkmcnt(0)\n"
"s_barrier\n"
"v_mfma_f32_16x16x32_bf16 a[224:227], v[232:235], v[208:211], a[224:227]\n"
"v_mul_f32_e32 v133, v245, v133\n"
"v_cvt_pk_bf16_f32 v64, v132, v133\n"
"v_mfma_f32_16x16x32_bf16 a[228:231], v[232:235], v[212:215], a[228:231]\n"
"v_accvgpr_read_b32 v134, a10\n"
"v_accvgpr_read_b32 v135, a11\n"
"v_mfma_f32_16x16x32_bf16 a[232:235], v[232:235], v[216:219], a[232:235]\n"
"v_mul_f32_e32 v134, v245, v134\n"
"v_mul_f32_e32 v135, v245, v135\n"
"v_mfma_f32_16x16x32_bf16 a[236:239], v[232:235], v[220:223], a[236:239]\n"
"v_cvt_pk_bf16_f32 v65, v134, v135\n"
"v_accvgpr_read_b32 v136, a24\n"
"v_mfma_f32_16x16x32_bf16 a[240:243], v[236:239], v[208:211], a[240:243]\n"
"v_accvgpr_read_b32 v137, a25\n"
"v_mul_f32_e32 v136, v245, v136\n"
"v_mfma_f32_16x16x32_bf16 a[244:247], v[236:239], v[212:215], a[244:247]\n"
"v_mul_f32_e32 v137, v245, v137\n"
"v_cvt_pk_bf16_f32 v66, v136, v137\n"
"v_mfma_f32_16x16x32_bf16 a[248:251], v[236:239], v[216:219], a[248:251]\n"
"v_accvgpr_read_b32 v138, a26\n"
"v_accvgpr_read_b32 v139, a27\n"
"v_mfma_f32_16x16x32_bf16 a[252:255], v[236:239], v[220:223], a[252:255]\n"
"v_mul_f32_e32 v138, v245, v138\n"
"v_mul_f32_e32 v139, v245, v139\n"
"v_cvt_pk_bf16_f32 v67, v138, v139\n"
"v_accvgpr_read_b32 v140, a12\n"
"v_accvgpr_read_b32 v141, a13\n"
"v_accvgpr_read_b32 v142, a14\n"
"ds_write_b128 v252, v[64:67] offset:0\n"
"v_accvgpr_read_b32 v143, a15\n"
"v_pk_mul_f32 v[140:141], v[140:141], v[246:247] op_sel:[0,0] op_sel_hi:[1,0]\n"
"v_accvgpr_read_b32 v144, a28\n"
"v_accvgpr_read_b32 v145, a29\n"
"v_pk_mul_f32 v[142:143], v[142:143], v[246:247] op_sel:[0,0] op_sel_hi:[1,0]\n"
"v_cvt_pk_bf16_f32 v68, v140, v141\n"
"v_accvgpr_read_b32 v146, a30\n"
"v_pk_mul_f32 v[144:145], v[144:145], v[246:247] op_sel:[0,0] op_sel_hi:[1,0]\n"
"v_cvt_pk_bf16_f32 v69, v142, v143\n"
"v_accvgpr_read_b32 v147, a31\n"
"v_accvgpr_read_b32 v148, a40\n"
"v_cvt_pk_bf16_f32 v70, v144, v145\n"
"v_accvgpr_read_b32 v149, a41\n"
"v_pk_mul_f32 v[146:147], v[146:147], v[246:247] op_sel:[0,0] op_sel_hi:[1,0]\n"
"v_accvgpr_read_b32 v150, a42\n"
"v_accvgpr_read_b32 v151, a43\n"
"v_pk_mul_f32 v[148:149], v[148:149], v[244:245] op_sel:[0,1] op_sel_hi:[1,1]\n"
"v_cvt_pk_bf16_f32 v71, v146, v147\n"
"v_accvgpr_read_b32 v152, a56\n"
"v_pk_mul_f32 v[150:151], v[150:151], v[244:245] op_sel:[0,1] op_sel_hi:[1,1]\n"
"v_cvt_pk_bf16_f32 v72, v148, v149\n"
"ds_write_b128 v252, v[68:71] offset:1024\n"
"v_accvgpr_read_b32 v153, a57\n"
"s_branch .Lpo8\n"
".Lpg8:\n"
"s_setpc_b64 s[78:79]\n"
".Lpo8:\n"
"v_cvt_pk_bf16_f32 v73, v150, v151\n"
"v_accvgpr_read_b32 v154, a58\n"
"v_accvgpr_read_b32 v155, a59\n"
"v_pk_mul_f32 v[152:153], v[152:153], v[244:245] op_sel:[0,1] op_sel_hi:[1,1]\n"
"v_accvgpr_read_b32 v156, a44\n"
"v_accvgpr_read_b32 v157, a45\n"
"v_pk_mul_f32 v[154:155], v[154:155], v[244:245] op_sel:[0,1] op_sel_hi:[1,1]\n"
"v_cvt_pk_bf16_f32 v74, v152, v153\n"
"v_accvgpr_read_b32 v158, a46\n"
"v_pk_mul_f32 v[156:157], v[156:157], v[246:247] op_sel:[0,0] op_sel_hi:[1,0]\n"
"v_cvt_pk_bf16_f32 v75, v154, v155\n"
"v_accvgpr_read_b32 v159, a47\n"
"v_accvgpr_read_b32 v160, a60\n"
"v_cvt_pk_bf16_f32 v76, v156, v157\n"
"ds_write_b128 v252, v[72:75] offset:2048\n"
"v_pk_mul_f32 v[158:159], v[158:159], v[246:247] op_sel:[0,0] op_sel_hi:[1,0]\n"
"v_accvgpr_read_b32 v161, a61\n"
"v_accvgpr_read_b32 v162, a62\n"
"v_accvgpr_read_b32 v163, a63\n"
"v_cvt_pk_bf16_f32 v77, v158, v159\n"
"v_pk_mul_f32 v[160:161], v[160:161], v[246:247] op_sel:[0,0] op_sel_hi:[1,0]\n"
"v_accvgpr_read_b32 v164, a72\n"
"v_pk_mul_f32 v[162:163], v[162:163], v[246:247] op_sel:[0,0] op_sel_hi:[1,0]\n"
"v_accvgpr_read_b32 v165, a73\n"
"v_cvt_pk_bf16_f32 v78, v160, v161\n"
"v_accvgpr_read_b32 v166, a74\n"
"v_cvt_pk_bf16_f32 v79, v162, v163\n"
"v_pk_mul_f32 v[164:165], v[164:165], v[244:245] op_sel:[0,1] op_sel_hi:[1,1]\n"
"v_accvgpr_read_b32 v167, a75\n"
"v_accvgpr_read_b32 v168, a88\n"
"ds_write_b128 v252, v[76:79] offset:3072\n"
"v_cvt_pk_bf16_f32 v80, v164, v165\n"
"v_pk_mul_f32 v[166:167], v[166:167], v[244:245] op_sel:[0,1] op_sel_hi:[1,1]\n"
"v_accvgpr_read_b32 v169, a89\n"
"v_accvgpr_read_b32 v170, a90\n"
"v_accvgpr_read_b32 v171, a91\n"
"v_cvt_pk_bf16_f32 v81, v166, v167\n"
"v_pk_mul_f32 v[168:169], v[168:169], v[244:245] op_sel:[0,1] op_sel_hi:[1,1]\n"
"v_accvgpr_read_b32 v172, a76\n"
"v_pk_mul_f32 v[170:171], v[170:171], v[244:245] op_sel:[0,1] op_sel_hi:[1,1]\n"
"v_accvgpr_read_b32 v173, a77\n"
"v_cvt_pk_bf16_f32 v82, v168, v169\n"
"v_accvgpr_read_b32 v174, a78\n"
"v_cvt_pk_bf16_f32 v83, v170, v171\n"
"v_pk_mul_f32 v[172:173], v[172:173], v[246:247] op_sel:[0,0] op_sel_hi:[1,0]\n"
"v_accvgpr_read_b32 v175, a79\n"
"v_accvgpr_read_b32 v176, a92\n"
"ds_write_b128 v252, v[80:83] offset:4096\n"
"v_cvt_pk_bf16_f32 v84, v172, v173\n"
"v_pk_mul_f32 v[174:175], v[174:175], v[246:247] op_sel:[0,0] op_sel_hi:[1,0]\n"
"v_accvgpr_read_b32 v177, a93\n"
"v_accvgpr_read_b32 v178, a94\n"
"v_accvgpr_read_b32 v179, a95\n"
"v_cvt_pk_bf16_f32 v85, v174, v175\n"
"v_pk_mul_f32 v[176:177], v[176:177], v[246:247] op_sel:[0,0] op_sel_hi:[1,0]\n"
"v_accvgpr_read_b32 v180, a104\n"
"v_pk_mul_f32 v[178:179], v[178:179], v[246:247] op_sel:[0,0] op_sel_hi:[1,0]\n"
"v_accvgpr_read_b32 v181, a105\n"
"v_cvt_pk_bf16_f32 v86, v176, v177\n"
"v_accvgpr_read_b32 v182, a106\n"
"v_cvt_pk_bf16_f32 v87, v178, v179\n"
"v_pk_mul_f32 v[180:181], v[180:181], v[244:245] op_sel:[0,1] op_sel_hi:[1,1]\n"
"v_accvgpr_read_b32 v183, a107\n"
"v_accvgpr_read_b32 v184, a120\n"
"ds_write_b128 v252, v[84:87] offset:5120\n"
"v_cvt_pk_bf16_f32 v88, v180, v181\n"
"v_pk_mul_f32 v[182:183], v[182:183], v[244:245] op_sel:[0,1] op_sel_hi:[1,1]\n"
"v_accvgpr_read_b32 v185, a121\n"
"v_accvgpr_read_b32 v186, a122\n"
"v_accvgpr_read_b32 v187, a123\n"
"v_cvt_pk_bf16_f32 v89, v182, v183\n"
"v_pk_mul_f32 v[184:185], v[184:185], v[244:245] op_sel:[0,1] op_sel_hi:[1,1]\n"
"v_accvgpr_read_b32 v188, a108\n"
"v_pk_mul_f32 v[186:187], v[186:187], v[244:245] op_sel:[0,1] op_sel_hi:[1,1]\n"
"v_accvgpr_read_b32 v189, a109\n"
"v_cvt_pk_bf16_f32 v90, v184, v185\n"
"v_accvgpr_read_b32 v190, a110\n"
"v_cvt_pk_bf16_f32 v91, v186, v187\n"
"v_pk_mul_f32 v[188:189], v[188:189], v[246:247] op_sel:[0,0] op_sel_hi:[1,0]\n"
"v_accvgpr_read_b32 v191, a111\n"
"v_accvgpr_read_b32 v132, a124\n"
"ds_write_b128 v252, v[88:91] offset:6144\n"
"v_cvt_pk_bf16_f32 v92, v188, v189\n"
"v_pk_mul_f32 v[190:191], v[190:191], v[246:247] op_sel:[0,0] op_sel_hi:[1,0]\n"
"v_accvgpr_read_b32 v133, a125\n"
"v_accvgpr_read_b32 v134, a126\n"
"v_accvgpr_read_b32 v135, a127\n"
"v_cvt_pk_bf16_f32 v93, v190, v191\n"
"v_pk_mul_f32 v[132:133], v[132:133], v[246:247] op_sel:[0,0] op_sel_hi:[1,0]\n"
"v_accvgpr_read_b32 v136, a136\n"
"v_pk_mul_f32 v[134:135], v[134:135], v[246:247] op_sel:[0,0] op_sel_hi:[1,0]\n"
"v_accvgpr_read_b32 v137, a137\n"
"v_cvt_pk_bf16_f32 v94, v132, v133\n"
"v_accvgpr_read_b32 v138, a138\n"
"v_cvt_pk_bf16_f32 v95, v134, v135\n"
"v_pk_mul_f32 v[136:137], v[136:137], v[244:245] op_sel:[0,1] op_sel_hi:[1,1]\n"
"v_accvgpr_read_b32 v139, a139\n"
"v_accvgpr_read_b32 v140, a152\n"
"ds_write_b128 v252, v[92:95] offset:7168\n"
"v_cvt_pk_bf16_f32 v96, v136, v137\n"
"v_pk_mul_f32 v[138:139], v[138:139], v[244:245] op_sel:[0,1] op_sel_hi:[1,1]\n"
"v_accvgpr_read_b32 v141, a153\n"
"v_accvgpr_read_b32 v142, a154\n"
"v_accvgpr_read_b32 v143, a155\n"
"v_cvt_pk_bf16_f32 v97, v138, v139\n"
"v_pk_mul_f32 v[140:141], v[140:141], v[244:245] op_sel:[0,1] op_sel_hi:[1,1]\n"
"v_accvgpr_read_b32 v144, a140\n"
"v_pk_mul_f32 v[142:143], v[142:143], v[244:245] op_sel:[0,1] op_sel_hi:[1,1]\n"
"v_accvgpr_read_b32 v145, a141\n"
"v_cvt_pk_bf16_f32 v98, v140, v141\n"
"v_accvgpr_read_b32 v146, a142\n"
"v_cvt_pk_bf16_f32 v99, v142, v143\n"
"v_pk_mul_f32 v[144:145], v[144:145], v[246:247] op_sel:[0,0] op_sel_hi:[1,0]\n"
"v_accvgpr_read_b32 v147, a143\n"
"v_accvgpr_read_b32 v148, a156\n"
"ds_write_b128 v252, v[96:99] offset:8192\n"
"v_cvt_pk_bf16_f32 v100, v144, v145\n"
"v_pk_mul_f32 v[146:147], v[146:147], v[246:247] op_sel:[0,0] op_sel_hi:[1,0]\n"
"v_accvgpr_read_b32 v149, a157\n"
"v_accvgpr_read_b32 v150, a158\n"
"v_accvgpr_read_b32 v151, a159\n"
"v_cvt_pk_bf16_f32 v101, v146, v147\n"
"v_pk_mul_f32 v[148:149], v[148:149], v[246:247] op_sel:[0,0] op_sel_hi:[1,0]\n"
"v_accvgpr_read_b32 v152, a168\n"
"v_pk_mul_f32 v[150:151], v[150:151], v[246:247] op_sel:[0,0] op_sel_hi:[1,0]\n"
"v_accvgpr_read_b32 v153, a169\n"
"v_cvt_pk_bf16_f32 v102, v148, v149\n"
"v_accvgpr_read_b32 v154, a170\n"
"v_cvt_pk_bf16_f32 v103, v150, v151\n"
"v_pk_mul_f32 v[152:153], v[152:153], v[244:245] op_sel:[0,1] op_sel_hi:[1,1]\n"
"v_accvgpr_read_b32 v155, a171\n"
"v_accvgpr_read_b32 v156, a184\n"
"ds_write_b128 v252, v[100:103] offset:9216\n"
"v_cvt_pk_bf16_f32 v104, v152, v153\n"
"v_pk_mul_f32 v[154:155], v[154:155], v[244:245] op_sel:[0,1] op_sel_hi:[1,1]\n"
"v_accvgpr_read_b32 v157, a185\n"
"v_accvgpr_read_b32 v158, a186\n"
"v_accvgpr_read_b32 v159, a187\n"
"v_cvt_pk_bf16_f32 v105, v154, v155\n"
"v_pk_mul_f32 v[156:157], v[156:157], v[244:245] op_sel:[0,1] op_sel_hi:[1,1]\n"
"v_accvgpr_read_b32 v160, a172\n"
"v_pk_mul_f32 v[158:159], v[158:159], v[244:245] op_sel:[0,1] op_sel_hi:[1,1]\n"
"v_accvgpr_read_b32 v161, a173\n"
"v_cvt_pk_bf16_f32 v106, v156, v157\n"
"v_accvgpr_read_b32 v162, a174\n"
"v_cvt_pk_bf16_f32 v107, v158, v159\n"
"v_pk_mul_f32 v[160:161], v[160:161], v[246:247] op_sel:[0,0] op_sel_hi:[1,0]\n"
"v_accvgpr_read_b32 v163, a175\n"
"v_accvgpr_read_b32 v164, a188\n"
"ds_write_b128 v252, v[104:107] offset:10240\n"
"v_cvt_pk_bf16_f32 v108, v160, v161\n"
"v_pk_mul_f32 v[162:163], v[162:163], v[246:247] op_sel:[0,0] op_sel_hi:[1,0]\n"
"v_accvgpr_read_b32 v165, a189\n"
"v_accvgpr_read_b32 v166, a190\n"
"v_accvgpr_read_b32 v167, a191\n"
"v_cvt_pk_bf16_f32 v109, v162, v163\n"
"v_pk_mul_f32 v[164:165], v[164:165], v[246:247] op_sel:[0,0] op_sel_hi:[1,0]\n"
"v_accvgpr_read_b32 v168, a200\n"
"v_pk_mul_f32 v[166:167], v[166:167], v[246:247] op_sel:[0,0] op_sel_hi:[1,0]\n"
"v_accvgpr_read_b32 v169, a201\n"
"v_cvt_pk_bf16_f32 v110, v164, v165\n"
"v_accvgpr_read_b32 v170, a202\n"
"v_cvt_pk_bf16_f32 v111, v166, v167\n"
"v_pk_mul_f32 v[168:169], v[168:169], v[244:245] op_sel:[0,1] op_sel_hi:[1,1]\n"
"v_accvgpr_read_b32 v171, a203\n"
"v_accvgpr_read_b32 v172, a216\n"
"ds_write_b128 v252, v[108:111] offset:11264\n"
"v_cvt_pk_bf16_f32 v112, v168, v169\n"
"v_pk_mul_f32 v[170:171], v[170:171], v[244:245] op_sel:[0,1] op_sel_hi:[1,1]\n"
"v_accvgpr_read_b32 v173, a217\n"
"v_accvgpr_read_b32 v174, a218\n"
"v_accvgpr_read_b32 v175, a219\n"
"v_cvt_pk_bf16_f32 v113, v170, v171\n"
"v_pk_mul_f32 v[172:173], v[172:173], v[244:245] op_sel:[0,1] op_sel_hi:[1,1]\n"
"v_accvgpr_read_b32 v176, a204\n"
"v_pk_mul_f32 v[174:175], v[174:175], v[244:245] op_sel:[0,1] op_sel_hi:[1,1]\n"
"v_accvgpr_read_b32 v177, a205\n"
"v_cvt_pk_bf16_f32 v114, v172, v173\n"
"v_accvgpr_read_b32 v178, a206\n"
"v_cvt_pk_bf16_f32 v115, v174, v175\n"
"v_pk_mul_f32 v[176:177], v[176:177], v[246:247] op_sel:[0,0] op_sel_hi:[1,0]\n"
"v_accvgpr_read_b32 v179, a207\n"
"v_accvgpr_read_b32 v180, a220\n"
"ds_write_b128 v252, v[112:115] offset:12288\n"
"v_cvt_pk_bf16_f32 v116, v176, v177\n"
"v_pk_mul_f32 v[178:179], v[178:179], v[246:247] op_sel:[0,0] op_sel_hi:[1,0]\n"
"v_accvgpr_read_b32 v181, a221\n"
"v_accvgpr_read_b32 v182, a222\n"
"v_accvgpr_read_b32 v183, a223\n"
"v_cvt_pk_bf16_f32 v117, v178, v179\n"
"v_pk_mul_f32 v[180:181], v[180:181], v[246:247] op_sel:[0,0] op_sel_hi:[1,0]\n"
"v_accvgpr_read_b32 v184, a232\n"
"v_pk_mul_f32 v[182:183], v[182:183], v[246:247] op_sel:[0,0] op_sel_hi:[1,0]\n"
"v_accvgpr_read_b32 v185, a233\n"
"v_cvt_pk_bf16_f32 v118, v180, v181\n"
"v_accvgpr_read_b32 v186, a234\n"
"v_cvt_pk_bf16_f32 v119, v182, v183\n"
"v_pk_mul_f32 v[184:185], v[184:185], v[244:245] op_sel:[0,1] op_sel_hi:[1,1]\n"
"v_accvgpr_read_b32 v187, a235\n"
"v_accvgpr_read_b32 v188, a248\n"
"ds_write_b128 v252, v[116:119] offset:13312\n"
"v_cvt_pk_bf16_f32 v120, v184, v185\n"
"v_pk_mul_f32 v[186:187], v[186:187], v[244:245] op_sel:[0,1] op_sel_hi:[1,1]\n"
"v_accvgpr_read_b32 v189, a249\n"
"v_accvgpr_read_b32 v190, a250\n"
"v_accvgpr_read_b32 v191, a251\n"
"v_cvt_pk_bf16_f32 v121, v186, v187\n"
"v_pk_mul_f32 v[188:189], v[188:189], v[244:245] op_sel:[0,1] op_sel_hi:[1,1]\n"
"v_accvgpr_read_b32 v132, a236\n"
"v_pk_mul_f32 v[190:191], v[190:191], v[244:245] op_sel:[0,1] op_sel_hi:[1,1]\n"
"v_accvgpr_read_b32 v133, a237\n"
"v_cvt_pk_bf16_f32 v122, v188, v189\n"
"v_accvgpr_read_b32 v134, a238\n"
"v_cvt_pk_bf16_f32 v123, v190, v191\n"
"v_pk_mul_f32 v[132:133], v[132:133], v[246:247] op_sel:[0,0] op_sel_hi:[1,0]\n"
"v_accvgpr_read_b32 v135, a239\n"
"v_accvgpr_read_b32 v136, a252\n"
"ds_write_b128 v252, v[120:123] offset:14336\n"
"v_cvt_pk_bf16_f32 v124, v132, v133\n"
"v_pk_mul_f32 v[134:135], v[134:135], v[246:247] op_sel:[0,0] op_sel_hi:[1,0]\n"
"v_accvgpr_read_b32 v137, a253\n"
"v_accvgpr_read_b32 v138, a254\n"
"v_accvgpr_read_b32 v139, a255\n"
"v_cvt_pk_bf16_f32 v125, v134, v135\n"
"v_pk_mul_f32 v[136:137], v[136:137], v[246:247] op_sel:[0,0] op_sel_hi:[1,0]\n"
"v_accvgpr_read_b32 v140, a0\n"
"v_pk_mul_f32 v[138:139], v[138:139], v[246:247] op_sel:[0,0] op_sel_hi:[1,0]\n"
"v_accvgpr_read_b32 v141, a1\n"
"v_cvt_pk_bf16_f32 v126, v136, v137\n"
"v_accvgpr_read_b32 v142, a2\n"
"v_cvt_pk_bf16_f32 v127, v138, v139\n"
"v_pk_mul_f32 v[140:141], v[140:141], v[242:243] op_sel:[0,1] op_sel_hi:[1,1]\n"
"v_accvgpr_read_b32 v143, a3\n"
"v_accvgpr_read_b32 v144, a16\n"
"ds_write_b128 v252, v[124:127] offset:15360\n"
"ds_read_b64_tr_b16 v[192:193], v241 offset:49920\n"
"ds_read_b64_tr_b16 v[194:195], v241 offset:50048\n"
"ds_read_b64_tr_b16 v[196:197], v241 offset:50176\n"
"ds_read_b64_tr_b16 v[198:199], v241 offset:50304\n"
"ds_read_b64_tr_b16 v[200:201], v241 offset:50432\n"
"ds_read_b64_tr_b16 v[202:203], v241 offset:50560\n"
"ds_read_b64_tr_b16 v[204:205], v241 offset:50688\n"
"ds_read_b64_tr_b16 v[206:207], v241 offset:50816\n"
"ds_read_b64_tr_b16 v[208:209], v241 offset:49952\n"
"ds_read_b64_tr_b16 v[210:211], v241 offset:50080\n"
"ds_read_b64_tr_b16 v[212:213], v241 offset:50208\n"
"ds_read_b64_tr_b16 v[214:215], v241 offset:50336\n"
"v_cvt_pk_bf16_f32 v0, v140, v141\n"
"v_pk_mul_f32 v[142:143], v[142:143], v[242:243] op_sel:[0,1] op_sel_hi:[1,1]\n"
"v_accvgpr_read_b32 v145, a17\n"
"v_accvgpr_read_b32 v146, a18\n"
"v_accvgpr_read_b32 v147, a19\n"
"v_cvt_pk_bf16_f32 v1, v142, v143\n"
"v_pk_mul_f32 v[144:145], v[144:145], v[242:243] op_sel:[0,1] op_sel_hi:[1,1]\n"
"v_accvgpr_read_b32 v148, a4\n"
"v_pk_mul_f32 v[146:147], v[146:147], v[242:243] op_sel:[0,1] op_sel_hi:[1,1]\n"
"v_accvgpr_read_b32 v149, a5\n"
"v_cvt_pk_bf16_f32 v2, v144, v145\n"
"v_accvgpr_read_b32 v150, a6\n"
"v_cvt_pk_bf16_f32 v3, v146, v147\n"
"v_pk_mul_f32 v[148:149], v[148:149], v[244:245] op_sel:[0,0] op_sel_hi:[1,0]\n"
"v_accvgpr_read_b32 v151, a7\n"
"v_accvgpr_read_b32 v152, a20\n"
"v_accvgpr_read_b32 v153, a21\n"
"v_cvt_pk_bf16_f32 v4, v148, v149\n"
"v_pk_mul_f32 v[150:151], v[150:151], v[244:245] op_sel:[0,0] op_sel_hi:[1,0]\n"
"v_accvgpr_read_b32 v154, a22\n"
"v_pk_mul_f32 v[152:153], v[152:153], v[244:245] op_sel:[0,0] op_sel_hi:[1,0]\n"
"v_accvgpr_read_b32 v155, a23\n"
"v_cvt_pk_bf16_f32 v5, v150, v151\n"
"v_accvgpr_read_b32 v156, a32\n"
"v_cvt_pk_bf16_f32 v6, v152, v153\n"
"v_pk_mul_f32 v[154:155], v[154:155], v[244:245] op_sel:[0,0] op_sel_hi:[1,0]\n"
"v_accvgpr_read_b32 v157, a33\n"
"v_accvgpr_read_b32 v158, a34\n"
"v_accvgpr_read_b32 v159, a35\n"
"v_cvt_pk_bf16_f32 v7, v154, v155\n"
"v_pk_mul_f32 v[156:157], v[156:157], v[242:243] op_sel:[0,1] op_sel_hi:[1,1]\n"
"v_accvgpr_read_b32 v160, a48\n"
"v_pk_mul_f32 v[158:159], v[158:159], v[242:243] op_sel:[0,1] op_sel_hi:[1,1]\n"
"v_accvgpr_read_b32 v161, a49\n"
"v_cvt_pk_bf16_f32 v8, v156, v157\n"
"v_accvgpr_read_b32 v162, a50\n"
"v_cvt_pk_bf16_f32 v9, v158, v159\n"
"v_pk_mul_f32 v[160:161], v[160:161], v[242:243] op_sel:[0,1] op_sel_hi:[1,1]\n"
"v_accvgpr_read_b32 v163, a51\n"
"v_accvgpr_read_b32 v164, a36\n"
"v_accvgpr_read_b32 v165, a37\n"
"v_cvt_pk_bf16_f32 v10, v160, v161\n"
"v_pk_mul_f32 v[162:163], v[162:163], v[242:243] op_sel:[0,1] op_sel_hi:[1,1]\n"
"v_accvgpr_read_b32 v166, a38\n"
"v_pk_mul_f32 v[164:165], v[164:165], v[244:245] op_sel:[0,0] op_sel_hi:[1,0]\n"
"v_accvgpr_read_b32 v167, a39\n"
"v_cvt_pk_bf16_f32 v11, v162, v163\n"
"v_accvgpr_read_b32 v168, a52\n"
"v_cvt_pk_bf16_f32 v12, v164, v165\n"
"v_pk_mul_f32 v[166:167], v[166:167], v[244:245] op_sel:[0,0] op_sel_hi:[1,0]\n"
"v_accvgpr_read_b32 v169, a53\n"
"v_accvgpr_read_b32 v170, a54\n"
"v_accvgpr_read_b32 v171, a55\n"
"v_cvt_pk_bf16_f32 v13, v166, v167\n"
"v_pk_mul_f32 v[168:169], v[168:169], v[244:245] op_sel:[0,0] op_sel_hi:[1,0]\n"
"v_pk_mul_f32 v[170:171], v[170:171], v[244:245] op_sel:[0,0] op_sel_hi:[1,0]\n"
"v_cvt_pk_bf16_f32 v14, v168, v169\n"
"v_cvt_pk_bf16_f32 v15, v170, v171\n"
"s_waitcnt lgkmcnt(0)\n"
"s_barrier\n"
"ds_read_b128 v[64:67], v253 offset:0\n"
"ds_read_b128 v[68:71], v253 offset:1024\n"
"ds_read_b128 v[72:75], v253 offset:2048\n"
"ds_read_b128 v[76:79], v253 offset:3072\n"
"ds_read_b128 v[80:83], v253 offset:4096\n"
"ds_read_b128 v[84:87], v253 offset:5120\n"
"ds_read_b128 v[88:91], v253 offset:6144\n"
"ds_read_b128 v[92:95], v253 offset:7168\n"
"ds_read_b128 v[96:99], v253 offset:8192\n"
"ds_read_b128 v[100:103], v253 offset:9216\n"
"ds_read_b128 v[104:107], v253 offset:10240\n"
"ds_read_b128 v[108:111], v253 offset:11264\n"
"ds_read_b128 v[112:115], v253 offset:12288\n"
"ds_read_b128 v[116:119], v253 offset:13312\n"
"ds_read_b128 v[120:123], v253 offset:14336\n"
"ds_read_b128 v[124:127], v253 offset:15360\n"
"v_accvgpr_read_b32 v172, a64\n"
"v_accvgpr_read_b32 v173, a65\n"
"v_accvgpr_read_b32 v174, a66\n"
"v_accvgpr_read_b32 v175, a67\n"
"v_accvgpr_read_b32 v176, a80\n"
"v_pk_mul_f32 v[172:173], v[172:173], v[242:243] op_sel:[0,1] op_sel_hi:[1,1]\n"
"v_accvgpr_read_b32 v177, a81\n"
"v_pk_mul_f32 v[174:175], v[174:175], v[242:243] op_sel:[0,1] op_sel_hi:[1,1]\n"
"v_accvgpr_read_b32 v178, a82\n"
"v_cvt_pk_bf16_f32 v16, v172, v173\n"
"v_pk_mul_f32 v[176:177], v[176:177], v[242:243] op_sel:[0,1] op_sel_hi:[1,1]\n"
"v_cvt_pk_bf16_f32 v17, v174, v175\n"
"v_accvgpr_read_b32 v179, a83\n"
"v_accvgpr_read_b32 v180, a68\n"
"v_cvt_pk_bf16_f32 v18, v176, v177\n"
"v_accvgpr_read_b32 v181, a69\n"
"v_pk_mul_f32 v[178:179], v[178:179], v[242:243] op_sel:[0,1] op_sel_hi:[1,1]\n"
"v_accvgpr_read_b32 v182, a70\n"
"v_accvgpr_read_b32 v183, a71\n"
"v_pk_mul_f32 v[180:181], v[180:181], v[244:245] op_sel:[0,0] op_sel_hi:[1,0]\n"
"v_cvt_pk_bf16_f32 v19, v178, v179\n"
"v_accvgpr_read_b32 v184, a84\n"
"v_pk_mul_f32 v[182:183], v[182:183], v[244:245] op_sel:[0,0] op_sel_hi:[1,0]\n"
"v_cvt_pk_bf16_f32 v20, v180, v181\n"
"v_accvgpr_read_b32 v185, a85\n"
"v_accvgpr_read_b32 v186, a86\n"
"v_cvt_pk_bf16_f32 v21, v182, v183\n"
"v_accvgpr_read_b32 v187, a87\n"
"v_pk_mul_f32 v[184:185], v[184:185], v[244:245] op_sel:[0,0] op_sel_hi:[1,0]\n"
"v_accvgpr_read_b32 v188, a96\n"
"v_accvgpr_read_b32 v189, a97\n"
"v_pk_mul_f32 v[186:187], v[186:187], v[244:245] op_sel:[0,0] op_sel_hi:[1,0]\n"
"v_cvt_pk_bf16_f32 v22, v184, v185\n"
"v_accvgpr_read_b32 v190, a98\n"
"v_pk_mul_f32 v[188:189], v[188:189], v[242:243] op_sel:[0,1] op_sel_hi:[1,1]\n"
"v_cvt_pk_bf16_f32 v23, v186, v187\n"
"v_accvgpr_read_b32 v191, a99\n"
"v_accvgpr_read_b32 v132, a112\n"
"v_cvt_pk_bf16_f32 v24, v188, v189\n"
"v_accvgpr_read_b32 v133, a113\n"
"v_pk_mul_f32 v[190:191], v[190:191], v[242:243] op_sel:[0,1] op_sel_hi:[1,1]\n"
"v_accvgpr_read_b32 v134, a114\n"
"v_accvgpr_read_b32 v135, a115\n"
"v_pk_mul_f32 v[132:133], v[132:133], v[242:243] op_sel:[0,1] op_sel_hi:[1,1]\n"
"v_cvt_pk_bf16_f32 v25, v190, v191\n"
"v_accvgpr_read_b32 v136, a100\n"
"v_pk_mul_f32 v[134:135], v[134:135], v[242:243] op_sel:[0,1] op_sel_hi:[1,1]\n"
"v_cvt_pk_bf16_f32 v26, v132, v133\n"
"v_accvgpr_read_b32 v137, a101\n"
"v_accvgpr_read_b32 v138, a102\n"
"v_cvt_pk_bf16_f32 v27, v134, v135\n"
"v_accvgpr_read_b32 v139, a103\n"
"v_pk_mul_f32 v[136:137], v[136:137], v[244:245] op_sel:[0,0] op_sel_hi:[1,0]\n"
"v_accvgpr_read_b32 v140, a116\n"
"v_accvgpr_read_b32 v141, a117\n"
"v_pk_mul_f32 v[138:139], v[138:139], v[244:245] op_sel:[0,0] op_sel_hi:[1,0]\n"
"v_cvt_pk_bf16_f32 v28, v136, v137\n"
"v_accvgpr_read_b32 v142, a118\n"
"v_pk_mul_f32 v[140:141], v[140:141], v[244:245] op_sel:[0,0] op_sel_hi:[1,0]\n"
"v_cvt_pk_bf16_f32 v29, v138, v139\n"
"v_accvgpr_read_b32 v143, a119\n"
"v_cvt_pk_bf16_f32 v30, v140, v141\n"
"v_pk_mul_f32 v[142:143], v[142:143], v[244:245] op_sel:[0,0] op_sel_hi:[1,0]\n"
"v_cvt_pk_bf16_f32 v31, v142, v143\n"
"s_waitcnt lgkmcnt(0)\n"
"s_barrier\n"
"v_mfma_f32_16x16x32_bf16 v[128:131], v[192:195], v[0:3], 0\n"
"ds_read_b64_tr_b16 v[216:217], v241 offset:50464\n"
"v_accvgpr_read_b32 v160, a128\n"
"v_mfma_f32_16x16x32_bf16 v[128:131], v[196:199], v[8:11], v[128:131]\n"
"ds_read_b64_tr_b16 v[218:219], v241 offset:50592\n"
"s_mov_b32 s69, 128\n"
"v_mfma_f32_16x16x32_bf16 v[144:147], v[192:195], v[4:7], 0\n"
"ds_read_b64_tr_b16 v[220:221], v241 offset:50720\n"
"s_add_u32 m0, s68, 0\n"
"v_mfma_f32_16x16x32_bf16 v[144:147], v[196:199], v[12:15], v[144:147]\n"
"buffer_load_dwordx4 v255, s[60:63], s69 offen offset:0 lds\n"
"ds_read_b64_tr_b16 v[222:223], v241 offset:50848\n"
"v_mfma_f32_16x16x32_bf16 v[144:147], v[200:203], v[20:23], v[144:147]\n"
"ds_read_b64_tr_b16 v[192:193], v241 offset:49984\n"
"v_accvgpr_read_b32 v161, a129\n"
"v_mfma_f32_16x16x32_bf16 v[144:147], v[204:207], v[28:31], v[144:147]\n"
"ds_read_b64_tr_b16 v[194:195], v241 offset:50112\n"
"v_mul_f32_e32 v160, v243, v160\n"
"v_mfma_f32_16x16x32_bf16 v[128:131], v[200:203], v[16:19], v[128:131]\n"
"ds_read_b64_tr_b16 v[196:197], v241 offset:50240\n"
"v_mul_f32_e32 v161, v243, v161\n"
"v_mfma_f32_16x16x32_bf16 v[128:131], v[204:207], v[24:27], v[128:131]\n"
"ds_read_b64_tr_b16 v[198:199], v241 offset:50368\n"
"v_mfma_f32_16x16x32_bf16 v[132:135], v[208:211], v[0:3], 0\n"
"ds_read_b64_tr_b16 v[200:201], v241 offset:50496\n"
"s_mov_b32 s70, 6256\n"
"v_mfma_f32_16x16x32_bf16 v[132:135], v[212:215], v[8:11], v[132:135]\n"
"buffer_load_dwordx4 v255, s[60:63], s70 offen offset:1040 lds\n"
"ds_read_b64_tr_b16 v[202:203], v241 offset:50624\n"
"v_mfma_f32_16x16x32_bf16 v[148:151], v[208:211], v[4:7], 0\n"
"ds_read_b64_tr_b16 v[204:205], v241 offset:50752\n"
"v_cvt_pk_bf16_f32 v32, v160, v161\n"
"v_mfma_f32_16x16x32_bf16 v[148:151], v[212:215], v[12:15], v[148:151]\n"
"ds_read_b64_tr_b16 v[206:207], v241 offset:50880\n"
"s_waitcnt lgkmcnt(8)\n"
"v_mfma_f32_16x16x32_bf16 v[148:151], v[216:219], v[20:23], v[148:151]\n"
"ds_read_b64_tr_b16 v[208:209], v241 offset:50016\n"
"v_accvgpr_read_b32 v162, a130\n"
"v_mfma_f32_16x16x32_bf16 v[148:151], v[220:223], v[28:31], v[148:151]\n"
"ds_read_b64_tr_b16 v[210:211], v241 offset:50144\n"
"v_accvgpr_read_b32 v163, a131\n"
"v_mfma_f32_16x16x32_bf16 v[132:135], v[216:219], v[16:19], v[132:135]\n"
"ds_read_b64_tr_b16 v[212:213], v241 offset:50272\n"
"s_mov_b32 s69, 1632\n"
"v_mfma_f32_16x16x32_bf16 v[132:135], v[220:223], v[24:27], v[132:135]\n"
"buffer_load_dwordx4 v255, s[60:63], s69 offen offset:2080 lds\n"
"ds_read_b64_tr_b16 v[214:215], v241 offset:50400\n"
"s_waitcnt lgkmcnt(8)\n"
"v_mfma_f32_16x16x32_bf16 v[136:139], v[192:195], v[0:3], 0\n"
"ds_read_b64_tr_b16 v[216:217], v241 offset:50528\n"
"v_mul_f32_e32 v162, v243, v162\n"
"v_mfma_f32_16x16x32_bf16 v[136:139], v[196:199], v[8:11], v[136:139]\n"
"ds_read_b64_tr_b16 v[218:219], v241 offset:50656\n"
"v_mul_f32_e32 v163, v243, v163\n"
"v_mfma_f32_16x16x32_bf16 v[152:155], v[192:195], v[4:7], 0\n"
"ds_read_b64_tr_b16 v[220:221], v241 offset:50784\n"
"v_cvt_pk_bf16_f32 v33, v162, v163\n"
"v_mfma_f32_16x16x32_bf16 v[152:155], v[196:199], v[12:15], v[152:155]\n"
"ds_read_b64_tr_b16 v[222:223], v241 offset:50912\n"
"s_waitcnt lgkmcnt(8)\n"
"v_mfma_f32_16x16x32_bf16 v[152:155], v[200:203], v[20:23], v[152:155]\n"
"ds_read_b64_tr_b16 v[192:193], v254 offset:49920\n"
"s_mov_b32 s70, 7760\n"
"v_mfma_f32_16x16x32_bf16 v[152:155], v[204:207], v[28:31], v[152:155]\n"
"buffer_load_dwordx4 v255, s[60:63], s70 offen offset:3120 lds\n"
"ds_read_b64_tr_b16 v[194:195], v254 offset:50048\n"
"v_mfma_f32_16x16x32_bf16 v[136:139], v[200:203], v[16:19], v[136:139]\n"
"ds_read_b64_tr_b16 v[196:197], v254 offset:50176\n"
"v_accvgpr_read_b32 v164, a144\n"
"v_mfma_f32_16x16x32_bf16 v[136:139], v[204:207], v[24:27], v[136:139]\n"
"ds_read_b64_tr_b16 v[198:199], v254 offset:50304\n"
"s_waitcnt lgkmcnt(8)\n"
"v_mfma_f32_16x16x32_bf16 v[140:143], v[208:211], v[0:3], 0\n"
"ds_read_b64_tr_b16 v[200:201], v254 offset:50432\n"
"v_accvgpr_read_b32 v165, a145\n"
"v_mfma_f32_16x16x32_bf16 v[140:143], v[212:215], v[8:11], v[140:143]\n"
"ds_read_b64_tr_b16 v[202:203], v254 offset:50560\n"
"s_mov_b32 s69, 1024\n"
"v_mfma_f32_16x16x32_bf16 v[156:159], v[208:211], v[4:7], 0\n"
"ds_read_b64_tr_b16 v[204:205], v254 offset:50688\n"
"s_add_u32 m0, s68, 4160\n"
"v_mfma_f32_16x16x32_bf16 v[156:159], v[212:215], v[12:15], v[156:159]\n"
"buffer_load_dwordx4 v255, s[60:63], s69 offen offset:0 lds\n"
"ds_read_b64_tr_b16 v[206:207], v254 offset:50816\n"
"s_waitcnt lgkmcnt(8)\n"
"v_mfma_f32_16x16x32_bf16 v[156:159], v[216:219], v[20:23], v[156:159]\n"
"ds_read_b64_tr_b16 v[208:209], v254 offset:49952\n"
"v_mul_f32_e32 v164, v243, v164\n"
"v_mfma_f32_16x16x32_bf16 v[156:159], v[220:223], v[28:31], v[156:159]\n"
"ds_read_b64_tr_b16 v[210:211], v254 offset:50080\n"
"v_mul_f32_e32 v165, v243, v165\n"
"v_mfma_f32_16x16x32_bf16 v[140:143], v[216:219], v[16:19], v[140:143]\n"
"ds_read_b64_tr_b16 v[212:213], v254 offset:50208\n"
"v_cvt_pk_bf16_f32 v34, v164, v165\n"
"v_mfma_f32_16x16x32_bf16 v[140:143], v[220:223], v[24:27], v[140:143]\n"
"ds_read_b64_tr_b16 v[214:215], v254 offset:50336\n"
"s_waitcnt lgkmcnt(8)\n"
"v_mfma_f32_16x16x32_bf16 v[128:131], v[192:195], v[64:67], v[128:131]\n"
"ds_read_b64_tr_b16 v[216:217], v254 offset:50464\n"
"s_mov_b32 s70, 7152\n"
"v_mfma_f32_16x16x32_bf16 v[128:131], v[196:199], v[72:75], v[128:131]\n"
"buffer_load_dwordx4 v255, s[60:63], s70 offen offset:1040 lds\n"
"ds_read_b64_tr_b16 v[218:219], v254 offset:50592\n"
"v_mfma_f32_16x16x32_bf16 v[144:147], v[192:195], v[68:71], v[144:147]\n"
"ds_read_b64_tr_b16 v[220:221], v254 offset:50720\n"
"v_accvgpr_read_b32 v166, a146\n"
"v_mfma_f32_16x16x32_bf16 v[144:147], v[196:199], v[76:79], v[144:147]\n"
"ds_read_b64_tr_b16 v[222:223], v254 offset:50848\n"
"s_waitcnt lgkmcnt(8)\n"
"v_mfma_f32_16x16x32_bf16 v[144:147], v[200:203], v[84:87], v[144:147]\n"
"ds_read_b64_tr_b16 v[192:193], v254 offset:49984\n"
"v_accvgpr_read_b32 v167, a147\n"
"v_mfma_f32_16x16x32_bf16 v[144:147], v[204:207], v[92:95], v[144:147]\n"
"ds_read_b64_tr_b16 v[194:195], v254 offset:50112\n"
"v_mul_f32_e32 v166, v243, v166\n"
"v_mfma_f32_16x16x32_bf16 v[128:131], v[200:203], v[80:83], v[128:131]\n"
"ds_read_b64_tr_b16 v[196:197], v254 offset:50240\n"
"s_mov_b32 s69, 2528\n"
"v_mfma_f32_16x16x32_bf16 v[128:131], v[204:207], v[88:91], v[128:131]\n"
"buffer_load_dwordx4 v255, s[60:63], s69 offen offset:2080 lds\n"
"ds_read_b64_tr_b16 v[198:199], v254 offset:50368\n"
"s_waitcnt lgkmcnt(8)\n"
"v_mfma_f32_16x16x32_bf16 v[132:135], v[208:211], v[64:67], v[132:135]\n"
"ds_read_b64_tr_b16 v[200:201], v254 offset:50496\n"
"v_mul_f32_e32 v167, v243, v167\n"
"v_mfma_f32_16x16x32_bf16 v[132:135], v[212:215], v[72:75], v[132:135]\n"
"ds_read_b64_tr_b16 v[202:203], v254 offset:50624\n"
"v_cvt_pk_bf16_f32 v35, v166, v167\n"
"v_mfma_f32_16x16x32_bf16 v[148:151], v[208:211], v[68:71], v[148:151]\n"
"ds_read_b64_tr_b16 v[204:205], v254 offset:50752\n"
"v_accvgpr_read_b32 v168, a132\n"
"v_mfma_f32_16x16x32_bf16 v[148:151], v[212:215], v[76:79], v[148:151]\n"
"ds_read_b64_tr_b16 v[206:207], v254 offset:50880\n"
"s_branch .Lpo9\n"
".Lpg9:\n"
"s_setpc_b64 s[78:79]\n"
".Lpo9:\n"
"s_waitcnt lgkmcnt(8)\n"
"v_mfma_f32_16x16x32_bf16 v[148:151], v[216:219], v[84:87], v[148:151]\n"
"ds_read_b64_tr_b16 v[208:209], v254 offset:50016\n"
"s_mov_b32 s70, 8656\n"
"v_mfma_f32_16x16x32_bf16 v[148:151], v[220:223], v[92:95], v[148:151]\n"
"buffer_load_dwordx4 v255, s[60:63], s70 offen offset:3120 lds\n"
"ds_read_b64_tr_b16 v[210:211], v254 offset:50144\n"
"v_mfma_f32_16x16x32_bf16 v[132:135], v[216:219], v[80:83], v[132:135]\n"
"ds_read_b64_tr_b16 v[212:213], v254 offset:50272\n"
"v_accvgpr_read_b32 v169, a133\n"
"v_mfma_f32_16x16x32_bf16 v[132:135], v[220:223], v[88:91], v[132:135]\n"
"ds_read_b64_tr_b16 v[214:215], v254 offset:50400\n"
"s_waitcnt lgkmcnt(8)\n"
"v_mfma_f32_16x16x32_bf16 v[136:139], v[192:195], v[64:67], v[136:139]\n"
"ds_read_b64_tr_b16 v[216:217], v254 offset:50528\n"
"s_waitcnt vmcnt(8)\n"
"v_mfma_f32_16x16x32_bf16 v[136:139], v[196:199], v[72:75], v[136:139]\n"
"ds_read_b64_tr_b16 v[218:219], v254 offset:50656\n"
"v_mul_f32_e32 v168, v244, v168\n"
"v_mfma_f32_16x16x32_bf16 v[152:155], v[192:195], v[68:71], v[152:155]\n"
"ds_read_b64_tr_b16 v[220:221], v254 offset:50784\n"
"s_barrier\n"
"v_mfma_f32_16x16x32_bf16 v[152:155], v[196:199], v[76:79], v[152:155]\n"
"ds_read_b64_tr_b16 v[222:223], v254 offset:50912\n"
"s_mov_b32 s69, 114816\n"
"s_waitcnt lgkmcnt(8)\n"
"v_mfma_f32_16x16x32_bf16 v[152:155], v[200:203], v[84:87], v[152:155]\n"
"ds_read_b64_tr_b16 v[192:193], v241 offset:16640\n"
"s_add_u32 m0, s68, 33280\n"
"v_mfma_f32_16x16x32_bf16 v[152:155], v[204:207], v[92:95], v[152:155]\n"
"buffer_load_dwordx4 v255, s[60:63], s69 offen offset:0 lds\n"
"ds_read_b64_tr_b16 v[194:195], v241 offset:16768\n"
"v_mfma_f32_16x16x32_bf16 v[136:139], v[200:203], v[80:83], v[136:139]\n"
"ds_read_b64_tr_b16 v[196:197], v241 offset:16896\n"
"v_mul_f32_e32 v169, v244, v169\n"
"v_mfma_f32_16x16x32_bf16 v[136:139], v[204:207], v[88:91], v[136:139]\n"
"ds_read_b64_tr_b16 v[198:199], v241 offset:17024\n"
"s_waitcnt lgkmcnt(8)\n"
"v_mfma_f32_16x16x32_bf16 v[140:143], v[208:211], v[64:67], v[140:143]\n"
"ds_read_b64_tr_b16 v[200:201], v241 offset:17152\n"
"v_cvt_pk_bf16_f32 v36, v168, v169\n"
"v_mfma_f32_16x16x32_bf16 v[140:143], v[212:215], v[72:75], v[140:143]\n"
"ds_read_b64_tr_b16 v[202:203], v241 offset:17280\n"
"v_accvgpr_read_b32 v170, a134\n"
"v_mfma_f32_16x16x32_bf16 v[156:159], v[208:211], v[68:71], v[156:159]\n"
"ds_read_b64_tr_b16 v[204:205], v241 offset:17408\n"
"v_accvgpr_read_b32 v171, a135\n"
"v_mfma_f32_16x16x32_bf16 v[156:159], v[212:215], v[76:79], v[156:159]\n"
"ds_read_b64_tr_b16 v[206:207], v241 offset:17536\n"
"s_waitcnt lgkmcnt(8)\n"
"v_mfma_f32_16x16x32_bf16 v[156:159], v[216:219], v[84:87], v[156:159]\n"
"ds_read_b64_tr_b16 v[208:209], v241 offset:16672\n"
"s_mov_b32 s70, 120944\n"
"v_mfma_f32_16x16x32_bf16 v[156:159], v[220:223], v[92:95], v[156:159]\n"
"buffer_load_dwordx4 v255, s[60:63], s70 offen offset:1040 lds\n"
"ds_read_b64_tr_b16 v[210:211], v241 offset:16800\n"
"v_mfma_f32_16x16x32_bf16 v[140:143], v[216:219], v[80:83], v[140:143]\n"
"ds_read_b64_tr_b16 v[212:213], v241 offset:16928\n"
"v_mul_f32_e32 v170, v244, v170\n"
"v_mul_f32_e32 v171, v244, v171\n"
"v_accvgpr_read_b32 v172, a148\n"
"v_accvgpr_read_b32 v173, a149\n"
"v_accvgpr_read_b32 v174, a150\n"
"v_cvt_pk_bf16_f32 v37, v170, v171\n"
"v_accvgpr_read_b32 v175, a151\n"
"v_pk_mul_f32 v[172:173], v[172:173], v[244:245] op_sel:[0,0] op_sel_hi:[1,0]\n"
"v_accvgpr_read_b32 v176, a160\n"
"v_accvgpr_read_b32 v177, a161\n"
"v_pk_mul_f32 v[174:175], v[174:175], v[244:245] op_sel:[0,0] op_sel_hi:[1,0]\n"
"v_cvt_pk_bf16_f32 v38, v172, v173\n"
"v_accvgpr_read_b32 v178, a162\n"
"v_pk_mul_f32 v[176:177], v[176:177], v[242:243] op_sel:[0,1] op_sel_hi:[1,1]\n"
"v_cvt_pk_bf16_f32 v39, v174, v175\n"
"v_accvgpr_read_b32 v179, a163\n"
"v_accvgpr_read_b32 v180, a176\n"
"v_cvt_pk_bf16_f32 v40, v176, v177\n"
"v_accvgpr_read_b32 v181, a177\n"
"v_pk_mul_f32 v[178:179], v[178:179], v[242:243] op_sel:[0,1] op_sel_hi:[1,1]\n"
"v_accvgpr_read_b32 v182, a178\n"
"v_accvgpr_read_b32 v183, a179\n"
"v_pk_mul_f32 v[180:181], v[180:181], v[242:243] op_sel:[0,1] op_sel_hi:[1,1]\n"
"v_cvt_pk_bf16_f32 v41, v178, v179\n"
"v_accvgpr_read_b32 v184, a164\n"
"v_pk_mul_f32 v[182:183], v[182:183], v[242:243] op_sel:[0,1] op_sel_hi:[1,1]\n"
"v_cvt_pk_bf16_f32 v42, v180, v181\n"
"v_accvgpr_read_b32 v185, a165\n"
"v_accvgpr_read_b32 v186, a166\n"
"v_cvt_pk_bf16_f32 v43, v182, v183\n"
"v_accvgpr_read_b32 v187, a167\n"
"v_pk_mul_f32 v[184:185], v[184:185], v[244:245] op_sel:[0,0] op_sel_hi:[1,0]\n"
"v_accvgpr_read_b32 v188, a180\n"
"v_accvgpr_read_b32 v189, a181\n"
"v_pk_mul_f32 v[186:187], v[186:187], v[244:245] op_sel:[0,0] op_sel_hi:[1,0]\n"
"v_cvt_pk_bf16_f32 v44, v184, v185\n"
"v_accvgpr_read_b32 v190, a182\n"
"v_pk_mul_f32 v[188:189], v[188:189], v[244:245] op_sel:[0,0] op_sel_hi:[1,0]\n"
"v_cvt_pk_bf16_f32 v45, v186, v187\n"
"v_accvgpr_read_b32 v191, a183\n"
"v_cvt_pk_bf16_f32 v46, v188, v189\n"
"v_pk_mul_f32 v[190:191], v[190:191], v[244:245] op_sel:[0,0] op_sel_hi:[1,0]\n"
"v_cvt_pk_bf16_f32 v47, v190, v191\n"
"v_mfma_f32_16x16x32_bf16 v[140:143], v[220:223], v[88:91], v[140:143]\n"
"ds_read_b64_tr_b16 v[214:215], v241 offset:17056\n"
"s_waitcnt lgkmcnt(8)\n"
"v_mfma_f32_16x16x32_bf16 v[128:131], v[192:195], v[32:35], v[128:131]\n"
"ds_read_b64_tr_b16 v[216:217], v241 offset:17184\n"
"v_accvgpr_read_b32 v160, a192\n"
"v_mfma_f32_16x16x32_bf16 v[128:131], v[196:199], v[40:43], v[128:131]\n"
"ds_read_b64_tr_b16 v[218:219], v241 offset:17312\n"
"v_accvgpr_read_b32 v161, a193\n"
"v_mfma_f32_16x16x32_bf16 v[144:147], v[192:195], v[36:39], v[144:147]\n"
"ds_read_b64_tr_b16 v[220:221], v241 offset:17440\n"
"v_mul_f32_e32 v160, v243, v160\n"
"v_mul_f32_e32 v161, v243, v161\n"
"v_accvgpr_read_b32 v162, a194\n"
"v_accvgpr_read_b32 v163, a195\n"
"v_accvgpr_read_b32 v164, a208\n"
"v_cvt_pk_bf16_f32 v48, v160, v161\n"
"v_accvgpr_read_b32 v165, a209\n"
"v_pk_mul_f32 v[162:163], v[162:163], v[242:243] op_sel:[0,1] op_sel_hi:[1,1]\n"
"v_accvgpr_read_b32 v166, a210\n"
"v_accvgpr_read_b32 v167, a211\n"
"v_pk_mul_f32 v[164:165], v[164:165], v[242:243] op_sel:[0,1] op_sel_hi:[1,1]\n"
"v_cvt_pk_bf16_f32 v49, v162, v163\n"
"v_accvgpr_read_b32 v168, a196\n"
"v_pk_mul_f32 v[166:167], v[166:167], v[242:243] op_sel:[0,1] op_sel_hi:[1,1]\n"
"v_cvt_pk_bf16_f32 v50, v164, v165\n"
"v_accvgpr_read_b32 v169, a197\n"
"v_accvgpr_read_b32 v170, a198\n"
"v_cvt_pk_bf16_f32 v51, v166, v167\n"
"v_accvgpr_read_b32 v171, a199\n"
"v_pk_mul_f32 v[168:169], v[168:169], v[244:245] op_sel:[0,0] op_sel_hi:[1,0]\n"
"v_accvgpr_read_b32 v172, a212\n"
"v_accvgpr_read_b32 v173, a213\n"
"v_pk_mul_f32 v[170:171], v[170:171], v[244:245] op_sel:[0,0] op_sel_hi:[1,0]\n"
"v_cvt_pk_bf16_f32 v52, v168, v169\n"
"v_accvgpr_read_b32 v174, a214\n"
"v_pk_mul_f32 v[172:173], v[172:173], v[244:245] op_sel:[0,0] op_sel_hi:[1,0]\n"
"v_cvt_pk_bf16_f32 v53, v170, v171\n"
"v_accvgpr_read_b32 v175, a215\n"
"v_accvgpr_read_b32 v176, a224\n"
"v_cvt_pk_bf16_f32 v54, v172, v173\n"
"v_accvgpr_read_b32 v177, a225\n"
"v_pk_mul_f32 v[174:175], v[174:175], v[244:245] op_sel:[0,0] op_sel_hi:[1,0]\n"
"v_accvgpr_read_b32 v178, a226\n"
"v_accvgpr_read_b32 v179, a227\n"
"v_pk_mul_f32 v[176:177], v[176:177], v[242:243] op_sel:[0,1] op_sel_hi:[1,1]\n"
"v_cvt_pk_bf16_f32 v55, v174, v175\n"
"v_accvgpr_read_b32 v180, a240\n"
"v_pk_mul_f32 v[178:179], v[178:179], v[242:243] op_sel:[0,1] op_sel_hi:[1,1]\n"
"v_cvt_pk_bf16_f32 v56, v176, v177\n"
"v_accvgpr_read_b32 v181, a241\n"
"v_accvgpr_read_b32 v182, a242\n"
"v_cvt_pk_bf16_f32 v57, v178, v179\n"
"v_accvgpr_read_b32 v183, a243\n"
"v_pk_mul_f32 v[180:181], v[180:181], v[242:243] op_sel:[0,1] op_sel_hi:[1,1]\n"
"v_accvgpr_read_b32 v184, a228\n"
"v_accvgpr_read_b32 v185, a229\n"
"v_pk_mul_f32 v[182:183], v[182:183], v[242:243] op_sel:[0,1] op_sel_hi:[1,1]\n"
"v_cvt_pk_bf16_f32 v58, v180, v181\n"
"v_accvgpr_read_b32 v186, a230\n"
"v_pk_mul_f32 v[184:185], v[184:185], v[244:245] op_sel:[0,0] op_sel_hi:[1,0]\n"
"v_cvt_pk_bf16_f32 v59, v182, v183\n"
"v_accvgpr_read_b32 v187, a231\n"
"v_accvgpr_read_b32 v188, a244\n"
"v_cvt_pk_bf16_f32 v60, v184, v185\n"
"v_accvgpr_read_b32 v189, a245\n"
"v_pk_mul_f32 v[186:187], v[186:187], v[244:245] op_sel:[0,0] op_sel_hi:[1,0]\n"
"v_accvgpr_read_b32 v190, a246\n"
"v_accvgpr_read_b32 v191, a247\n"
"v_pk_mul_f32 v[188:189], v[188:189], v[244:245] op_sel:[0,0] op_sel_hi:[1,0]\n"
"v_cvt_pk_bf16_f32 v61, v186, v187\n"
"v_pk_mul_f32 v[190:191], v[190:191], v[244:245] op_sel:[0,0] op_sel_hi:[1,0]\n"
"v_cvt_pk_bf16_f32 v62, v188, v189\n"
"v_cvt_pk_bf16_f32 v63, v190, v191\n"
"v_mfma_f32_16x16x32_bf16 v[144:147], v[196:199], v[44:47], v[144:147]\n"
"ds_read_b64_tr_b16 v[222:223], v241 offset:17568\n"
"s_waitcnt lgkmcnt(8)\n"
"v_mfma_f32_16x16x32_bf16 v[144:147], v[200:203], v[52:55], v[144:147]\n"
"ds_read_b64_tr_b16 v[192:193], v241 offset:16704\n"
"s_mov_b32 s69, 116320\n"
"v_mfma_f32_16x16x32_bf16 v[144:147], v[204:207], v[60:63], v[144:147]\n"
"buffer_load_dwordx4 v255, s[60:63], s69 offen offset:2080 lds\n"
"ds_read_b64_tr_b16 v[194:195], v241 offset:16832\n"
"v_mfma_f32_16x16x32_bf16 v[128:131], v[200:203], v[48:51], v[128:131]\n"
"ds_read_b64_tr_b16 v[196:197], v241 offset:16960\n"
"v_mfma_f32_16x16x32_bf16 v[128:131], v[204:207], v[56:59], v[128:131]\n"
"ds_read_b64_tr_b16 v[198:199], v241 offset:17088\n"
"s_waitcnt lgkmcnt(8)\n"
"v_mfma_f32_16x16x32_bf16 v[132:135], v[208:211], v[32:35], v[132:135]\n"
"ds_read_b64_tr_b16 v[200:201], v241 offset:17216\n"
"v_mfma_f32_16x16x32_bf16 v[132:135], v[212:215], v[40:43], v[132:135]\n"
"ds_read_b64_tr_b16 v[202:203], v241 offset:17344\n"
"v_mfma_f32_16x16x32_bf16 v[148:151], v[208:211], v[36:39], v[148:151]\n"
"ds_read_b64_tr_b16 v[204:205], v241 offset:17472\n"
"v_mfma_f32_16x16x32_bf16 v[148:151], v[212:215], v[44:47], v[148:151]\n"
"ds_read_b64_tr_b16 v[206:207], v241 offset:17600\n"
"s_waitcnt lgkmcnt(8)\n"
"v_mfma_f32_16x16x32_bf16 v[148:151], v[216:219], v[52:55], v[148:151]\n"
"ds_read_b64_tr_b16 v[208:209], v241 offset:16736\n"
"s_mov_b32 s70, 122448\n"
"v_mfma_f32_16x16x32_bf16 v[148:151], v[220:223], v[60:63], v[148:151]\n"
"buffer_load_dwordx4 v255, s[60:63], s70 offen offset:3120 lds\n"
"ds_read_b64_tr_b16 v[210:211], v241 offset:16864\n"
"v_mfma_f32_16x16x32_bf16 v[132:135], v[216:219], v[48:51], v[132:135]\n"
"ds_read_b64_tr_b16 v[212:213], v241 offset:16992\n"
"v_mfma_f32_16x16x32_bf16 v[132:135], v[220:223], v[56:59], v[132:135]\n"
"ds_read_b64_tr_b16 v[214:215], v241 offset:17120\n"
"s_waitcnt lgkmcnt(8)\n"
"v_mfma_f32_16x16x32_bf16 v[136:139], v[192:195], v[32:35], v[136:139]\n"
"ds_read_b64_tr_b16 v[216:217], v241 offset:17248\n"
"v_mfma_f32_16x16x32_bf16 v[136:139], v[196:199], v[40:43], v[136:139]\n"
"ds_read_b64_tr_b16 v[218:219], v241 offset:17376\n"
"v_mfma_f32_16x16x32_bf16 v[152:155], v[192:195], v[36:39], v[152:155]\n"
"ds_read_b64_tr_b16 v[220:221], v241 offset:17504\n"
"v_mfma_f32_16x16x32_bf16 v[152:155], v[196:199], v[44:47], v[152:155]\n"
"ds_read_b64_tr_b16 v[222:223], v241 offset:17632\n"
"s_mov_b32 s69, 115712\n"
"s_waitcnt lgkmcnt(8)\n"
"v_mfma_f32_16x16x32_bf16 v[152:155], v[200:203], v[52:55], v[152:155]\n"
"ds_read_b64_tr_b16 v[192:193], v254 offset:16640\n"
"s_add_u32 m0, s68, 37440\n"
"v_mfma_f32_16x16x32_bf16 v[152:155], v[204:207], v[60:63], v[152:155]\n"
"buffer_load_dwordx4 v255, s[60:63], s69 offen offset:0 lds\n"
"ds_read_b64_tr_b16 v[194:195], v254 offset:16768\n"
"v_mfma_f32_16x16x32_bf16 v[136:139], v[200:203], v[48:51], v[136:139]\n"
"ds_read_b64_tr_b16 v[196:197], v254 offset:16896\n"
"v_mfma_f32_16x16x32_bf16 v[136:139], v[204:207], v[56:59], v[136:139]\n"
"ds_read_b64_tr_b16 v[198:199], v254 offset:17024\n"
"s_waitcnt lgkmcnt(8)\n"
"v_mfma_f32_16x16x32_bf16 v[140:143], v[208:211], v[32:35], v[140:143]\n"
"ds_read_b64_tr_b16 v[200:201], v254 offset:17152\n"
"v_mfma_f32_16x16x32_bf16 v[140:143], v[212:215], v[40:43], v[140:143]\n"
"ds_read_b64_tr_b16 v[202:203], v254 offset:17280\n"
"v_mfma_f32_16x16x32_bf16 v[156:159], v[208:211], v[36:39], v[156:159]\n"
"ds_read_b64_tr_b16 v[204:205], v254 offset:17408\n"
"v_mfma_f32_16x16x32_bf16 v[156:159], v[212:215], v[44:47], v[156:159]\n"
"ds_read_b64_tr_b16 v[206:207], v254 offset:17536\n"
"s_waitcnt lgkmcnt(8)\n"
"v_mfma_f32_16x16x32_bf16 v[156:159], v[216:219], v[52:55], v[156:159]\n"
"ds_read_b64_tr_b16 v[208:209], v254 offset:16672\n"
"s_mov_b32 s70, 121840\n"
"v_mfma_f32_16x16x32_bf16 v[156:159], v[220:223], v[60:63], v[156:159]\n"
"buffer_load_dwordx4 v255, s[60:63], s70 offen offset:1040 lds\n"
"ds_read_b64_tr_b16 v[210:211], v254 offset:16800\n"
"v_mfma_f32_16x16x32_bf16 v[140:143], v[216:219], v[48:51], v[140:143]\n"
"ds_read_b64_tr_b16 v[212:213], v254 offset:16928\n"
"v_mfma_f32_16x16x32_bf16 v[140:143], v[220:223], v[56:59], v[140:143]\n"
"ds_read_b64_tr_b16 v[214:215], v254 offset:17056\n"
"s_waitcnt lgkmcnt(8)\n"
"v_mfma_f32_16x16x32_bf16 v[128:131], v[192:195], v[96:99], v[128:131]\n"
"ds_read_b64_tr_b16 v[216:217], v254 offset:17184\n"
"v_mfma_f32_16x16x32_bf16 v[128:131], v[196:199], v[104:107], v[128:131]\n"
"ds_read_b64_tr_b16 v[218:219], v254 offset:17312\n"
"v_mfma_f32_16x16x32_bf16 v[144:147], v[192:195], v[100:103], v[144:147]\n"
"ds_read_b64_tr_b16 v[220:221], v254 offset:17440\n"
"v_mfma_f32_16x16x32_bf16 v[144:147], v[196:199], v[108:111], v[144:147]\n"
"ds_read_b64_tr_b16 v[222:223], v254 offset:17568\n"
"s_waitcnt lgkmcnt(8)\n"
"v_mfma_f32_16x16x32_bf16 v[144:147], v[200:203], v[116:119], v[144:147]\n"
"ds_read_b64_tr_b16 v[192:193], v254 offset:16704\n"
"s_mov_b32 s69, 117216\n"
"v_mfma_f32_16x16x32_bf16 v[144:147], v[204:207], v[124:127], v[144:147]\n"
"buffer_load_dwordx4 v255, s[60:63], s69 offen offset:2080 lds\n"
"ds_read_b64_tr_b16 v[194:195], v254 offset:16832\n"
"v_mfma_f32_16x16x32_bf16 v[128:131], v[200:203], v[112:115], v[128:131]\n"
"ds_read_b64_tr_b16 v[196:197], v254 offset:16960\n"
"v_mfma_f32_16x16x32_bf16 v[128:131], v[204:207], v[120:123], v[128:131]\n"
"ds_read_b64_tr_b16 v[198:199], v254 offset:17088\n"
"s_waitcnt lgkmcnt(8)\n"
"v_mfma_f32_16x16x32_bf16 v[132:135], v[208:211], v[96:99], v[132:135]\n"
"ds_read_b64_tr_b16 v[200:201], v254 offset:17216\n"
"v_mfma_f32_16x16x32_bf16 v[132:135], v[212:215], v[104:107], v[132:135]\n"
"ds_read_b64_tr_b16 v[202:203], v254 offset:17344\n"
"v_mfma_f32_16x16x32_bf16 v[148:151], v[208:211], v[100:103], v[148:151]\n"
"ds_read_b64_tr_b16 v[204:205], v254 offset:17472\n"
"v_mfma_f32_16x16x32_bf16 v[148:151], v[212:215], v[108:111], v[148:151]\n"
"ds_read_b64_tr_b16 v[206:207], v254 offset:17600\n"
"s_waitcnt lgkmcnt(8)\n"
"v_mfma_f32_16x16x32_bf16 v[148:151], v[216:219], v[116:119], v[148:151]\n"
"ds_read_b64_tr_b16 v[208:209], v254 offset:16736\n"
"s_mov_b32 s70, 123344\n"
"v_mfma_f32_16x16x32_bf16 v[148:151], v[220:223], v[124:127], v[148:151]\n"
"buffer_load_dwordx4 v255, s[60:63], s70 offen offset:3120 lds\n"
"ds_read_b64_tr_b16 v[210:211], v254 offset:16864\n"
"v_mfma_f32_16x16x32_bf16 v[132:135], v[216:219], v[112:115], v[132:135]\n"
"ds_read_b64_tr_b16 v[212:213], v254 offset:16992\n"
"v_mfma_f32_16x16x32_bf16 v[132:135], v[220:223], v[120:123], v[132:135]\n"
"ds_read_b64_tr_b16 v[214:215], v254 offset:17120\n"
"s_waitcnt lgkmcnt(8)\n"
"v_mfma_f32_16x16x32_bf16 v[136:139], v[192:195], v[96:99], v[136:139]\n"
"ds_read_b64_tr_b16 v[216:217], v254 offset:17248\n"
"s_waitcnt vmcnt(8)\n"
"v_mfma_f32_16x16x32_bf16 v[136:139], v[196:199], v[104:107], v[136:139]\n"
"ds_read_b64_tr_b16 v[218:219], v254 offset:17376\n"
"v_mfma_f32_16x16x32_bf16 v[152:155], v[192:195], v[100:103], v[152:155]\n"
"ds_read_b64_tr_b16 v[220:221], v254 offset:17504\n"
"s_barrier\n"
"v_mfma_f32_16x16x32_bf16 v[152:155], v[196:199], v[108:111], v[152:155]\n"
"ds_read_b64_tr_b16 v[222:223], v254 offset:17632\n"
"s_mov_b32 s69, 256\n"
"s_waitcnt lgkmcnt(8)\n"
"v_mfma_f32_16x16x32_bf16 v[152:155], v[200:203], v[116:119], v[152:155]\n"
"ds_read_b64_tr_b16 v[192:193], v241 offset:0\n"
"s_add_u32 m0, s68, 49920\n"
"v_mfma_f32_16x16x32_bf16 v[152:155], v[204:207], v[124:127], v[152:155]\n"
"buffer_load_dwordx4 v255, s[60:63], s69 offen offset:0 lds\n"
"ds_read_b64_tr_b16 v[194:195], v241 offset:128\n"
"v_mfma_f32_16x16x32_bf16 v[136:139], v[200:203], v[112:115], v[136:139]\n"
"ds_read_b64_tr_b16 v[196:197], v241 offset:256\n"
"v_mfma_f32_16x16x32_bf16 v[136:139], v[204:207], v[120:123], v[136:139]\n"
"ds_read_b64_tr_b16 v[198:199], v241 offset:384\n"
"s_waitcnt lgkmcnt(8)\n"
"v_mfma_f32_16x16x32_bf16 v[140:143], v[208:211], v[96:99], v[140:143]\n"
"ds_read_b64_tr_b16 v[200:201], v241 offset:512\n"
"v_mfma_f32_16x16x32_bf16 v[140:143], v[212:215], v[104:107], v[140:143]\n"
"ds_read_b64_tr_b16 v[202:203], v241 offset:640\n"
"v_mfma_f32_16x16x32_bf16 v[156:159], v[208:211], v[100:103], v[156:159]\n"
"ds_read_b64_tr_b16 v[204:205], v241 offset:768\n"
"v_mfma_f32_16x16x32_bf16 v[156:159], v[212:215], v[108:111], v[156:159]\n"
"ds_read_b64_tr_b16 v[206:207], v241 offset:896\n"
"s_waitcnt lgkmcnt(8)\n"
"v_mfma_f32_16x16x32_bf16 v[156:159], v[216:219], v[116:119], v[156:159]\n"
"ds_read_b64_tr_b16 v[208:209], v241 offset:32\n"
"s_mov_b32 s70, 6384\n"
"v_mfma_f32_16x16x32_bf16 v[156:159], v[220:223], v[124:127], v[156:159]\n"
"buffer_load_dwordx4 v255, s[60:63], s70 offen offset:1040 lds\n"
"ds_read_b64_tr_b16 v[210:211], v241 offset:160\n"
"v_mfma_f32_16x16x32_bf16 v[140:143], v[216:219], v[112:115], v[140:143]\n"
"ds_read_b64_tr_b16 v[212:213], v241 offset:288\n"
"v_mfma_f32_16x16x32_bf16 v[140:143], v[220:223], v[120:123], v[140:143]\n"
"ds_read_b64_tr_b16 v[214:215], v241 offset:416\n"
"s_waitcnt lgkmcnt(8)\n"
"v_mfma_f32_16x16x32_bf16 v[160:163], v[192:195], v[0:3], 0\n"
"ds_read_b64_tr_b16 v[216:217], v241 offset:544\n"
"s_mov_b64 s[56:57], s[64:65]\n"
"v_mfma_f32_16x16x32_bf16 v[160:163], v[196:199], v[8:11], v[160:163]\n"
"ds_read_b64_tr_b16 v[218:219], v241 offset:672\n"
"s_mov_b32 s59, 0x20000\n"
"v_mfma_f32_16x16x32_bf16 v[176:179], v[192:195], v[4:7], 0\n"
"ds_read_b64_tr_b16 v[220:221], v241 offset:800\n"
"s_lshl_b32 s77, s83, 1\n"
"v_mfma_f32_16x16x32_bf16 v[176:179], v[196:199], v[12:15], v[176:179]\n"
"ds_read_b64_tr_b16 v[222:223], v241 offset:928\n"
"s_waitcnt lgkmcnt(8)\n"
"v_mfma_f32_16x16x32_bf16 v[176:179], v[200:203], v[20:23], v[176:179]\n"
"ds_read_b64_tr_b16 v[192:193], v241 offset:64\n"
"s_mov_b32 s69, 1760\n"
"v_mfma_f32_16x16x32_bf16 v[176:179], v[204:207], v[28:31], v[176:179]\n"
"buffer_load_dwordx4 v255, s[60:63], s69 offen offset:2080 lds\n"
"ds_read_b64_tr_b16 v[194:195], v241 offset:192\n"
"v_mfma_f32_16x16x32_bf16 v[160:163], v[200:203], v[16:19], v[160:163]\n"
"ds_read_b64_tr_b16 v[196:197], v241 offset:320\n"
"v_cvt_pk_bf16_f32 v224, v128, v129\n"
"v_mfma_f32_16x16x32_bf16 v[160:163], v[204:207], v[24:27], v[160:163]\n"
"ds_read_b64_tr_b16 v[198:199], v241 offset:448\n"
"s_waitcnt lgkmcnt(8)\n"
"v_mfma_f32_16x16x32_bf16 v[164:167], v[208:211], v[0:3], 0\n"
"ds_read_b64_tr_b16 v[200:201], v241 offset:576\n"
"v_cvt_pk_bf16_f32 v225, v130, v131\n"
"v_mfma_f32_16x16x32_bf16 v[164:167], v[212:215], v[8:11], v[164:167]\n"
"ds_read_b64_tr_b16 v[202:203], v241 offset:704\n"
"v_cvt_pk_bf16_f32 v226, v132, v133\n"
"v_mfma_f32_16x16x32_bf16 v[180:183], v[208:211], v[4:7], 0\n"
"ds_read_b64_tr_b16 v[204:205], v241 offset:832\n"
"v_cvt_pk_bf16_f32 v227, v134, v135\n"
"v_mfma_f32_16x16x32_bf16 v[180:183], v[212:215], v[12:15], v[180:183]\n"
"ds_read_b64_tr_b16 v[206:207], v241 offset:960\n"
"s_waitcnt lgkmcnt(8)\n"
"v_mfma_f32_16x16x32_bf16 v[180:183], v[216:219], v[20:23], v[180:183]\n"
"ds_read_b64_tr_b16 v[208:209], v241 offset:96\n"
"s_mov_b32 s70, 7888\n"
"v_mfma_f32_16x16x32_bf16 v[180:183], v[220:223], v[28:31], v[180:183]\n"
"buffer_load_dwordx4 v255, s[60:63], s70 offen offset:3120 lds\n"
"ds_read_b64_tr_b16 v[210:211], v241 offset:224\n"
"v_mfma_f32_16x16x32_bf16 v[164:167], v[216:219], v[16:19], v[164:167]\n"
"ds_read_b64_tr_b16 v[212:213], v241 offset:352\n"
"v_accvgpr_write_b32 a0, v224\n"
"v_mfma_f32_16x16x32_bf16 v[164:167], v[220:223], v[24:27], v[164:167]\n"
"ds_read_b64_tr_b16 v[214:215], v241 offset:480\n"
"s_waitcnt lgkmcnt(8)\n"
"v_mfma_f32_16x16x32_bf16 v[168:171], v[192:195], v[0:3], 0\n"
"ds_read_b64_tr_b16 v[216:217], v241 offset:608\n"
"v_accvgpr_write_b32 a1, v225\n"
"v_mfma_f32_16x16x32_bf16 v[168:171], v[196:199], v[8:11], v[168:171]\n"
"ds_read_b64_tr_b16 v[218:219], v241 offset:736\n"
"v_accvgpr_write_b32 a2, v226\n"
"v_mfma_f32_16x16x32_bf16 v[184:187], v[192:195], v[4:7], 0\n"
"ds_read_b64_tr_b16 v[220:221], v241 offset:864\n"
"v_accvgpr_write_b32 a3, v227\n"
"v_mfma_f32_16x16x32_bf16 v[184:187], v[196:199], v[12:15], v[184:187]\n"
"ds_read_b64_tr_b16 v[222:223], v241 offset:992\n"
"s_mov_b32 s69, 1152\n"
"s_waitcnt lgkmcnt(8)\n"
"v_mfma_f32_16x16x32_bf16 v[184:187], v[200:203], v[20:23], v[184:187]\n"
"ds_read_b64_tr_b16 v[192:193], v254 offset:0\n"
"s_add_u32 m0, s68, 54080\n"
"v_mfma_f32_16x16x32_bf16 v[184:187], v[204:207], v[28:31], v[184:187]\n"
"buffer_load_dwordx4 v255, s[60:63], s69 offen offset:0 lds\n"
"ds_read_b64_tr_b16 v[194:195], v254 offset:128\n"
"v_mfma_f32_16x16x32_bf16 v[168:171], v[200:203], v[16:19], v[168:171]\n"
"ds_read_b64_tr_b16 v[196:197], v254 offset:256\n"
"s_or_b32 s80, s77, 1\n"
"v_mfma_f32_16x16x32_bf16 v[168:171], v[204:207], v[24:27], v[168:171]\n"
"ds_read_b64_tr_b16 v[198:199], v254 offset:384\n"
"s_waitcnt lgkmcnt(8)\n"
"v_mfma_f32_16x16x32_bf16 v[172:175], v[208:211], v[0:3], 0\n"
"ds_read_b64_tr_b16 v[200:201], v254 offset:512\n"
"s_cmp_eq_u32 s80, s87\n"
"s_cselect_b32 s58, 0, s66\n"
"v_mfma_f32_16x16x32_bf16 v[172:175], v[212:215], v[8:11], v[172:175]\n"
"ds_read_b64_tr_b16 v[202:203], v254 offset:640\n"
"s_cmp_eq_u32 s77, s87\n"
"s_cselect_b32 s66, 0, s66\n"
"v_mfma_f32_16x16x32_bf16 v[188:191], v[208:211], v[4:7], 0\n"
"ds_read_b64_tr_b16 v[204:205], v254 offset:768\n"
"s_lshl2_add_u32 s77, s86, s83\n"
"v_mfma_f32_16x16x32_bf16 v[188:191], v[212:215], v[12:15], v[188:191]\n"
"ds_read_b64_tr_b16 v[206:207], v254 offset:896\n"
"s_waitcnt lgkmcnt(8)\n"
"v_mfma_f32_16x16x32_bf16 v[188:191], v[216:219], v[20:23], v[188:191]\n"
"ds_read_b64_tr_b16 v[208:209], v254 offset:32\n"
"s_mov_b32 s70, 7280\n"
"v_mfma_f32_16x16x32_bf16 v[188:191], v[220:223], v[28:31], v[188:191]\n"
"buffer_load_dwordx4 v255, s[60:63], s70 offen offset:1040 lds\n"
"ds_read_b64_tr_b16 v[210:211], v254 offset:160\n"
"v_mfma_f32_16x16x32_bf16 v[172:175], v[216:219], v[16:19], v[172:175]\n"
"ds_read_b64_tr_b16 v[212:213], v254 offset:288\n"
"s_cmp_eq_u32 s77, 7\n"
"s_cselect_b32 s58, 0, s58\n"
"v_mfma_f32_16x16x32_bf16 v[172:175], v[220:223], v[24:27], v[172:175]\n"
"ds_read_b64_tr_b16 v[214:215], v254 offset:416\n"
"s_waitcnt lgkmcnt(8)\n"
"v_mfma_f32_16x16x32_bf16 v[160:163], v[192:195], v[64:67], v[160:163]\n"
"ds_read_b64_tr_b16 v[216:217], v254 offset:544\n"
"s_add_u32 s77, s87, 1\n"
"v_mfma_f32_16x16x32_bf16 v[160:163], v[196:199], v[72:75], v[160:163]\n"
"ds_read_b64_tr_b16 v[218:219], v254 offset:672\n"
"s_and_b32 s77, s77, 7\n"
"v_mfma_f32_16x16x32_bf16 v[176:179], v[192:195], v[68:71], v[176:179]\n"
"ds_read_b64_tr_b16 v[220:221], v254 offset:800\n"
"s_mul_i32 s77, s77, 14336\n"
"v_mfma_f32_16x16x32_bf16 v[176:179], v[196:199], v[76:79], v[176:179]\n"
"ds_read_b64_tr_b16 v[222:223], v254 offset:928\n"
"s_waitcnt lgkmcnt(8)\n"
"v_mfma_f32_16x16x32_bf16 v[176:179], v[200:203], v[84:87], v[176:179]\n"
"ds_read_b64_tr_b16 v[192:193], v254 offset:64\n"
"s_mov_b32 s69, 2656\n"
"v_mfma_f32_16x16x32_bf16 v[176:179], v[204:207], v[92:95], v[176:179]\n"
"buffer_load_dwordx4 v255, s[60:63], s69 offen offset:2080 lds\n"
"ds_read_b64_tr_b16 v[194:195], v254 offset:192\n"
"v_mfma_f32_16x16x32_bf16 v[160:163], v[200:203], v[80:83], v[160:163]\n"
"ds_read_b64_tr_b16 v[196:197], v254 offset:320\n"
"v_cvt_pk_bf16_f32 v228, v136, v137\n"
"v_mfma_f32_16x16x32_bf16 v[160:163], v[204:207], v[88:91], v[160:163]\n"
"ds_read_b64_tr_b16 v[198:199], v254 offset:448\n"
"s_waitcnt lgkmcnt(8)\n"
"v_mfma_f32_16x16x32_bf16 v[164:167], v[208:211], v[64:67], v[164:167]\n"
"ds_read_b64_tr_b16 v[200:201], v254 offset:576\n"
"v_cvt_pk_bf16_f32 v229, v138, v139\n"
"v_mfma_f32_16x16x32_bf16 v[164:167], v[212:215], v[72:75], v[164:167]\n"
"ds_read_b64_tr_b16 v[202:203], v254 offset:704\n"
"v_cvt_pk_bf16_f32 v230, v140, v141\n"
"v_mfma_f32_16x16x32_bf16 v[180:183], v[208:211], v[68:71], v[180:183]\n"
"ds_read_b64_tr_b16 v[204:205], v254 offset:832\n"
"v_cvt_pk_bf16_f32 v231, v142, v143\n"
"v_mfma_f32_16x16x32_bf16 v[180:183], v[212:215], v[76:79], v[180:183]\n"
"ds_read_b64_tr_b16 v[206:207], v254 offset:960\n"
"s_waitcnt lgkmcnt(8)\n"
"v_mfma_f32_16x16x32_bf16 v[180:183], v[216:219], v[84:87], v[180:183]\n"
"ds_read_b64_tr_b16 v[208:209], v254 offset:96\n"
"s_mov_b32 s70, 8784\n"
"v_mfma_f32_16x16x32_bf16 v[180:183], v[220:223], v[92:95], v[180:183]\n"
"buffer_load_dwordx4 v255, s[60:63], s70 offen offset:3120 lds\n"
"ds_read_b64_tr_b16 v[210:211], v254 offset:224\n"
"v_mfma_f32_16x16x32_bf16 v[164:167], v[216:219], v[80:83], v[164:167]\n"
"ds_read_b64_tr_b16 v[212:213], v254 offset:352\n"
"v_accvgpr_write_b32 a4, v228\n"
"v_mfma_f32_16x16x32_bf16 v[164:167], v[220:223], v[88:91], v[164:167]\n"
"ds_read_b64_tr_b16 v[214:215], v254 offset:480\n"
"s_waitcnt lgkmcnt(8)\n"
"v_mfma_f32_16x16x32_bf16 v[168:171], v[192:195], v[64:67], v[168:171]\n"
"ds_read_b64_tr_b16 v[216:217], v254 offset:608\n"
"s_waitcnt vmcnt(8)\n"
"v_mfma_f32_16x16x32_bf16 v[168:171], v[196:199], v[72:75], v[168:171]\n"
"ds_read_b64_tr_b16 v[218:219], v254 offset:736\n"
"v_accvgpr_write_b32 a5, v229\n"
"v_mfma_f32_16x16x32_bf16 v[184:187], v[192:195], v[68:71], v[184:187]\n"
"ds_read_b64_tr_b16 v[220:221], v254 offset:864\n"
"s_barrier\n"
"v_mfma_f32_16x16x32_bf16 v[184:187], v[196:199], v[76:79], v[184:187]\n"
"ds_read_b64_tr_b16 v[222:223], v254 offset:992\n"
"s_mov_b32 s69, 114944\n"
"s_waitcnt lgkmcnt(8)\n"
"v_mfma_f32_16x16x32_bf16 v[184:187], v[200:203], v[84:87], v[184:187]\n"
"ds_read_b64_tr_b16 v[192:193], v241 offset:33280\n"
"s_add_u32 m0, s68, 16640\n"
"v_mfma_f32_16x16x32_bf16 v[184:187], v[204:207], v[92:95], v[184:187]\n"
"buffer_load_dwordx4 v255, s[60:63], s69 offen offset:0 lds\n"
"ds_read_b64_tr_b16 v[194:195], v241 offset:33408\n"
"v_mfma_f32_16x16x32_bf16 v[168:171], v[200:203], v[80:83], v[168:171]\n"
"ds_read_b64_tr_b16 v[196:197], v241 offset:33536\n"
"v_accvgpr_write_b32 a6, v230\n"
"v_mfma_f32_16x16x32_bf16 v[168:171], v[204:207], v[88:91], v[168:171]\n"
"ds_read_b64_tr_b16 v[198:199], v241 offset:33664\n"
"s_waitcnt lgkmcnt(8)\n"
"v_mfma_f32_16x16x32_bf16 v[172:175], v[208:211], v[64:67], v[172:175]\n"
"ds_read_b64_tr_b16 v[200:201], v241 offset:33792\n"
"v_accvgpr_write_b32 a7, v231\n"
"v_mfma_f32_16x16x32_bf16 v[172:175], v[212:215], v[72:75], v[172:175]\n"
"ds_read_b64_tr_b16 v[202:203], v241 offset:33920\n"
"s_add_u32 s45, s77, s91\n"
"v_mfma_f32_16x16x32_bf16 v[188:191], v[208:211], v[68:71], v[188:191]\n"
"ds_read_b64_tr_b16 v[204:205], v241 offset:34048\n"
"s_add_u32 s77, s87, 2\n"
"v_mfma_f32_16x16x32_bf16 v[188:191], v[212:215], v[76:79], v[188:191]\n"
"ds_read_b64_tr_b16 v[206:207], v241 offset:34176\n"
"s_waitcnt lgkmcnt(8)\n"
"v_mfma_f32_16x16x32_bf16 v[188:191], v[216:219], v[84:87], v[188:191]\n"
"ds_read_b64_tr_b16 v[208:209], v241 offset:33312\n"
"s_mov_b32 s70, 121072\n"
"v_mfma_f32_16x16x32_bf16 v[188:191], v[220:223], v[92:95], v[188:191]\n"
"buffer_load_dwordx4 v255, s[60:63], s70 offen offset:1040 lds\n"
"ds_read_b64_tr_b16 v[210:211], v241 offset:33440\n"
"v_mfma_f32_16x16x32_bf16 v[172:175], v[216:219], v[80:83], v[172:175]\n"
"ds_read_b64_tr_b16 v[212:213], v241 offset:33568\n"
"s_and_b32 s77, s77, 7\n"
"v_mfma_f32_16x16x32_bf16 v[172:175], v[220:223], v[88:91], v[172:175]\n"
"ds_read_b64_tr_b16 v[214:215], v241 offset:33696\n"
"s_waitcnt lgkmcnt(8)\n"
"v_mfma_f32_16x16x32_bf16 v[160:163], v[192:195], v[32:35], v[160:163]\n"
"ds_read_b64_tr_b16 v[216:217], v241 offset:33824\n"
"s_mul_i32 s77, s77, 14336\n"
"v_mfma_f32_16x16x32_bf16 v[160:163], v[196:199], v[40:43], v[160:163]\n"
"ds_read_b64_tr_b16 v[218:219], v241 offset:33952\n"
"s_add_u32 s46, s77, s91\n"
"v_mfma_f32_16x16x32_bf16 v[176:179], v[192:195], v[36:39], v[176:179]\n"
"ds_read_b64_tr_b16 v[220:221], v241 offset:34080\n"
"s_add_u32 s77, s87, 3\n"
"v_mfma_f32_16x16x32_bf16 v[176:179], v[196:199], v[44:47], v[176:179]\n"
"ds_read_b64_tr_b16 v[222:223], v241 offset:34208\n"
"s_waitcnt lgkmcnt(8)\n"
"v_mfma_f32_16x16x32_bf16 v[176:179], v[200:203], v[52:55], v[176:179]\n"
"ds_read_b64_tr_b16 v[192:193], v241 offset:33344\n"
"s_mov_b32 s69, 116448\n"
"s_branch .Lpo10\n"
".Lpg10:\n"
"s_setpc_b64 s[78:79]\n"
".Lpo10:\n"
"v_mfma_f32_16x16x32_bf16 v[176:179], v[204:207], v[60:63], v[176:179]\n"
"buffer_load_dwordx4 v255, s[60:63], s69 offen offset:2080 lds\n"
"ds_read_b64_tr_b16 v[194:195], v241 offset:33472\n"
"v_mfma_f32_16x16x32_bf16 v[160:163], v[200:203], v[48:51], v[160:163]\n"
"ds_read_b64_tr_b16 v[196:197], v241 offset:33600\n"
"v_cvt_pk_bf16_f32 v232, v144, v145\n"
"v_mfma_f32_16x16x32_bf16 v[160:163], v[204:207], v[56:59], v[160:163]\n"
"ds_read_b64_tr_b16 v[198:199], v241 offset:33728\n"
"s_waitcnt lgkmcnt(8)\n"
"v_mfma_f32_16x16x32_bf16 v[164:167], v[208:211], v[32:35], v[164:167]\n"
"ds_read_b64_tr_b16 v[200:201], v241 offset:33856\n"
"v_cvt_pk_bf16_f32 v233, v146, v147\n"
"v_mfma_f32_16x16x32_bf16 v[164:167], v[212:215], v[40:43], v[164:167]\n"
"ds_read_b64_tr_b16 v[202:203], v241 offset:33984\n"
"v_cvt_pk_bf16_f32 v234, v148, v149\n"
"v_mfma_f32_16x16x32_bf16 v[180:183], v[208:211], v[36:39], v[180:183]\n"
"ds_read_b64_tr_b16 v[204:205], v241 offset:34112\n"
"v_cvt_pk_bf16_f32 v235, v150, v151\n"
"v_mfma_f32_16x16x32_bf16 v[180:183], v[212:215], v[44:47], v[180:183]\n"
"ds_read_b64_tr_b16 v[206:207], v241 offset:34240\n"
"s_waitcnt lgkmcnt(8)\n"
"v_mfma_f32_16x16x32_bf16 v[180:183], v[216:219], v[52:55], v[180:183]\n"
"ds_read_b64_tr_b16 v[208:209], v241 offset:33376\n"
"s_mov_b32 s70, 122576\n"
"v_mfma_f32_16x16x32_bf16 v[180:183], v[220:223], v[60:63], v[180:183]\n"
"buffer_load_dwordx4 v255, s[60:63], s70 offen offset:3120 lds\n"
"ds_read_b64_tr_b16 v[210:211], v241 offset:33504\n"
"v_mfma_f32_16x16x32_bf16 v[164:167], v[216:219], v[48:51], v[164:167]\n"
"ds_read_b64_tr_b16 v[212:213], v241 offset:33632\n"
"v_accvgpr_write_b32 a8, v232\n"
"v_mfma_f32_16x16x32_bf16 v[164:167], v[220:223], v[56:59], v[164:167]\n"
"ds_read_b64_tr_b16 v[214:215], v241 offset:33760\n"
"s_waitcnt lgkmcnt(8)\n"
"v_mfma_f32_16x16x32_bf16 v[168:171], v[192:195], v[32:35], v[168:171]\n"
"ds_read_b64_tr_b16 v[216:217], v241 offset:33888\n"
"v_accvgpr_write_b32 a9, v233\n"
"v_mfma_f32_16x16x32_bf16 v[168:171], v[196:199], v[40:43], v[168:171]\n"
"ds_read_b64_tr_b16 v[218:219], v241 offset:34016\n"
"v_accvgpr_write_b32 a10, v234\n"
"v_mfma_f32_16x16x32_bf16 v[184:187], v[192:195], v[36:39], v[184:187]\n"
"ds_read_b64_tr_b16 v[220:221], v241 offset:34144\n"
"v_accvgpr_write_b32 a11, v235\n"
"v_mfma_f32_16x16x32_bf16 v[184:187], v[196:199], v[44:47], v[184:187]\n"
"ds_read_b64_tr_b16 v[222:223], v241 offset:34272\n"
"s_mov_b32 s69, 115840\n"
"s_waitcnt lgkmcnt(8)\n"
"v_mfma_f32_16x16x32_bf16 v[184:187], v[200:203], v[52:55], v[184:187]\n"
"ds_read_b64_tr_b16 v[192:193], v254 offset:33280\n"
"s_add_u32 m0, s68, 20800\n"
"v_mfma_f32_16x16x32_bf16 v[184:187], v[204:207], v[60:63], v[184:187]\n"
"buffer_load_dwordx4 v255, s[60:63], s69 offen offset:0 lds\n"
"ds_read_b64_tr_b16 v[194:195], v254 offset:33408\n"
"v_mfma_f32_16x16x32_bf16 v[168:171], v[200:203], v[48:51], v[168:171]\n"
"ds_read_b64_tr_b16 v[196:197], v254 offset:33536\n"
"s_and_b32 s77, s77, 7\n"
"v_mfma_f32_16x16x32_bf16 v[168:171], v[204:207], v[56:59], v[168:171]\n"
"ds_read_b64_tr_b16 v[198:199], v254 offset:33664\n"
"s_waitcnt lgkmcnt(8)\n"
"v_mfma_f32_16x16x32_bf16 v[172:175], v[208:211], v[32:35], v[172:175]\n"
"ds_read_b64_tr_b16 v[200:201], v254 offset:33792\n"
"s_mul_i32 s77, s77, 14336\n"
"v_mfma_f32_16x16x32_bf16 v[172:175], v[212:215], v[40:43], v[172:175]\n"
"ds_read_b64_tr_b16 v[202:203], v254 offset:33920\n"
"s_add_u32 s47, s77, s91\n"
"v_mfma_f32_16x16x32_bf16 v[188:191], v[208:211], v[36:39], v[188:191]\n"
"ds_read_b64_tr_b16 v[204:205], v254 offset:34048\n"
"s_add_u32 s77, s87, 4\n"
"v_mfma_f32_16x16x32_bf16 v[188:191], v[212:215], v[44:47], v[188:191]\n"
"ds_read_b64_tr_b16 v[206:207], v254 offset:34176\n"
"s_waitcnt lgkmcnt(8)\n"
"v_mfma_f32_16x16x32_bf16 v[188:191], v[216:219], v[52:55], v[188:191]\n"
"ds_read_b64_tr_b16 v[208:209], v254 offset:33312\n"
"s_mov_b32 s70, 121968\n"
"v_mfma_f32_16x16x32_bf16 v[188:191], v[220:223], v[60:63], v[188:191]\n"
"buffer_load_dwordx4 v255, s[60:63], s70 offen offset:1040 lds\n"
"ds_read_b64_tr_b16 v[210:211], v254 offset:33440\n"
"v_mfma_f32_16x16x32_bf16 v[172:175], v[216:219], v[48:51], v[172:175]\n"
"ds_read_b64_tr_b16 v[212:213], v254 offset:33568\n"
"s_and_b32 s77, s77, 7\n"
"v_mfma_f32_16x16x32_bf16 v[172:175], v[220:223], v[56:59], v[172:175]\n"
"ds_read_b64_tr_b16 v[214:215], v254 offset:33696\n"
"s_waitcnt lgkmcnt(8)\n"
"v_mfma_f32_16x16x32_bf16 v[160:163], v[192:195], v[96:99], v[160:163]\n"
"ds_read_b64_tr_b16 v[216:217], v254 offset:33824\n"
"s_mul_i32 s77, s77, 14336\n"
"v_mfma_f32_16x16x32_bf16 v[160:163], v[196:199], v[104:107], v[160:163]\n"
"ds_read_b64_tr_b16 v[218:219], v254 offset:33952\n"
"s_add_u32 s48, s77, s91\n"
"v_mfma_f32_16x16x32_bf16 v[176:179], v[192:195], v[100:103], v[176:179]\n"
"ds_read_b64_tr_b16 v[220:221], v254 offset:34080\n"
"s_add_u32 s77, s87, 5\n"
"v_mfma_f32_16x16x32_bf16 v[176:179], v[196:199], v[108:111], v[176:179]\n"
"ds_read_b64_tr_b16 v[222:223], v254 offset:34208\n"
"s_waitcnt lgkmcnt(8)\n"
"v_mfma_f32_16x16x32_bf16 v[176:179], v[200:203], v[116:119], v[176:179]\n"
"ds_read_b64_tr_b16 v[192:193], v254 offset:33344\n"
"s_mov_b32 s69, 117344\n"
"v_mfma_f32_16x16x32_bf16 v[176:179], v[204:207], v[124:127], v[176:179]\n"
"buffer_load_dwordx4 v255, s[60:63], s69 offen offset:2080 lds\n"
"ds_read_b64_tr_b16 v[194:195], v254 offset:33472\n"
"v_mfma_f32_16x16x32_bf16 v[160:163], v[200:203], v[112:115], v[160:163]\n"
"ds_read_b64_tr_b16 v[196:197], v254 offset:33600\n"
"v_cvt_pk_bf16_f32 v236, v152, v153\n"
"v_mfma_f32_16x16x32_bf16 v[160:163], v[204:207], v[120:123], v[160:163]\n"
"ds_read_b64_tr_b16 v[198:199], v254 offset:33728\n"
"s_waitcnt lgkmcnt(8)\n"
"v_mfma_f32_16x16x32_bf16 v[164:167], v[208:211], v[96:99], v[164:167]\n"
"ds_read_b64_tr_b16 v[200:201], v254 offset:33856\n"
"v_cvt_pk_bf16_f32 v237, v154, v155\n"
"v_mfma_f32_16x16x32_bf16 v[164:167], v[212:215], v[104:107], v[164:167]\n"
"ds_read_b64_tr_b16 v[202:203], v254 offset:33984\n"
"v_cvt_pk_bf16_f32 v238, v156, v157\n"
"v_mfma_f32_16x16x32_bf16 v[180:183], v[208:211], v[100:103], v[180:183]\n"
"ds_read_b64_tr_b16 v[204:205], v254 offset:34112\n"
"v_cvt_pk_bf16_f32 v239, v158, v159\n"
"v_mfma_f32_16x16x32_bf16 v[180:183], v[212:215], v[108:111], v[180:183]\n"
"ds_read_b64_tr_b16 v[206:207], v254 offset:34240\n"
"s_waitcnt lgkmcnt(8)\n"
"v_mfma_f32_16x16x32_bf16 v[180:183], v[216:219], v[116:119], v[180:183]\n"
"ds_read_b64_tr_b16 v[208:209], v254 offset:33376\n"
"s_mov_b32 s70, 123472\n"
"v_mfma_f32_16x16x32_bf16 v[180:183], v[220:223], v[124:127], v[180:183]\n"
"buffer_load_dwordx4 v255, s[60:63], s70 offen offset:3120 lds\n"
"ds_read_b64_tr_b16 v[210:211], v254 offset:33504\n"
"v_mfma_f32_16x16x32_bf16 v[164:167], v[216:219], v[112:115], v[164:167]\n"
"ds_read_b64_tr_b16 v[212:213], v254 offset:33632\n"
"v_accvgpr_write_b32 a12, v236\n"
"v_mfma_f32_16x16x32_bf16 v[164:167], v[220:223], v[120:123], v[164:167]\n"
"ds_read_b64_tr_b16 v[214:215], v254 offset:33760\n"
"s_waitcnt lgkmcnt(8)\n"
"v_mfma_f32_16x16x32_bf16 v[168:171], v[192:195], v[96:99], v[168:171]\n"
"ds_read_b64_tr_b16 v[216:217], v254 offset:33888\n"
"s_waitcnt vmcnt(8)\n"
"v_mfma_f32_16x16x32_bf16 v[168:171], v[196:199], v[104:107], v[168:171]\n"
"ds_read_b64_tr_b16 v[218:219], v254 offset:34016\n"
"v_accvgpr_write_b32 a13, v237\n"
"v_mfma_f32_16x16x32_bf16 v[184:187], v[192:195], v[100:103], v[184:187]\n"
"ds_read_b64_tr_b16 v[220:221], v254 offset:34144\n"
"s_barrier\n"
"v_mfma_f32_16x16x32_bf16 v[184:187], v[196:199], v[108:111], v[184:187]\n"
"ds_read_b64_tr_b16 v[222:223], v254 offset:34272\n"
"s_mov_b32 s69, 384\n"
"s_waitcnt lgkmcnt(8)\n"
"v_mfma_f32_16x16x32_bf16 v[184:187], v[200:203], v[116:119], v[184:187]\n"
"ds_read_b64_tr_b16 v[192:193], v241 offset:49920\n"
"s_add_u32 m0, s68, 0\n"
"v_mfma_f32_16x16x32_bf16 v[184:187], v[204:207], v[124:127], v[184:187]\n"
"buffer_load_dwordx4 v255, s[60:63], s69 offen offset:0 lds\n"
"ds_read_b64_tr_b16 v[194:195], v241 offset:50048\n"
"v_mfma_f32_16x16x32_bf16 v[168:171], v[200:203], v[112:115], v[168:171]\n"
"ds_read_b64_tr_b16 v[196:197], v241 offset:50176\n"
"v_accvgpr_write_b32 a14, v238\n"
"v_mfma_f32_16x16x32_bf16 v[168:171], v[204:207], v[120:123], v[168:171]\n"
"ds_read_b64_tr_b16 v[198:199], v241 offset:50304\n"
"s_waitcnt lgkmcnt(8)\n"
"v_mfma_f32_16x16x32_bf16 v[172:175], v[208:211], v[96:99], v[172:175]\n"
"ds_read_b64_tr_b16 v[200:201], v241 offset:50432\n"
"v_accvgpr_write_b32 a15, v239\n"
"v_mfma_f32_16x16x32_bf16 v[172:175], v[212:215], v[104:107], v[172:175]\n"
"ds_read_b64_tr_b16 v[202:203], v241 offset:50560\n"
"s_and_b32 s77, s77, 7\n"
"v_mfma_f32_16x16x32_bf16 v[188:191], v[208:211], v[100:103], v[188:191]\n"
"ds_read_b64_tr_b16 v[204:205], v241 offset:50688\n"
"s_mul_i32 s77, s77, 14336\n"
"v_mfma_f32_16x16x32_bf16 v[188:191], v[212:215], v[108:111], v[188:191]\n"
"ds_read_b64_tr_b16 v[206:207], v241 offset:50816\n"
"s_waitcnt lgkmcnt(8)\n"
"v_mfma_f32_16x16x32_bf16 v[188:191], v[216:219], v[116:119], v[188:191]\n"
"ds_read_b64_tr_b16 v[208:209], v241 offset:49952\n"
"s_mov_b32 s70, 6512\n"
"v_mfma_f32_16x16x32_bf16 v[188:191], v[220:223], v[124:127], v[188:191]\n"
"buffer_load_dwordx4 v255, s[60:63], s70 offen offset:1040 lds\n"
"ds_read_b64_tr_b16 v[210:211], v241 offset:50080\n"
"v_mfma_f32_16x16x32_bf16 v[172:175], v[216:219], v[112:115], v[172:175]\n"
"ds_read_b64_tr_b16 v[212:213], v241 offset:50208\n"
"s_add_u32 s49, s77, s91\n"
"v_mfma_f32_16x16x32_bf16 v[172:175], v[220:223], v[120:123], v[172:175]\n"
"ds_read_b64_tr_b16 v[214:215], v241 offset:50336\n"
"s_waitcnt lgkmcnt(8)\n"
"v_mfma_f32_16x16x32_bf16 v[128:131], v[192:195], v[0:3], 0\n"
"ds_read_b64_tr_b16 v[216:217], v241 offset:50464\n"
"s_add_u32 s77, s87, 6\n"
"v_mfma_f32_16x16x32_bf16 v[128:131], v[196:199], v[8:11], v[128:131]\n"
"ds_read_b64_tr_b16 v[218:219], v241 offset:50592\n"
"s_and_b32 s77, s77, 7\n"
"v_mfma_f32_16x16x32_bf16 v[144:147], v[192:195], v[4:7], 0\n"
"ds_read_b64_tr_b16 v[220:221], v241 offset:50720\n"
"s_mul_i32 s77, s77, 14336\n"
"v_mfma_f32_16x16x32_bf16 v[144:147], v[196:199], v[12:15], v[144:147]\n"
"ds_read_b64_tr_b16 v[222:223], v241 offset:50848\n"
"s_waitcnt lgkmcnt(8)\n"
"v_mfma_f32_16x16x32_bf16 v[144:147], v[200:203], v[20:23], v[144:147]\n"
"ds_read_b64_tr_b16 v[192:193], v241 offset:49984\n"
"s_mov_b32 s69, 1888\n"
"v_mfma_f32_16x16x32_bf16 v[144:147], v[204:207], v[28:31], v[144:147]\n"
"buffer_load_dwordx4 v255, s[60:63], s69 offen offset:2080 lds\n"
"ds_read_b64_tr_b16 v[194:195], v241 offset:50112\n"
"v_mfma_f32_16x16x32_bf16 v[128:131], v[200:203], v[16:19], v[128:131]\n"
"ds_read_b64_tr_b16 v[196:197], v241 offset:50240\n"
"v_cvt_pk_bf16_f32 v224, v160, v161\n"
"v_mfma_f32_16x16x32_bf16 v[128:131], v[204:207], v[24:27], v[128:131]\n"
"ds_read_b64_tr_b16 v[198:199], v241 offset:50368\n"
"s_waitcnt lgkmcnt(8)\n"
"v_mfma_f32_16x16x32_bf16 v[132:135], v[208:211], v[0:3], 0\n"
"ds_read_b64_tr_b16 v[200:201], v241 offset:50496\n"
"v_cvt_pk_bf16_f32 v225, v162, v163\n"
"v_mfma_f32_16x16x32_bf16 v[132:135], v[212:215], v[8:11], v[132:135]\n"
"ds_read_b64_tr_b16 v[202:203], v241 offset:50624\n"
"v_cvt_pk_bf16_f32 v226, v164, v165\n"
"v_mfma_f32_16x16x32_bf16 v[148:151], v[208:211], v[4:7], 0\n"
"ds_read_b64_tr_b16 v[204:205], v241 offset:50752\n"
"v_cvt_pk_bf16_f32 v227, v166, v167\n"
"v_mfma_f32_16x16x32_bf16 v[148:151], v[212:215], v[12:15], v[148:151]\n"
"ds_read_b64_tr_b16 v[206:207], v241 offset:50880\n"
"s_waitcnt lgkmcnt(8)\n"
"v_mfma_f32_16x16x32_bf16 v[148:151], v[216:219], v[20:23], v[148:151]\n"
"ds_read_b64_tr_b16 v[208:209], v241 offset:50016\n"
"s_mov_b32 s70, 8016\n"
"v_mfma_f32_16x16x32_bf16 v[148:151], v[220:223], v[28:31], v[148:151]\n"
"buffer_load_dwordx4 v255, s[60:63], s70 offen offset:3120 lds\n"
"ds_read_b64_tr_b16 v[210:211], v241 offset:50144\n"
"v_mfma_f32_16x16x32_bf16 v[132:135], v[216:219], v[16:19], v[132:135]\n"
"ds_read_b64_tr_b16 v[212:213], v241 offset:50272\n"
"v_accvgpr_write_b32 a16, v224\n"
"v_mfma_f32_16x16x32_bf16 v[132:135], v[220:223], v[24:27], v[132:135]\n"
"ds_read_b64_tr_b16 v[214:215], v241 offset:50400\n"
"s_waitcnt lgkmcnt(8)\n"
"v_mfma_f32_16x16x32_bf16 v[136:139], v[192:195], v[0:3], 0\n"
"ds_read_b64_tr_b16 v[216:217], v241 offset:50528\n"
"v_accvgpr_write_b32 a17, v225\n"
"v_mfma_f32_16x16x32_bf16 v[136:139], v[196:199], v[8:11], v[136:139]\n"
"ds_read_b64_tr_b16 v[218:219], v241 offset:50656\n"
"v_accvgpr_write_b32 a18, v226\n"
"v_mfma_f32_16x16x32_bf16 v[152:155], v[192:195], v[4:7], 0\n"
"ds_read_b64_tr_b16 v[220:221], v241 offset:50784\n"
"v_accvgpr_write_b32 a19, v227\n"
"v_mfma_f32_16x16x32_bf16 v[152:155], v[196:199], v[12:15], v[152:155]\n"
"ds_read_b64_tr_b16 v[222:223], v241 offset:50912\n"
"s_mov_b32 s69, 1280\n"
"s_waitcnt lgkmcnt(8)\n"
"v_mfma_f32_16x16x32_bf16 v[152:155], v[200:203], v[20:23], v[152:155]\n"
"ds_read_b64_tr_b16 v[192:193], v254 offset:49920\n"
"s_add_u32 m0, s68, 4160\n"
"v_mfma_f32_16x16x32_bf16 v[152:155], v[204:207], v[28:31], v[152:155]\n"
"buffer_load_dwordx4 v255, s[60:63], s69 offen offset:0 lds\n"
"ds_read_b64_tr_b16 v[194:195], v254 offset:50048\n"
"v_mfma_f32_16x16x32_bf16 v[136:139], v[200:203], v[16:19], v[136:139]\n"
"ds_read_b64_tr_b16 v[196:197], v254 offset:50176\n"
"s_add_u32 s50, s77, s91\n"
"v_mfma_f32_16x16x32_bf16 v[136:139], v[204:207], v[24:27], v[136:139]\n"
"ds_read_b64_tr_b16 v[198:199], v254 offset:50304\n"
"s_waitcnt lgkmcnt(8)\n"
"v_mfma_f32_16x16x32_bf16 v[140:143], v[208:211], v[0:3], 0\n"
"ds_read_b64_tr_b16 v[200:201], v254 offset:50432\n"
"s_add_u32 s77, s87, 7\n"
"v_mfma_f32_16x16x32_bf16 v[140:143], v[212:215], v[8:11], v[140:143]\n"
"ds_read_b64_tr_b16 v[202:203], v254 offset:50560\n"
"s_and_b32 s77, s77, 7\n"
"v_mfma_f32_16x16x32_bf16 v[156:159], v[208:211], v[4:7], 0\n"
"ds_read_b64_tr_b16 v[204:205], v254 offset:50688\n"
"s_mul_i32 s77, s77, 14336\n"
"v_mfma_f32_16x16x32_bf16 v[156:159], v[212:215], v[12:15], v[156:159]\n"
"ds_read_b64_tr_b16 v[206:207], v254 offset:50816\n"
"s_waitcnt lgkmcnt(8)\n"
"v_mfma_f32_16x16x32_bf16 v[156:159], v[216:219], v[20:23], v[156:159]\n"
"ds_read_b64_tr_b16 v[208:209], v254 offset:49952\n"
"s_mov_b32 s70, 7408\n"
"v_mfma_f32_16x16x32_bf16 v[156:159], v[220:223], v[28:31], v[156:159]\n"
"buffer_load_dwordx4 v255, s[60:63], s70 offen offset:1040 lds\n"
"ds_read_b64_tr_b16 v[210:211], v254 offset:50080\n"
"v_mfma_f32_16x16x32_bf16 v[140:143], v[216:219], v[16:19], v[140:143]\n"
"ds_read_b64_tr_b16 v[212:213], v254 offset:50208\n"
"s_add_u32 s51, s77, s91\n"
"v_mfma_f32_16x16x32_bf16 v[140:143], v[220:223], v[24:27], v[140:143]\n"
"ds_read_b64_tr_b16 v[214:215], v254 offset:50336\n"
"s_waitcnt lgkmcnt(8)\n"
"v_mfma_f32_16x16x32_bf16 v[128:131], v[192:195], v[64:67], v[128:131]\n"
"ds_read_b64_tr_b16 v[216:217], v254 offset:50464\n"
"s_add_u32 s44, s34, 12288\n"
"v_mfma_f32_16x16x32_bf16 v[128:131], v[196:199], v[72:75], v[128:131]\n"
"ds_read_b64_tr_b16 v[218:219], v254 offset:50592\n"
"s_add_u32 s77, %3, 133120\n"
"v_mfma_f32_16x16x32_bf16 v[144:147], v[192:195], v[68:71], v[144:147]\n"
"ds_read_b64_tr_b16 v[220:221], v254 offset:50720\n"
"s_add_u32 s80, s77, s91\n"
"v_mfma_f32_16x16x32_bf16 v[144:147], v[196:199], v[76:79], v[144:147]\n"
"ds_read_b64_tr_b16 v[222:223], v254 offset:50848\n"
"s_waitcnt lgkmcnt(8)\n"
"v_mfma_f32_16x16x32_bf16 v[144:147], v[200:203], v[84:87], v[144:147]\n"
"ds_read_b64_tr_b16 v[192:193], v254 offset:49984\n"
"s_mov_b32 s69, 2784\n"
"v_mfma_f32_16x16x32_bf16 v[144:147], v[204:207], v[92:95], v[144:147]\n"
"buffer_load_dwordx4 v255, s[60:63], s69 offen offset:2080 lds\n"
"ds_read_b64_tr_b16 v[194:195], v254 offset:50112\n"
"v_mfma_f32_16x16x32_bf16 v[128:131], v[200:203], v[80:83], v[128:131]\n"
"ds_read_b64_tr_b16 v[196:197], v254 offset:50240\n"
"v_cvt_pk_bf16_f32 v228, v168, v169\n"
"v_mfma_f32_16x16x32_bf16 v[128:131], v[204:207], v[88:91], v[128:131]\n"
"ds_read_b64_tr_b16 v[198:199], v254 offset:50368\n"
"s_waitcnt lgkmcnt(8)\n"
"v_mfma_f32_16x16x32_bf16 v[132:135], v[208:211], v[64:67], v[132:135]\n"
"ds_read_b64_tr_b16 v[200:201], v254 offset:50496\n"
"v_cvt_pk_bf16_f32 v229, v170, v171\n"
"v_mfma_f32_16x16x32_bf16 v[132:135], v[212:215], v[72:75], v[132:135]\n"
"ds_read_b64_tr_b16 v[202:203], v254 offset:50624\n"
"v_cvt_pk_bf16_f32 v230, v172, v173\n"
"v_mfma_f32_16x16x32_bf16 v[148:151], v[208:211], v[68:71], v[148:151]\n"
"ds_read_b64_tr_b16 v[204:205], v254 offset:50752\n"
"v_cvt_pk_bf16_f32 v231, v174, v175\n"
"v_mfma_f32_16x16x32_bf16 v[148:151], v[212:215], v[76:79], v[148:151]\n"
"ds_read_b64_tr_b16 v[206:207], v254 offset:50880\n"
"s_waitcnt lgkmcnt(8)\n"
"v_mfma_f32_16x16x32_bf16 v[148:151], v[216:219], v[84:87], v[148:151]\n"
"ds_read_b64_tr_b16 v[208:209], v254 offset:50016\n"
"s_mov_b32 s70, 8912\n"
"v_mfma_f32_16x16x32_bf16 v[148:151], v[220:223], v[92:95], v[148:151]\n"
"buffer_load_dwordx4 v255, s[60:63], s70 offen offset:3120 lds\n"
"ds_read_b64_tr_b16 v[210:211], v254 offset:50144\n"
"v_mfma_f32_16x16x32_bf16 v[132:135], v[216:219], v[80:83], v[132:135]\n"
"ds_read_b64_tr_b16 v[212:213], v254 offset:50272\n"
"v_accvgpr_write_b32 a20, v228\n"
"v_mfma_f32_16x16x32_bf16 v[132:135], v[220:223], v[88:91], v[132:135]\n"
"ds_read_b64_tr_b16 v[214:215], v254 offset:50400\n"
"s_waitcnt lgkmcnt(8)\n"
"v_mfma_f32_16x16x32_bf16 v[136:139], v[192:195], v[64:67], v[136:139]\n"
"ds_read_b64_tr_b16 v[216:217], v254 offset:50528\n"
"s_waitcnt vmcnt(8)\n"
"v_mfma_f32_16x16x32_bf16 v[136:139], v[196:199], v[72:75], v[136:139]\n"
"ds_read_b64_tr_b16 v[218:219], v254 offset:50656\n"
"v_accvgpr_write_b32 a21, v229\n"
"v_mfma_f32_16x16x32_bf16 v[152:155], v[192:195], v[68:71], v[152:155]\n"
"ds_read_b64_tr_b16 v[220:221], v254 offset:50784\n"
"s_barrier\n"
"v_mfma_f32_16x16x32_bf16 v[152:155], v[196:199], v[76:79], v[152:155]\n"
"ds_read_b64_tr_b16 v[222:223], v254 offset:50912\n"
"s_mov_b32 s69, 115072\n"
"s_waitcnt lgkmcnt(8)\n"
"v_mfma_f32_16x16x32_bf16 v[152:155], v[200:203], v[84:87], v[152:155]\n"
"ds_read_b64_tr_b16 v[192:193], v241 offset:16640\n"
"s_add_u32 m0, s68, 33280\n"
"v_mfma_f32_16x16x32_bf16 v[152:155], v[204:207], v[92:95], v[152:155]\n"
"buffer_load_dwordx4 v255, s[60:63], s69 offen offset:0 lds\n"
"ds_read_b64_tr_b16 v[194:195], v241 offset:16768\n"
"v_mfma_f32_16x16x32_bf16 v[136:139], v[200:203], v[80:83], v[136:139]\n"
"ds_read_b64_tr_b16 v[196:197], v241 offset:16896\n"
"v_accvgpr_write_b32 a22, v230\n"
"v_mfma_f32_16x16x32_bf16 v[136:139], v[204:207], v[88:91], v[136:139]\n"
"ds_read_b64_tr_b16 v[198:199], v241 offset:17024\n"
"s_waitcnt lgkmcnt(8)\n"
"v_mfma_f32_16x16x32_bf16 v[140:143], v[208:211], v[64:67], v[140:143]\n"
"ds_read_b64_tr_b16 v[200:201], v241 offset:17152\n"
"v_accvgpr_write_b32 a23, v231\n"
"v_mfma_f32_16x16x32_bf16 v[140:143], v[212:215], v[72:75], v[140:143]\n"
"ds_read_b64_tr_b16 v[202:203], v241 offset:17280\n"
"v_add_u32_e32 v253, s77, v242\n"
"v_mfma_f32_16x16x32_bf16 v[156:159], v[208:211], v[68:71], v[156:159]\n"
"ds_read_b64_tr_b16 v[204:205], v241 offset:17408\n"
"v_add_u32_e32 v252, s80, v242\n"
"v_mfma_f32_16x16x32_bf16 v[156:159], v[212:215], v[76:79], v[156:159]\n"
"ds_read_b64_tr_b16 v[206:207], v241 offset:17536\n"
"s_waitcnt lgkmcnt(8)\n"
"v_mfma_f32_16x16x32_bf16 v[156:159], v[216:219], v[84:87], v[156:159]\n"
"ds_read_b64_tr_b16 v[208:209], v241 offset:16672\n"
"s_mov_b32 s70, 121200\n"
"v_mfma_f32_16x16x32_bf16 v[156:159], v[220:223], v[92:95], v[156:159]\n"
"buffer_load_dwordx4 v255, s[60:63], s70 offen offset:1040 lds\n"
"ds_read_b64_tr_b16 v[210:211], v241 offset:16800\n"
"v_mfma_f32_16x16x32_bf16 v[140:143], v[216:219], v[80:83], v[140:143]\n"
"ds_read_b64_tr_b16 v[212:213], v241 offset:16928\n"
"v_mfma_f32_16x16x32_bf16 v[140:143], v[220:223], v[88:91], v[140:143]\n"
"ds_read_b64_tr_b16 v[214:215], v241 offset:17056\n"
"s_waitcnt lgkmcnt(8)\n"
"v_mfma_f32_16x16x32_bf16 v[128:131], v[192:195], v[32:35], v[128:131]\n"
"ds_read_b64_tr_b16 v[216:217], v241 offset:17184\n"
"v_mfma_f32_16x16x32_bf16 v[128:131], v[196:199], v[40:43], v[128:131]\n"
"ds_read_b64_tr_b16 v[218:219], v241 offset:17312\n"
"v_mfma_f32_16x16x32_bf16 v[144:147], v[192:195], v[36:39], v[144:147]\n"
"ds_read_b64_tr_b16 v[220:221], v241 offset:17440\n"
"v_mfma_f32_16x16x32_bf16 v[144:147], v[196:199], v[44:47], v[144:147]\n"
"ds_read_b64_tr_b16 v[222:223], v241 offset:17568\n"
"s_waitcnt lgkmcnt(8)\n"
"v_mfma_f32_16x16x32_bf16 v[144:147], v[200:203], v[52:55], v[144:147]\n"
"ds_read_b64_tr_b16 v[192:193], v241 offset:16704\n"
"s_mov_b32 s69, 116576\n"
"v_mfma_f32_16x16x32_bf16 v[144:147], v[204:207], v[60:63], v[144:147]\n"
"buffer_load_dwordx4 v255, s[60:63], s69 offen offset:2080 lds\n"
"ds_read_b64_tr_b16 v[194:195], v241 offset:16832\n"
"v_mfma_f32_16x16x32_bf16 v[128:131], v[200:203], v[48:51], v[128:131]\n"
"ds_read_b64_tr_b16 v[196:197], v241 offset:16960\n"
"v_cvt_pk_bf16_f32 v232, v176, v177\n"
"v_mfma_f32_16x16x32_bf16 v[128:131], v[204:207], v[56:59], v[128:131]\n"
"ds_read_b64_tr_b16 v[198:199], v241 offset:17088\n"
"s_waitcnt lgkmcnt(8)\n"
"v_mfma_f32_16x16x32_bf16 v[132:135], v[208:211], v[32:35], v[132:135]\n"
"ds_read_b64_tr_b16 v[200:201], v241 offset:17216\n"
"v_cvt_pk_bf16_f32 v233, v178, v179\n"
"v_mfma_f32_16x16x32_bf16 v[132:135], v[212:215], v[40:43], v[132:135]\n"
"ds_read_b64_tr_b16 v[202:203], v241 offset:17344\n"
"v_cvt_pk_bf16_f32 v234, v180, v181\n"
"v_mfma_f32_16x16x32_bf16 v[148:151], v[208:211], v[36:39], v[148:151]\n"
"ds_read_b64_tr_b16 v[204:205], v241 offset:17472\n"
"v_cvt_pk_bf16_f32 v235, v182, v183\n"
"v_mfma_f32_16x16x32_bf16 v[148:151], v[212:215], v[44:47], v[148:151]\n"
"ds_read_b64_tr_b16 v[206:207], v241 offset:17600\n"
"s_waitcnt lgkmcnt(8)\n"
"v_mfma_f32_16x16x32_bf16 v[148:151], v[216:219], v[52:55], v[148:151]\n"
"ds_read_b64_tr_b16 v[208:209], v241 offset:16736\n"
"s_mov_b32 s70, 122704\n"
"v_mfma_f32_16x16x32_bf16 v[148:151], v[220:223], v[60:63], v[148:151]\n"
"buffer_load_dwordx4 v255, s[60:63], s70 offen offset:3120 lds\n"
"ds_read_b64_tr_b16 v[210:211], v241 offset:16864\n"
"v_mfma_f32_16x16x32_bf16 v[132:135], v[216:219], v[48:51], v[132:135]\n"
"ds_read_b64_tr_b16 v[212:213], v241 offset:16992\n"
"v_accvgpr_write_b32 a24, v232\n"
"v_mfma_f32_16x16x32_bf16 v[132:135], v[220:223], v[56:59], v[132:135]\n"
"ds_read_b64_tr_b16 v[214:215], v241 offset:17120\n"
"s_waitcnt lgkmcnt(8)\n"
"v_mfma_f32_16x16x32_bf16 v[136:139], v[192:195], v[32:35], v[136:139]\n"
"ds_read_b64_tr_b16 v[216:217], v241 offset:17248\n"
"v_accvgpr_write_b32 a25, v233\n"
"v_mfma_f32_16x16x32_bf16 v[136:139], v[196:199], v[40:43], v[136:139]\n"
"ds_read_b64_tr_b16 v[218:219], v241 offset:17376\n"
"v_accvgpr_write_b32 a26, v234\n"
"v_mfma_f32_16x16x32_bf16 v[152:155], v[192:195], v[36:39], v[152:155]\n"
"ds_read_b64_tr_b16 v[220:221], v241 offset:17504\n"
"v_accvgpr_write_b32 a27, v235\n"
"v_mfma_f32_16x16x32_bf16 v[152:155], v[196:199], v[44:47], v[152:155]\n"
"ds_read_b64_tr_b16 v[222:223], v241 offset:17632\n"
"s_mov_b32 s69, 115968\n"
"s_waitcnt lgkmcnt(8)\n"
"v_mfma_f32_16x16x32_bf16 v[152:155], v[200:203], v[52:55], v[152:155]\n"
"ds_read_b64_tr_b16 v[192:193], v254 offset:16640\n"
"s_add_u32 m0, s68, 37440\n"
"v_mfma_f32_16x16x32_bf16 v[152:155], v[204:207], v[60:63], v[152:155]\n"
"buffer_load_dwordx4 v255, s[60:63], s69 offen offset:0 lds\n"
"ds_read_b64_tr_b16 v[194:195], v254 offset:16768\n"
"v_mfma_f32_16x16x32_bf16 v[136:139], v[200:203], v[48:51], v[136:139]\n"
"ds_read_b64_tr_b16 v[196:197], v254 offset:16896\n"
"v_mfma_f32_16x16x32_bf16 v[136:139], v[204:207], v[56:59], v[136:139]\n"
"ds_read_b64_tr_b16 v[198:199], v254 offset:17024\n"
"s_waitcnt lgkmcnt(8)\n"
"v_mfma_f32_16x16x32_bf16 v[140:143], v[208:211], v[32:35], v[140:143]\n"
"ds_read_b64_tr_b16 v[200:201], v254 offset:17152\n"
"v_mfma_f32_16x16x32_bf16 v[140:143], v[212:215], v[40:43], v[140:143]\n"
"ds_read_b64_tr_b16 v[202:203], v254 offset:17280\n"
"v_mfma_f32_16x16x32_bf16 v[156:159], v[208:211], v[36:39], v[156:159]\n"
"ds_read_b64_tr_b16 v[204:205], v254 offset:17408\n"
"v_mfma_f32_16x16x32_bf16 v[156:159], v[212:215], v[44:47], v[156:159]\n"
"ds_read_b64_tr_b16 v[206:207], v254 offset:17536\n"
"s_waitcnt lgkmcnt(8)\n"
"v_mfma_f32_16x16x32_bf16 v[156:159], v[216:219], v[52:55], v[156:159]\n"
"ds_read_b64_tr_b16 v[208:209], v254 offset:16672\n"
"s_mov_b32 s70, 122096\n"
"v_mfma_f32_16x16x32_bf16 v[156:159], v[220:223], v[60:63], v[156:159]\n"
"buffer_load_dwordx4 v255, s[60:63], s70 offen offset:1040 lds\n"
"ds_read_b64_tr_b16 v[210:211], v254 offset:16800\n"
"v_mfma_f32_16x16x32_bf16 v[140:143], v[216:219], v[48:51], v[140:143]\n"
"ds_read_b64_tr_b16 v[212:213], v254 offset:16928\n"
"v_mfma_f32_16x16x32_bf16 v[140:143], v[220:223], v[56:59], v[140:143]\n"
"ds_read_b64_tr_b16 v[214:215], v254 offset:17056\n"
"s_waitcnt lgkmcnt(8)\n"
"v_mfma_f32_16x16x32_bf16 v[128:131], v[192:195], v[96:99], v[128:131]\n"
"ds_read_b64_tr_b16 v[216:217], v254 offset:17184\n"
"v_mfma_f32_16x16x32_bf16 v[128:131], v[196:199], v[104:107], v[128:131]\n"
"ds_read_b64_tr_b16 v[218:219], v254 offset:17312\n"
"v_mfma_f32_16x16x32_bf16 v[144:147], v[192:195], v[100:103], v[144:147]\n"
"ds_read_b64_tr_b16 v[220:221], v254 offset:17440\n"
"v_mfma_f32_16x16x32_bf16 v[144:147], v[196:199], v[108:111], v[144:147]\n"
"ds_read_b64_tr_b16 v[222:223], v254 offset:17568\n"
"s_waitcnt lgkmcnt(8)\n"
"v_mfma_f32_16x16x32_bf16 v[144:147], v[200:203], v[116:119], v[144:147]\n"
"ds_read_b64_tr_b16 v[192:193], v254 offset:16704\n"
"s_mov_b32 s69, 117472\n"
"v_mfma_f32_16x16x32_bf16 v[144:147], v[204:207], v[124:127], v[144:147]\n"
"buffer_load_dwordx4 v255, s[60:63], s69 offen offset:2080 lds\n"
"ds_read_b64_tr_b16 v[194:195], v254 offset:16832\n"
"v_mfma_f32_16x16x32_bf16 v[128:131], v[200:203], v[112:115], v[128:131]\n"
"ds_read_b64_tr_b16 v[196:197], v254 offset:16960\n"
"v_cvt_pk_bf16_f32 v236, v184, v185\n"
"v_mfma_f32_16x16x32_bf16 v[128:131], v[204:207], v[120:123], v[128:131]\n"
"ds_read_b64_tr_b16 v[198:199], v254 offset:17088\n"
"s_waitcnt lgkmcnt(8)\n"
"v_mfma_f32_16x16x32_bf16 v[132:135], v[208:211], v[96:99], v[132:135]\n"
"ds_read_b64_tr_b16 v[200:201], v254 offset:17216\n"
"v_cvt_pk_bf16_f32 v237, v186, v187\n"
"v_mfma_f32_16x16x32_bf16 v[132:135], v[212:215], v[104:107], v[132:135]\n"
"ds_read_b64_tr_b16 v[202:203], v254 offset:17344\n"
"v_cvt_pk_bf16_f32 v238, v188, v189\n"
"v_mfma_f32_16x16x32_bf16 v[148:151], v[208:211], v[100:103], v[148:151]\n"
"ds_read_b64_tr_b16 v[204:205], v254 offset:17472\n"
"v_cvt_pk_bf16_f32 v239, v190, v191\n"
"v_mfma_f32_16x16x32_bf16 v[148:151], v[212:215], v[108:111], v[148:151]\n"
"ds_read_b64_tr_b16 v[206:207], v254 offset:17600\n"
"s_waitcnt lgkmcnt(8)\n"
"v_mfma_f32_16x16x32_bf16 v[148:151], v[216:219], v[116:119], v[148:151]\n"
"ds_read_b64_tr_b16 v[208:209], v254 offset:16736\n"
"s_mov_b32 s70, 123600\n"
"v_mfma_f32_16x16x32_bf16 v[148:151], v[220:223], v[124:127], v[148:151]\n"
"buffer_load_dwordx4 v255, s[60:63], s70 offen offset:3120 lds\n"
"ds_read_b64_tr_b16 v[210:211], v254 offset:16864\n"
"v_mfma_f32_16x16x32_bf16 v[132:135], v[216:219], v[112:115], v[132:135]\n"
"ds_read_b64_tr_b16 v[212:213], v254 offset:16992\n"
"v_accvgpr_write_b32 a28, v236\n"
"v_mfma_f32_16x16x32_bf16 v[132:135], v[220:223], v[120:123], v[132:135]\n"
"ds_read_b64_tr_b16 v[214:215], v254 offset:17120\n"
"s_waitcnt lgkmcnt(8)\n"
"v_mfma_f32_16x16x32_bf16 v[136:139], v[192:195], v[96:99], v[136:139]\n"
"ds_read_b64_tr_b16 v[216:217], v254 offset:17248\n"
"s_waitcnt vmcnt(8)\n"
"v_mfma_f32_16x16x32_bf16 v[136:139], v[196:199], v[104:107], v[136:139]\n"
"ds_read_b64_tr_b16 v[218:219], v254 offset:17376\n"
"v_accvgpr_write_b32 a29, v237\n"
"v_mfma_f32_16x16x32_bf16 v[152:155], v[192:195], v[100:103], v[152:155]\n"
"ds_read_b64_tr_b16 v[220:221], v254 offset:17504\n"
"s_barrier\n"
"v_mfma_f32_16x16x32_bf16 v[152:155], v[196:199], v[108:111], v[152:155]\n"
"ds_read_b64_tr_b16 v[222:223], v254 offset:17632\n"
"s_mov_b32 s69, 512\n"
"s_waitcnt lgkmcnt(8)\n"
"v_mfma_f32_16x16x32_bf16 v[152:155], v[200:203], v[116:119], v[152:155]\n"
"ds_read_b64_tr_b16 v[192:193], v241 offset:0\n"
"s_add_u32 m0, s68, 49920\n"
"v_mfma_f32_16x16x32_bf16 v[152:155], v[204:207], v[124:127], v[152:155]\n"
"buffer_load_dwordx4 v255, s[60:63], s69 offen offset:0 lds\n"
"ds_read_b64_tr_b16 v[194:195], v241 offset:128\n"
"v_mfma_f32_16x16x32_bf16 v[136:139], v[200:203], v[112:115], v[136:139]\n"
"ds_read_b64_tr_b16 v[196:197], v241 offset:256\n"
"v_accvgpr_write_b32 a30, v238\n"
"v_mfma_f32_16x16x32_bf16 v[136:139], v[204:207], v[120:123], v[136:139]\n"
"ds_read_b64_tr_b16 v[198:199], v241 offset:384\n"
"s_waitcnt lgkmcnt(8)\n"
"v_mfma_f32_16x16x32_bf16 v[140:143], v[208:211], v[96:99], v[140:143]\n"
"ds_read_b64_tr_b16 v[200:201], v241 offset:512\n"
"v_accvgpr_write_b32 a31, v239\n"
"v_mfma_f32_16x16x32_bf16 v[140:143], v[212:215], v[104:107], v[140:143]\n"
"ds_read_b64_tr_b16 v[202:203], v241 offset:640\n"
"v_mfma_f32_16x16x32_bf16 v[156:159], v[208:211], v[100:103], v[156:159]\n"
"ds_read_b64_tr_b16 v[204:205], v241 offset:768\n"
"v_mfma_f32_16x16x32_bf16 v[156:159], v[212:215], v[108:111], v[156:159]\n"
"ds_read_b64_tr_b16 v[206:207], v241 offset:896\n"
"s_waitcnt lgkmcnt(8)\n"
"v_mfma_f32_16x16x32_bf16 v[156:159], v[216:219], v[116:119], v[156:159]\n"
"ds_read_b64_tr_b16 v[208:209], v241 offset:32\n"
"s_mov_b32 s70, 6640\n"
"v_mfma_f32_16x16x32_bf16 v[156:159], v[220:223], v[124:127], v[156:159]\n"
"buffer_load_dwordx4 v255, s[60:63], s70 offen offset:1040 lds\n"
"s_branch .Lpo11\n"
".Lpg11:\n"
"s_setpc_b64 s[78:79]\n"
".Lpo11:\n"
"ds_read_b64_tr_b16 v[210:211], v241 offset:160\n"
"v_mfma_f32_16x16x32_bf16 v[140:143], v[216:219], v[112:115], v[140:143]\n"
"ds_read_b64_tr_b16 v[212:213], v241 offset:288\n"
"v_mfma_f32_16x16x32_bf16 v[140:143], v[220:223], v[120:123], v[140:143]\n"
"ds_read_b64_tr_b16 v[214:215], v241 offset:416\n"
"s_waitcnt lgkmcnt(8)\n"
"v_mfma_f32_16x16x32_bf16 v[160:163], v[192:195], v[0:3], 0\n"
"ds_read_b64_tr_b16 v[216:217], v241 offset:544\n"
"v_mfma_f32_16x16x32_bf16 v[160:163], v[196:199], v[8:11], v[160:163]\n"
"ds_read_b64_tr_b16 v[218:219], v241 offset:672\n"
"v_mfma_f32_16x16x32_bf16 v[176:179], v[192:195], v[4:7], 0\n"
"ds_read_b64_tr_b16 v[220:221], v241 offset:800\n"
"v_mfma_f32_16x16x32_bf16 v[176:179], v[196:199], v[12:15], v[176:179]\n"
"ds_read_b64_tr_b16 v[222:223], v241 offset:928\n"
"s_waitcnt lgkmcnt(8)\n"
"v_mfma_f32_16x16x32_bf16 v[176:179], v[200:203], v[20:23], v[176:179]\n"
"ds_read_b64_tr_b16 v[192:193], v241 offset:64\n"
"s_mov_b32 s69, 2016\n"
"v_mfma_f32_16x16x32_bf16 v[176:179], v[204:207], v[28:31], v[176:179]\n"
"buffer_load_dwordx4 v255, s[60:63], s69 offen offset:2080 lds\n"
"ds_read_b64_tr_b16 v[194:195], v241 offset:192\n"
"v_mfma_f32_16x16x32_bf16 v[160:163], v[200:203], v[16:19], v[160:163]\n"
"ds_read_b64_tr_b16 v[196:197], v241 offset:320\n"
"v_cvt_pk_bf16_f32 v224, v128, v129\n"
"v_mfma_f32_16x16x32_bf16 v[160:163], v[204:207], v[24:27], v[160:163]\n"
"ds_read_b64_tr_b16 v[198:199], v241 offset:448\n"
"s_waitcnt lgkmcnt(8)\n"
"v_mfma_f32_16x16x32_bf16 v[164:167], v[208:211], v[0:3], 0\n"
"ds_read_b64_tr_b16 v[200:201], v241 offset:576\n"
"v_cvt_pk_bf16_f32 v225, v130, v131\n"
"v_mfma_f32_16x16x32_bf16 v[164:167], v[212:215], v[8:11], v[164:167]\n"
"ds_read_b64_tr_b16 v[202:203], v241 offset:704\n"
"v_cvt_pk_bf16_f32 v226, v132, v133\n"
"v_mfma_f32_16x16x32_bf16 v[180:183], v[208:211], v[4:7], 0\n"
"ds_read_b64_tr_b16 v[204:205], v241 offset:832\n"
"v_cvt_pk_bf16_f32 v227, v134, v135\n"
"v_mfma_f32_16x16x32_bf16 v[180:183], v[212:215], v[12:15], v[180:183]\n"
"ds_read_b64_tr_b16 v[206:207], v241 offset:960\n"
"s_waitcnt lgkmcnt(8)\n"
"v_mfma_f32_16x16x32_bf16 v[180:183], v[216:219], v[20:23], v[180:183]\n"
"ds_read_b64_tr_b16 v[208:209], v241 offset:96\n"
"s_mov_b32 s70, 8144\n"
"v_mfma_f32_16x16x32_bf16 v[180:183], v[220:223], v[28:31], v[180:183]\n"
"buffer_load_dwordx4 v255, s[60:63], s70 offen offset:3120 lds\n"
"ds_read_b64_tr_b16 v[210:211], v241 offset:224\n"
"v_mfma_f32_16x16x32_bf16 v[164:167], v[216:219], v[16:19], v[164:167]\n"
"ds_read_b64_tr_b16 v[212:213], v241 offset:352\n"
"v_accvgpr_write_b32 a32, v224\n"
"v_mfma_f32_16x16x32_bf16 v[164:167], v[220:223], v[24:27], v[164:167]\n"
"ds_read_b64_tr_b16 v[214:215], v241 offset:480\n"
"s_waitcnt lgkmcnt(8)\n"
"v_mfma_f32_16x16x32_bf16 v[168:171], v[192:195], v[0:3], 0\n"
"ds_read_b64_tr_b16 v[216:217], v241 offset:608\n"
"v_accvgpr_write_b32 a33, v225\n"
"v_mfma_f32_16x16x32_bf16 v[168:171], v[196:199], v[8:11], v[168:171]\n"
"ds_read_b64_tr_b16 v[218:219], v241 offset:736\n"
"v_accvgpr_write_b32 a34, v226\n"
"v_mfma_f32_16x16x32_bf16 v[184:187], v[192:195], v[4:7], 0\n"
"ds_read_b64_tr_b16 v[220:221], v241 offset:864\n"
"v_accvgpr_write_b32 a35, v227\n"
"v_mfma_f32_16x16x32_bf16 v[184:187], v[196:199], v[12:15], v[184:187]\n"
"ds_read_b64_tr_b16 v[222:223], v241 offset:992\n"
"s_mov_b32 s69, 1408\n"
"s_waitcnt lgkmcnt(8)\n"
"v_mfma_f32_16x16x32_bf16 v[184:187], v[200:203], v[20:23], v[184:187]\n"
"ds_read_b64_tr_b16 v[192:193], v254 offset:0\n"
"s_add_u32 m0, s68, 54080\n"
"v_mfma_f32_16x16x32_bf16 v[184:187], v[204:207], v[28:31], v[184:187]\n"
"buffer_load_dwordx4 v255, s[60:63], s69 offen offset:0 lds\n"
"ds_read_b64_tr_b16 v[194:195], v254 offset:128\n"
"v_mfma_f32_16x16x32_bf16 v[168:171], v[200:203], v[16:19], v[168:171]\n"
"ds_read_b64_tr_b16 v[196:197], v254 offset:256\n"
"v_mfma_f32_16x16x32_bf16 v[168:171], v[204:207], v[24:27], v[168:171]\n"
"ds_read_b64_tr_b16 v[198:199], v254 offset:384\n"
"s_waitcnt lgkmcnt(8)\n"
"v_mfma_f32_16x16x32_bf16 v[172:175], v[208:211], v[0:3], 0\n"
"ds_read_b64_tr_b16 v[200:201], v254 offset:512\n"
"v_mfma_f32_16x16x32_bf16 v[172:175], v[212:215], v[8:11], v[172:175]\n"
"ds_read_b64_tr_b16 v[202:203], v254 offset:640\n"
"v_mfma_f32_16x16x32_bf16 v[188:191], v[208:211], v[4:7], 0\n"
"ds_read_b64_tr_b16 v[204:205], v254 offset:768\n"
"v_mfma_f32_16x16x32_bf16 v[188:191], v[212:215], v[12:15], v[188:191]\n"
"ds_read_b64_tr_b16 v[206:207], v254 offset:896\n"
"s_waitcnt lgkmcnt(8)\n"
"v_mfma_f32_16x16x32_bf16 v[188:191], v[216:219], v[20:23], v[188:191]\n"
"ds_read_b64_tr_b16 v[208:209], v254 offset:32\n"
"s_mov_b32 s70, 7536\n"
"v_mfma_f32_16x16x32_bf16 v[188:191], v[220:223], v[28:31], v[188:191]\n"
"buffer_load_dwordx4 v255, s[60:63], s70 offen offset:1040 lds\n"
"ds_read_b64_tr_b16 v[210:211], v254 offset:160\n"
"v_mfma_f32_16x16x32_bf16 v[172:175], v[216:219], v[16:19], v[172:175]\n"
"ds_read_b64_tr_b16 v[212:213], v254 offset:288\n"
"v_mfma_f32_16x16x32_bf16 v[172:175], v[220:223], v[24:27], v[172:175]\n"
"ds_read_b64_tr_b16 v[214:215], v254 offset:416\n"
"s_waitcnt lgkmcnt(8)\n"
"v_mfma_f32_16x16x32_bf16 v[160:163], v[192:195], v[64:67], v[160:163]\n"
"ds_read_b64_tr_b16 v[216:217], v254 offset:544\n"
"v_mfma_f32_16x16x32_bf16 v[160:163], v[196:199], v[72:75], v[160:163]\n"
"ds_read_b64_tr_b16 v[218:219], v254 offset:672\n"
"v_mfma_f32_16x16x32_bf16 v[176:179], v[192:195], v[68:71], v[176:179]\n"
"ds_read_b64_tr_b16 v[220:221], v254 offset:800\n"
"v_mfma_f32_16x16x32_bf16 v[176:179], v[196:199], v[76:79], v[176:179]\n"
"ds_read_b64_tr_b16 v[222:223], v254 offset:928\n"
"s_waitcnt lgkmcnt(8)\n"
"v_mfma_f32_16x16x32_bf16 v[176:179], v[200:203], v[84:87], v[176:179]\n"
"ds_read_b64_tr_b16 v[192:193], v254 offset:64\n"
"s_mov_b32 s69, 2912\n"
"v_mfma_f32_16x16x32_bf16 v[176:179], v[204:207], v[92:95], v[176:179]\n"
"buffer_load_dwordx4 v255, s[60:63], s69 offen offset:2080 lds\n"
"ds_read_b64_tr_b16 v[194:195], v254 offset:192\n"
"v_mfma_f32_16x16x32_bf16 v[160:163], v[200:203], v[80:83], v[160:163]\n"
"ds_read_b64_tr_b16 v[196:197], v254 offset:320\n"
"v_cvt_pk_bf16_f32 v228, v136, v137\n"
"v_mfma_f32_16x16x32_bf16 v[160:163], v[204:207], v[88:91], v[160:163]\n"
"ds_read_b64_tr_b16 v[198:199], v254 offset:448\n"
"s_waitcnt lgkmcnt(8)\n"
"v_mfma_f32_16x16x32_bf16 v[164:167], v[208:211], v[64:67], v[164:167]\n"
"ds_read_b64_tr_b16 v[200:201], v254 offset:576\n"
"v_cvt_pk_bf16_f32 v229, v138, v139\n"
"v_mfma_f32_16x16x32_bf16 v[164:167], v[212:215], v[72:75], v[164:167]\n"
"ds_read_b64_tr_b16 v[202:203], v254 offset:704\n"
"v_cvt_pk_bf16_f32 v230, v140, v141\n"
"v_mfma_f32_16x16x32_bf16 v[180:183], v[208:211], v[68:71], v[180:183]\n"
"ds_read_b64_tr_b16 v[204:205], v254 offset:832\n"
"v_cvt_pk_bf16_f32 v231, v142, v143\n"
"v_mfma_f32_16x16x32_bf16 v[180:183], v[212:215], v[76:79], v[180:183]\n"
"ds_read_b64_tr_b16 v[206:207], v254 offset:960\n"
"s_waitcnt lgkmcnt(8)\n"
"v_mfma_f32_16x16x32_bf16 v[180:183], v[216:219], v[84:87], v[180:183]\n"
"ds_read_b64_tr_b16 v[208:209], v254 offset:96\n"
"s_mov_b32 s70, 9040\n"
"v_mfma_f32_16x16x32_bf16 v[180:183], v[220:223], v[92:95], v[180:183]\n"
"buffer_load_dwordx4 v255, s[60:63], s70 offen offset:3120 lds\n"
"ds_read_b64_tr_b16 v[210:211], v254 offset:224\n"
"v_mfma_f32_16x16x32_bf16 v[164:167], v[216:219], v[80:83], v[164:167]\n"
"ds_read_b64_tr_b16 v[212:213], v254 offset:352\n"
"v_accvgpr_write_b32 a36, v228\n"
"v_mfma_f32_16x16x32_bf16 v[164:167], v[220:223], v[88:91], v[164:167]\n"
"ds_read_b64_tr_b16 v[214:215], v254 offset:480\n"
"s_waitcnt lgkmcnt(8)\n"
"v_mfma_f32_16x16x32_bf16 v[168:171], v[192:195], v[64:67], v[168:171]\n"
"ds_read_b64_tr_b16 v[216:217], v254 offset:608\n"
"s_waitcnt vmcnt(8)\n"
"v_mfma_f32_16x16x32_bf16 v[168:171], v[196:199], v[72:75], v[168:171]\n"
"ds_read_b64_tr_b16 v[218:219], v254 offset:736\n"
"v_accvgpr_write_b32 a37, v229\n"
"v_mfma_f32_16x16x32_bf16 v[184:187], v[192:195], v[68:71], v[184:187]\n"
"ds_read_b64_tr_b16 v[220:221], v254 offset:864\n"
"s_barrier\n"
"v_mfma_f32_16x16x32_bf16 v[184:187], v[196:199], v[76:79], v[184:187]\n"
"ds_read_b64_tr_b16 v[222:223], v254 offset:992\n"
"s_mov_b32 s69, 115200\n"
"s_waitcnt lgkmcnt(8)\n"
"v_mfma_f32_16x16x32_bf16 v[184:187], v[200:203], v[84:87], v[184:187]\n"
"ds_read_b64_tr_b16 v[192:193], v241 offset:33280\n"
"s_add_u32 m0, s68, 16640\n"
"v_mfma_f32_16x16x32_bf16 v[184:187], v[204:207], v[92:95], v[184:187]\n"
"buffer_load_dwordx4 v255, s[60:63], s69 offen offset:0 lds\n"
"ds_read_b64_tr_b16 v[194:195], v241 offset:33408\n"
"v_mfma_f32_16x16x32_bf16 v[168:171], v[200:203], v[80:83], v[168:171]\n"
"ds_read_b64_tr_b16 v[196:197], v241 offset:33536\n"
"v_accvgpr_write_b32 a38, v230\n"
"v_mfma_f32_16x16x32_bf16 v[168:171], v[204:207], v[88:91], v[168:171]\n"
"ds_read_b64_tr_b16 v[198:199], v241 offset:33664\n"
"s_waitcnt lgkmcnt(8)\n"
"v_mfma_f32_16x16x32_bf16 v[172:175], v[208:211], v[64:67], v[172:175]\n"
"ds_read_b64_tr_b16 v[200:201], v241 offset:33792\n"
"v_accvgpr_write_b32 a39, v231\n"
"v_mfma_f32_16x16x32_bf16 v[172:175], v[212:215], v[72:75], v[172:175]\n"
"ds_read_b64_tr_b16 v[202:203], v241 offset:33920\n"
"v_mfma_f32_16x16x32_bf16 v[188:191], v[208:211], v[68:71], v[188:191]\n"
"ds_read_b64_tr_b16 v[204:205], v241 offset:34048\n"
"v_mfma_f32_16x16x32_bf16 v[188:191], v[212:215], v[76:79], v[188:191]\n"
"ds_read_b64_tr_b16 v[206:207], v241 offset:34176\n"
"s_waitcnt lgkmcnt(8)\n"
"v_mfma_f32_16x16x32_bf16 v[188:191], v[216:219], v[84:87], v[188:191]\n"
"ds_read_b64_tr_b16 v[208:209], v241 offset:33312\n"
"s_mov_b32 s70, 121328\n"
"v_mfma_f32_16x16x32_bf16 v[188:191], v[220:223], v[92:95], v[188:191]\n"
"buffer_load_dwordx4 v255, s[60:63], s70 offen offset:1040 lds\n"
"ds_read_b64_tr_b16 v[210:211], v241 offset:33440\n"
"v_mfma_f32_16x16x32_bf16 v[172:175], v[216:219], v[80:83], v[172:175]\n"
"ds_read_b64_tr_b16 v[212:213], v241 offset:33568\n"
"v_mfma_f32_16x16x32_bf16 v[172:175], v[220:223], v[88:91], v[172:175]\n"
"ds_read_b64_tr_b16 v[214:215], v241 offset:33696\n"
"s_waitcnt lgkmcnt(8)\n"
"v_mfma_f32_16x16x32_bf16 v[160:163], v[192:195], v[32:35], v[160:163]\n"
"ds_read_b64_tr_b16 v[216:217], v241 offset:33824\n"
"v_mfma_f32_16x16x32_bf16 v[160:163], v[196:199], v[40:43], v[160:163]\n"
"ds_read_b64_tr_b16 v[218:219], v241 offset:33952\n"
"v_mfma_f32_16x16x32_bf16 v[176:179], v[192:195], v[36:39], v[176:179]\n"
"ds_read_b64_tr_b16 v[220:221], v241 offset:34080\n"
"v_mfma_f32_16x16x32_bf16 v[176:179], v[196:199], v[44:47], v[176:179]\n"
"ds_read_b64_tr_b16 v[222:223], v241 offset:34208\n"
"s_waitcnt lgkmcnt(8)\n"
"v_mfma_f32_16x16x32_bf16 v[176:179], v[200:203], v[52:55], v[176:179]\n"
"ds_read_b64_tr_b16 v[192:193], v241 offset:33344\n"
"s_mov_b32 s69, 116704\n"
"v_mfma_f32_16x16x32_bf16 v[176:179], v[204:207], v[60:63], v[176:179]\n"
"buffer_load_dwordx4 v255, s[60:63], s69 offen offset:2080 lds\n"
"ds_read_b64_tr_b16 v[194:195], v241 offset:33472\n"
"v_mfma_f32_16x16x32_bf16 v[160:163], v[200:203], v[48:51], v[160:163]\n"
"ds_read_b64_tr_b16 v[196:197], v241 offset:33600\n"
"v_cvt_pk_bf16_f32 v232, v144, v145\n"
"v_mfma_f32_16x16x32_bf16 v[160:163], v[204:207], v[56:59], v[160:163]\n"
"ds_read_b64_tr_b16 v[198:199], v241 offset:33728\n"
"s_waitcnt lgkmcnt(8)\n"
"v_mfma_f32_16x16x32_bf16 v[164:167], v[208:211], v[32:35], v[164:167]\n"
"ds_read_b64_tr_b16 v[200:201], v241 offset:33856\n"
"v_cvt_pk_bf16_f32 v233, v146, v147\n"
"v_mfma_f32_16x16x32_bf16 v[164:167], v[212:215], v[40:43], v[164:167]\n"
"ds_read_b64_tr_b16 v[202:203], v241 offset:33984\n"
"v_cvt_pk_bf16_f32 v234, v148, v149\n"
"v_mfma_f32_16x16x32_bf16 v[180:183], v[208:211], v[36:39], v[180:183]\n"
"ds_read_b64_tr_b16 v[204:205], v241 offset:34112\n"
"v_cvt_pk_bf16_f32 v235, v150, v151\n"
"v_mfma_f32_16x16x32_bf16 v[180:183], v[212:215], v[44:47], v[180:183]\n"
"ds_read_b64_tr_b16 v[206:207], v241 offset:34240\n"
"s_waitcnt lgkmcnt(8)\n"
"v_mfma_f32_16x16x32_bf16 v[180:183], v[216:219], v[52:55], v[180:183]\n"
"ds_read_b64_tr_b16 v[208:209], v241 offset:33376\n"
"s_mov_b32 s70, 122832\n"
"v_mfma_f32_16x16x32_bf16 v[180:183], v[220:223], v[60:63], v[180:183]\n"
"buffer_load_dwordx4 v255, s[60:63], s70 offen offset:3120 lds\n"
"ds_read_b64_tr_b16 v[210:211], v241 offset:33504\n"
"v_mfma_f32_16x16x32_bf16 v[164:167], v[216:219], v[48:51], v[164:167]\n"
"ds_read_b64_tr_b16 v[212:213], v241 offset:33632\n"
"v_accvgpr_write_b32 a40, v232\n"
"v_mfma_f32_16x16x32_bf16 v[164:167], v[220:223], v[56:59], v[164:167]\n"
"ds_read_b64_tr_b16 v[214:215], v241 offset:33760\n"
"s_waitcnt lgkmcnt(8)\n"
"v_mfma_f32_16x16x32_bf16 v[168:171], v[192:195], v[32:35], v[168:171]\n"
"ds_read_b64_tr_b16 v[216:217], v241 offset:33888\n"
"v_accvgpr_write_b32 a41, v233\n"
"v_mfma_f32_16x16x32_bf16 v[168:171], v[196:199], v[40:43], v[168:171]\n"
"ds_read_b64_tr_b16 v[218:219], v241 offset:34016\n"
"v_accvgpr_write_b32 a42, v234\n"
"v_mfma_f32_16x16x32_bf16 v[184:187], v[192:195], v[36:39], v[184:187]\n"
"ds_read_b64_tr_b16 v[220:221], v241 offset:34144\n"
"v_accvgpr_write_b32 a43, v235\n"
"v_mfma_f32_16x16x32_bf16 v[184:187], v[196:199], v[44:47], v[184:187]\n"
"ds_read_b64_tr_b16 v[222:223], v241 offset:34272\n"
"s_mov_b32 s69, 116096\n"
"s_waitcnt lgkmcnt(8)\n"
"v_mfma_f32_16x16x32_bf16 v[184:187], v[200:203], v[52:55], v[184:187]\n"
"ds_read_b64_tr_b16 v[192:193], v254 offset:33280\n"
"s_add_u32 m0, s68, 20800\n"
"v_mfma_f32_16x16x32_bf16 v[184:187], v[204:207], v[60:63], v[184:187]\n"
"buffer_load_dwordx4 v255, s[60:63], s69 offen offset:0 lds\n"
"ds_read_b64_tr_b16 v[194:195], v254 offset:33408\n"
"v_mfma_f32_16x16x32_bf16 v[168:171], v[200:203], v[48:51], v[168:171]\n"
"ds_read_b64_tr_b16 v[196:197], v254 offset:33536\n"
"v_mfma_f32_16x16x32_bf16 v[168:171], v[204:207], v[56:59], v[168:171]\n"
"ds_read_b64_tr_b16 v[198:199], v254 offset:33664\n"
"s_waitcnt lgkmcnt(8)\n"
"v_mfma_f32_16x16x32_bf16 v[172:175], v[208:211], v[32:35], v[172:175]\n"
"ds_read_b64_tr_b16 v[200:201], v254 offset:33792\n"
"v_mfma_f32_16x16x32_bf16 v[172:175], v[212:215], v[40:43], v[172:175]\n"
"ds_read_b64_tr_b16 v[202:203], v254 offset:33920\n"
"v_mfma_f32_16x16x32_bf16 v[188:191], v[208:211], v[36:39], v[188:191]\n"
"ds_read_b64_tr_b16 v[204:205], v254 offset:34048\n"
"v_mfma_f32_16x16x32_bf16 v[188:191], v[212:215], v[44:47], v[188:191]\n"
"ds_read_b64_tr_b16 v[206:207], v254 offset:34176\n"
"s_waitcnt lgkmcnt(8)\n"
"v_mfma_f32_16x16x32_bf16 v[188:191], v[216:219], v[52:55], v[188:191]\n"
"ds_read_b64_tr_b16 v[208:209], v254 offset:33312\n"
"s_mov_b32 s70, 122224\n"
"v_mfma_f32_16x16x32_bf16 v[188:191], v[220:223], v[60:63], v[188:191]\n"
"buffer_load_dwordx4 v255, s[60:63], s70 offen offset:1040 lds\n"
"ds_read_b64_tr_b16 v[210:211], v254 offset:33440\n"
"v_mfma_f32_16x16x32_bf16 v[172:175], v[216:219], v[48:51], v[172:175]\n"
"ds_read_b64_tr_b16 v[212:213], v254 offset:33568\n"
"v_mfma_f32_16x16x32_bf16 v[172:175], v[220:223], v[56:59], v[172:175]\n"
"ds_read_b64_tr_b16 v[214:215], v254 offset:33696\n"
"s_waitcnt lgkmcnt(8)\n"
"v_mfma_f32_16x16x32_bf16 v[160:163], v[192:195], v[96:99], v[160:163]\n"
"ds_read_b64_tr_b16 v[216:217], v254 offset:33824\n"
"v_mfma_f32_16x16x32_bf16 v[160:163], v[196:199], v[104:107], v[160:163]\n"
"ds_read_b64_tr_b16 v[218:219], v254 offset:33952\n"
"v_mfma_f32_16x16x32_bf16 v[176:179], v[192:195], v[100:103], v[176:179]\n"
"ds_read_b64_tr_b16 v[220:221], v254 offset:34080\n"
"v_mfma_f32_16x16x32_bf16 v[176:179], v[196:199], v[108:111], v[176:179]\n"
"ds_read_b64_tr_b16 v[222:223], v254 offset:34208\n"
"s_waitcnt lgkmcnt(8)\n"
"v_mfma_f32_16x16x32_bf16 v[176:179], v[200:203], v[116:119], v[176:179]\n"
"ds_read_b64_tr_b16 v[192:193], v254 offset:33344\n"
"s_mov_b32 s69, 117600\n"
"v_mfma_f32_16x16x32_bf16 v[176:179], v[204:207], v[124:127], v[176:179]\n"
"buffer_load_dwordx4 v255, s[60:63], s69 offen offset:2080 lds\n"
"ds_read_b64_tr_b16 v[194:195], v254 offset:33472\n"
"v_mfma_f32_16x16x32_bf16 v[160:163], v[200:203], v[112:115], v[160:163]\n"
"ds_read_b64_tr_b16 v[196:197], v254 offset:33600\n"
"v_cvt_pk_bf16_f32 v236, v152, v153\n"
"v_mfma_f32_16x16x32_bf16 v[160:163], v[204:207], v[120:123], v[160:163]\n"
"ds_read_b64_tr_b16 v[198:199], v254 offset:33728\n"
"s_waitcnt lgkmcnt(8)\n"
"v_mfma_f32_16x16x32_bf16 v[164:167], v[208:211], v[96:99], v[164:167]\n"
"ds_read_b64_tr_b16 v[200:201], v254 offset:33856\n"
"v_cvt_pk_bf16_f32 v237, v154, v155\n"
"v_mfma_f32_16x16x32_bf16 v[164:167], v[212:215], v[104:107], v[164:167]\n"
"ds_read_b64_tr_b16 v[202:203], v254 offset:33984\n"
"v_cvt_pk_bf16_f32 v238, v156, v157\n"
"v_mfma_f32_16x16x32_bf16 v[180:183], v[208:211], v[100:103], v[180:183]\n"
"ds_read_b64_tr_b16 v[204:205], v254 offset:34112\n"
"v_cvt_pk_bf16_f32 v239, v158, v159\n"
"v_mfma_f32_16x16x32_bf16 v[180:183], v[212:215], v[108:111], v[180:183]\n"
"ds_read_b64_tr_b16 v[206:207], v254 offset:34240\n"
"s_waitcnt lgkmcnt(8)\n"
"v_mfma_f32_16x16x32_bf16 v[180:183], v[216:219], v[116:119], v[180:183]\n"
"ds_read_b64_tr_b16 v[208:209], v254 offset:33376\n"
"s_mov_b32 s70, 123728\n"
"v_mfma_f32_16x16x32_bf16 v[180:183], v[220:223], v[124:127], v[180:183]\n"
"buffer_load_dwordx4 v255, s[60:63], s70 offen offset:3120 lds\n"
"ds_read_b64_tr_b16 v[210:211], v254 offset:33504\n"
"v_mfma_f32_16x16x32_bf16 v[164:167], v[216:219], v[112:115], v[164:167]\n"
"ds_read_b64_tr_b16 v[212:213], v254 offset:33632\n"
"v_accvgpr_write_b32 a44, v236\n"
"v_mfma_f32_16x16x32_bf16 v[164:167], v[220:223], v[120:123], v[164:167]\n"
"ds_read_b64_tr_b16 v[214:215], v254 offset:33760\n"
"s_waitcnt lgkmcnt(8)\n"
"v_mfma_f32_16x16x32_bf16 v[168:171], v[192:195], v[96:99], v[168:171]\n"
"ds_read_b64_tr_b16 v[216:217], v254 offset:33888\n"
"s_waitcnt vmcnt(8)\n"
"v_mfma_f32_16x16x32_bf16 v[168:171], v[196:199], v[104:107], v[168:171]\n"
"ds_read_b64_tr_b16 v[218:219], v254 offset:34016\n"
"v_accvgpr_write_b32 a45, v237\n"
"v_mfma_f32_16x16x32_bf16 v[184:187], v[192:195], v[100:103], v[184:187]\n"
"ds_read_b64_tr_b16 v[220:221], v254 offset:34144\n"
"s_barrier\n"
"v_mfma_f32_16x16x32_bf16 v[184:187], v[196:199], v[108:111], v[184:187]\n"
"ds_read_b64_tr_b16 v[222:223], v254 offset:34272\n"
"s_mov_b32 s69, 640\n"
"s_waitcnt lgkmcnt(8)\n"
"v_mfma_f32_16x16x32_bf16 v[184:187], v[200:203], v[116:119], v[184:187]\n"
"ds_read_b64_tr_b16 v[192:193], v241 offset:49920\n"
"s_add_u32 m0, s68, 0\n"
"v_mfma_f32_16x16x32_bf16 v[184:187], v[204:207], v[124:127], v[184:187]\n"
"buffer_load_dwordx4 v255, s[60:63], s69 offen offset:0 lds\n"
"ds_read_b64_tr_b16 v[194:195], v241 offset:50048\n"
"v_mfma_f32_16x16x32_bf16 v[168:171], v[200:203], v[112:115], v[168:171]\n"
"ds_read_b64_tr_b16 v[196:197], v241 offset:50176\n"
"v_accvgpr_write_b32 a46, v238\n"
"v_mfma_f32_16x16x32_bf16 v[168:171], v[204:207], v[120:123], v[168:171]\n"
"ds_read_b64_tr_b16 v[198:199], v241 offset:50304\n"
"s_waitcnt lgkmcnt(8)\n"
"v_mfma_f32_16x16x32_bf16 v[172:175], v[208:211], v[96:99], v[172:175]\n"
"ds_read_b64_tr_b16 v[200:201], v241 offset:50432\n"
"v_accvgpr_write_b32 a47, v239\n"
"v_mfma_f32_16x16x32_bf16 v[172:175], v[212:215], v[104:107], v[172:175]\n"
"ds_read_b64_tr_b16 v[202:203], v241 offset:50560\n"
"v_mfma_f32_16x16x32_bf16 v[188:191], v[208:211], v[100:103], v[188:191]\n"
"ds_read_b64_tr_b16 v[204:205], v241 offset:50688\n"
"v_mfma_f32_16x16x32_bf16 v[188:191], v[212:215], v[108:111], v[188:191]\n"
"ds_read_b64_tr_b16 v[206:207], v241 offset:50816\n"
"s_waitcnt lgkmcnt(8)\n"
"v_mfma_f32_16x16x32_bf16 v[188:191], v[216:219], v[116:119], v[188:191]\n"
"ds_read_b64_tr_b16 v[208:209], v241 offset:49952\n"
"s_mov_b32 s70, 6768\n"
"v_mfma_f32_16x16x32_bf16 v[188:191], v[220:223], v[124:127], v[188:191]\n"
"buffer_load_dwordx4 v255, s[60:63], s70 offen offset:1040 lds\n"
"ds_read_b64_tr_b16 v[210:211], v241 offset:50080\n"
"v_mfma_f32_16x16x32_bf16 v[172:175], v[216:219], v[112:115], v[172:175]\n"
"ds_read_b64_tr_b16 v[212:213], v241 offset:50208\n"
"v_mfma_f32_16x16x32_bf16 v[172:175], v[220:223], v[120:123], v[172:175]\n"
"ds_read_b64_tr_b16 v[214:215], v241 offset:50336\n"
"s_waitcnt lgkmcnt(8)\n"
"v_mfma_f32_16x16x32_bf16 v[128:131], v[192:195], v[0:3], 0\n"
"ds_read_b64_tr_b16 v[216:217], v241 offset:50464\n"
"v_mfma_f32_16x16x32_bf16 v[128:131], v[196:199], v[8:11], v[128:131]\n"
"ds_read_b64_tr_b16 v[218:219], v241 offset:50592\n"
"v_mfma_f32_16x16x32_bf16 v[144:147], v[192:195], v[4:7], 0\n"
"ds_read_b64_tr_b16 v[220:221], v241 offset:50720\n"
"v_mfma_f32_16x16x32_bf16 v[144:147], v[196:199], v[12:15], v[144:147]\n"
"ds_read_b64_tr_b16 v[222:223], v241 offset:50848\n"
"s_waitcnt lgkmcnt(8)\n"
"v_mfma_f32_16x16x32_bf16 v[144:147], v[200:203], v[20:23], v[144:147]\n"
"ds_read_b64_tr_b16 v[192:193], v241 offset:49984\n"
"s_mov_b32 s69, 2144\n"
"v_mfma_f32_16x16x32_bf16 v[144:147], v[204:207], v[28:31], v[144:147]\n"
"buffer_load_dwordx4 v255, s[60:63], s69 offen offset:2080 lds\n"
"ds_read_b64_tr_b16 v[194:195], v241 offset:50112\n"
"v_mfma_f32_16x16x32_bf16 v[128:131], v[200:203], v[16:19], v[128:131]\n"
"ds_read_b64_tr_b16 v[196:197], v241 offset:50240\n"
"v_cvt_pk_bf16_f32 v224, v160, v161\n"
"v_mfma_f32_16x16x32_bf16 v[128:131], v[204:207], v[24:27], v[128:131]\n"
"ds_read_b64_tr_b16 v[198:199], v241 offset:50368\n"
"s_waitcnt lgkmcnt(8)\n"
"v_mfma_f32_16x16x32_bf16 v[132:135], v[208:211], v[0:3], 0\n"
"ds_read_b64_tr_b16 v[200:201], v241 offset:50496\n"
"v_cvt_pk_bf16_f32 v225, v162, v163\n"
"v_mfma_f32_16x16x32_bf16 v[132:135], v[212:215], v[8:11], v[132:135]\n"
"ds_read_b64_tr_b16 v[202:203], v241 offset:50624\n"
"v_cvt_pk_bf16_f32 v226, v164, v165\n"
"v_mfma_f32_16x16x32_bf16 v[148:151], v[208:211], v[4:7], 0\n"
"ds_read_b64_tr_b16 v[204:205], v241 offset:50752\n"
"v_cvt_pk_bf16_f32 v227, v166, v167\n"
"v_mfma_f32_16x16x32_bf16 v[148:151], v[212:215], v[12:15], v[148:151]\n"
"ds_read_b64_tr_b16 v[206:207], v241 offset:50880\n"
"s_waitcnt lgkmcnt(8)\n"
"v_mfma_f32_16x16x32_bf16 v[148:151], v[216:219], v[20:23], v[148:151]\n"
"ds_read_b64_tr_b16 v[208:209], v241 offset:50016\n"
"s_mov_b32 s70, 8272\n"
"v_mfma_f32_16x16x32_bf16 v[148:151], v[220:223], v[28:31], v[148:151]\n"
"buffer_load_dwordx4 v255, s[60:63], s70 offen offset:3120 lds\n"
"ds_read_b64_tr_b16 v[210:211], v241 offset:50144\n"
"v_mfma_f32_16x16x32_bf16 v[132:135], v[216:219], v[16:19], v[132:135]\n"
"ds_read_b64_tr_b16 v[212:213], v241 offset:50272\n"
"v_accvgpr_write_b32 a48, v224\n"
"v_mfma_f32_16x16x32_bf16 v[132:135], v[220:223], v[24:27], v[132:135]\n"
"ds_read_b64_tr_b16 v[214:215], v241 offset:50400\n"
"s_waitcnt lgkmcnt(8)\n"
"v_mfma_f32_16x16x32_bf16 v[136:139], v[192:195], v[0:3], 0\n"
"ds_read_b64_tr_b16 v[216:217], v241 offset:50528\n"
"v_accvgpr_write_b32 a49, v225\n"
"v_mfma_f32_16x16x32_bf16 v[136:139], v[196:199], v[8:11], v[136:139]\n"
"ds_read_b64_tr_b16 v[218:219], v241 offset:50656\n"
"v_accvgpr_write_b32 a50, v226\n"
"v_mfma_f32_16x16x32_bf16 v[152:155], v[192:195], v[4:7], 0\n"
"ds_read_b64_tr_b16 v[220:221], v241 offset:50784\n"
"v_accvgpr_write_b32 a51, v227\n"
"v_mfma_f32_16x16x32_bf16 v[152:155], v[196:199], v[12:15], v[152:155]\n"
"ds_read_b64_tr_b16 v[222:223], v241 offset:50912\n"
"s_mov_b32 s69, 1536\n"
"s_waitcnt lgkmcnt(8)\n"
"v_mfma_f32_16x16x32_bf16 v[152:155], v[200:203], v[20:23], v[152:155]\n"
"ds_read_b64_tr_b16 v[192:193], v254 offset:49920\n"
"s_add_u32 m0, s68, 4160\n"
"v_mfma_f32_16x16x32_bf16 v[152:155], v[204:207], v[28:31], v[152:155]\n"
"buffer_load_dwordx4 v255, s[60:63], s69 offen offset:0 lds\n"
"ds_read_b64_tr_b16 v[194:195], v254 offset:50048\n"
"v_mfma_f32_16x16x32_bf16 v[136:139], v[200:203], v[16:19], v[136:139]\n"
"ds_read_b64_tr_b16 v[196:197], v254 offset:50176\n"
"v_mfma_f32_16x16x32_bf16 v[136:139], v[204:207], v[24:27], v[136:139]\n"
"ds_read_b64_tr_b16 v[198:199], v254 offset:50304\n"
"s_waitcnt lgkmcnt(8)\n"
"v_mfma_f32_16x16x32_bf16 v[140:143], v[208:211], v[0:3], 0\n"
"ds_read_b64_tr_b16 v[200:201], v254 offset:50432\n"
"v_mfma_f32_16x16x32_bf16 v[140:143], v[212:215], v[8:11], v[140:143]\n"
"ds_read_b64_tr_b16 v[202:203], v254 offset:50560\n"
"v_mfma_f32_16x16x32_bf16 v[156:159], v[208:211], v[4:7], 0\n"
"ds_read_b64_tr_b16 v[204:205], v254 offset:50688\n"
"v_mfma_f32_16x16x32_bf16 v[156:159], v[212:215], v[12:15], v[156:159]\n"
"ds_read_b64_tr_b16 v[206:207], v254 offset:50816\n"
"s_waitcnt lgkmcnt(8)\n"
"v_mfma_f32_16x16x32_bf16 v[156:159], v[216:219], v[20:23], v[156:159]\n"
"ds_read_b64_tr_b16 v[208:209], v254 offset:49952\n"
"s_mov_b32 s70, 7664\n"
"v_mfma_f32_16x16x32_bf16 v[156:159], v[220:223], v[28:31], v[156:159]\n"
"buffer_load_dwordx4 v255, s[60:63], s70 offen offset:1040 lds\n"
"ds_read_b64_tr_b16 v[210:211], v254 offset:50080\n"
"v_mfma_f32_16x16x32_bf16 v[140:143], v[216:219], v[16:19], v[140:143]\n"
"ds_read_b64_tr_b16 v[212:213], v254 offset:50208\n"
"v_mfma_f32_16x16x32_bf16 v[140:143], v[220:223], v[24:27], v[140:143]\n"
"ds_read_b64_tr_b16 v[214:215], v254 offset:50336\n"
"s_waitcnt lgkmcnt(8)\n"
"v_mfma_f32_16x16x32_bf16 v[128:131], v[192:195], v[64:67], v[128:131]\n"
"ds_read_b64_tr_b16 v[216:217], v254 offset:50464\n"
"v_mfma_f32_16x16x32_bf16 v[128:131], v[196:199], v[72:75], v[128:131]\n"
"ds_read_b64_tr_b16 v[218:219], v254 offset:50592\n"
"v_mfma_f32_16x16x32_bf16 v[144:147], v[192:195], v[68:71], v[144:147]\n"
"ds_read_b64_tr_b16 v[220:221], v254 offset:50720\n"
"v_mfma_f32_16x16x32_bf16 v[144:147], v[196:199], v[76:79], v[144:147]\n"
"ds_read_b64_tr_b16 v[222:223], v254 offset:50848\n"
"s_waitcnt lgkmcnt(8)\n"
"v_mfma_f32_16x16x32_bf16 v[144:147], v[200:203], v[84:87], v[144:147]\n"
"ds_read_b64_tr_b16 v[192:193], v254 offset:49984\n"
"s_mov_b32 s69, 3040\n"
"v_mfma_f32_16x16x32_bf16 v[144:147], v[204:207], v[92:95], v[144:147]\n"
"buffer_load_dwordx4 v255, s[60:63], s69 offen offset:2080 lds\n"
"ds_read_b64_tr_b16 v[194:195], v254 offset:50112\n"
"v_mfma_f32_16x16x32_bf16 v[128:131], v[200:203], v[80:83], v[128:131]\n"
"ds_read_b64_tr_b16 v[196:197], v254 offset:50240\n"
"v_cvt_pk_bf16_f32 v228, v168, v169\n"
"v_mfma_f32_16x16x32_bf16 v[128:131], v[204:207], v[88:91], v[128:131]\n"
"ds_read_b64_tr_b16 v[198:199], v254 offset:50368\n"
"s_waitcnt lgkmcnt(8)\n"
"v_mfma_f32_16x16x32_bf16 v[132:135], v[208:211], v[64:67], v[132:135]\n"
"ds_read_b64_tr_b16 v[200:201], v254 offset:50496\n"
"v_cvt_pk_bf16_f32 v229, v170, v171\n"
"v_mfma_f32_16x16x32_bf16 v[132:135], v[212:215], v[72:75], v[132:135]\n"
"ds_read_b64_tr_b16 v[202:203], v254 offset:50624\n"
"v_cvt_pk_bf16_f32 v230, v172, v173\n"
"v_mfma_f32_16x16x32_bf16 v[148:151], v[208:211], v[68:71], v[148:151]\n"
"ds_read_b64_tr_b16 v[204:205], v254 offset:50752\n"
"v_cvt_pk_bf16_f32 v231, v174, v175\n"
"v_mfma_f32_16x16x32_bf16 v[148:151], v[212:215], v[76:79], v[148:151]\n"
"ds_read_b64_tr_b16 v[206:207], v254 offset:50880\n"
"s_waitcnt lgkmcnt(8)\n"
"v_mfma_f32_16x16x32_bf16 v[148:151], v[216:219], v[84:87], v[148:151]\n"
"ds_read_b64_tr_b16 v[208:209], v254 offset:50016\n"
"s_mov_b32 s70, 9168\n"
"v_mfma_f32_16x16x32_bf16 v[148:151], v[220:223], v[92:95], v[148:151]\n"
"buffer_load_dwordx4 v255, s[60:63], s70 offen offset:3120 lds\n"
"ds_read_b64_tr_b16 v[210:211], v254 offset:50144\n"
"v_mfma_f32_16x16x32_bf16 v[132:135], v[216:219], v[80:83], v[132:135]\n"
"ds_read_b64_tr_b16 v[212:213], v254 offset:50272\n"
"v_accvgpr_write_b32 a52, v228\n"
"v_mfma_f32_16x16x32_bf16 v[132:135], v[220:223], v[88:91], v[132:135]\n"
"ds_read_b64_tr_b16 v[214:215], v254 offset:50400\n"
"s_waitcnt lgkmcnt(8)\n"
"v_mfma_f32_16x16x32_bf16 v[136:139], v[192:195], v[64:67], v[136:139]\n"
"ds_read_b64_tr_b16 v[216:217], v254 offset:50528\n"
"s_waitcnt vmcnt(8)\n"
"v_mfma_f32_16x16x32_bf16 v[136:139], v[196:199], v[72:75], v[136:139]\n"
"ds_read_b64_tr_b16 v[218:219], v254 offset:50656\n"
"v_accvgpr_write_b32 a53, v229\n"
"v_mfma_f32_16x16x32_bf16 v[152:155], v[192:195], v[68:71], v[152:155]\n"
"ds_read_b64_tr_b16 v[220:221], v254 offset:50784\n"
"s_barrier\n"
"v_mfma_f32_16x16x32_bf16 v[152:155], v[196:199], v[76:79], v[152:155]\n"
"ds_read_b64_tr_b16 v[222:223], v254 offset:50912\n"
"s_mov_b32 s69, 115328\n"
"s_waitcnt lgkmcnt(8)\n"
"v_mfma_f32_16x16x32_bf16 v[152:155], v[200:203], v[84:87], v[152:155]\n"
"ds_read_b64_tr_b16 v[192:193], v241 offset:16640\n"
"s_add_u32 m0, s68, 33280\n"
"v_mfma_f32_16x16x32_bf16 v[152:155], v[204:207], v[92:95], v[152:155]\n"
"buffer_load_dwordx4 v255, s[60:63], s69 offen offset:0 lds\n"
"ds_read_b64_tr_b16 v[194:195], v241 offset:16768\n"
"v_mfma_f32_16x16x32_bf16 v[136:139], v[200:203], v[80:83], v[136:139]\n"
"ds_read_b64_tr_b16 v[196:197], v241 offset:16896\n"
"v_accvgpr_write_b32 a54, v230\n"
"v_mfma_f32_16x16x32_bf16 v[136:139], v[204:207], v[88:91], v[136:139]\n"
"ds_read_b64_tr_b16 v[198:199], v241 offset:17024\n"
"s_waitcnt lgkmcnt(8)\n"
"v_mfma_f32_16x16x32_bf16 v[140:143], v[208:211], v[64:67], v[140:143]\n"
"ds_read_b64_tr_b16 v[200:201], v241 offset:17152\n"
"v_accvgpr_write_b32 a55, v231\n"
"v_mfma_f32_16x16x32_bf16 v[140:143], v[212:215], v[72:75], v[140:143]\n"
"ds_read_b64_tr_b16 v[202:203], v241 offset:17280\n"
"v_mfma_f32_16x16x32_bf16 v[156:159], v[208:211], v[68:71], v[156:159]\n"
"ds_read_b64_tr_b16 v[204:205], v241 offset:17408\n"
"v_mfma_f32_16x16x32_bf16 v[156:159], v[212:215], v[76:79], v[156:159]\n"
"ds_read_b64_tr_b16 v[206:207], v241 offset:17536\n"
"s_waitcnt lgkmcnt(8)\n"
"v_mfma_f32_16x16x32_bf16 v[156:159], v[216:219], v[84:87], v[156:159]\n"
"ds_read_b64_tr_b16 v[208:209], v241 offset:16672\n"
"s_mov_b32 s70, 121456\n"
"s_branch .Lpo12\n"
".Lpg12:\n"
"s_setpc_b64 s[78:79]\n"
".Lpo12:\n"
"v_mfma_f32_16x16x32_bf16 v[156:159], v[220:223], v[92:95], v[156:159]\n"
"buffer_load_dwordx4 v255, s[60:63], s70 offen offset:1040 lds\n"
"ds_read_b64_tr_b16 v[210:211], v241 offset:16800\n"
"v_mfma_f32_16x16x32_bf16 v[140:143], v[216:219], v[80:83], v[140:143]\n"
"ds_read_b64_tr_b16 v[212:213], v241 offset:16928\n"
"v_mfma_f32_16x16x32_bf16 v[140:143], v[220:223], v[88:91], v[140:143]\n"
"ds_read_b64_tr_b16 v[214:215], v241 offset:17056\n"
"s_waitcnt lgkmcnt(8)\n"
"v_mfma_f32_16x16x32_bf16 v[128:131], v[192:195], v[32:35], v[128:131]\n"
"ds_read_b64_tr_b16 v[216:217], v241 offset:17184\n"
"s_mov_b32 s71, 0x3f80\n"
"v_mfma_f32_16x16x32_bf16 v[128:131], v[196:199], v[40:43], v[128:131]\n"
"ds_read_b64_tr_b16 v[218:219], v241 offset:17312\n"
"s_mov_b32 s76, 0x3f800000\n"
"v_mfma_f32_16x16x32_bf16 v[144:147], v[192:195], v[36:39], v[144:147]\n"
"ds_read_b64_tr_b16 v[220:221], v241 offset:17440\n"
"s_lshl_b32 s77, s87, 7\n"
"v_mfma_f32_16x16x32_bf16 v[144:147], v[196:199], v[44:47], v[144:147]\n"
"ds_read_b64_tr_b16 v[222:223], v241 offset:17568\n"
"s_waitcnt lgkmcnt(8)\n"
"v_mfma_f32_16x16x32_bf16 v[144:147], v[200:203], v[52:55], v[144:147]\n"
"ds_read_b64_tr_b16 v[192:193], v241 offset:16704\n"
"s_mov_b32 s69, 116832\n"
"v_mfma_f32_16x16x32_bf16 v[144:147], v[204:207], v[60:63], v[144:147]\n"
"buffer_load_dwordx4 v255, s[60:63], s69 offen offset:2080 lds\n"
"ds_read_b64_tr_b16 v[194:195], v241 offset:16832\n"
"v_mfma_f32_16x16x32_bf16 v[128:131], v[200:203], v[48:51], v[128:131]\n"
"ds_read_b64_tr_b16 v[196:197], v241 offset:16960\n"
"v_cvt_pk_bf16_f32 v232, v176, v177\n"
"v_mfma_f32_16x16x32_bf16 v[128:131], v[204:207], v[56:59], v[128:131]\n"
"ds_read_b64_tr_b16 v[198:199], v241 offset:17088\n"
"s_waitcnt lgkmcnt(8)\n"
"v_mfma_f32_16x16x32_bf16 v[132:135], v[208:211], v[32:35], v[132:135]\n"
"ds_read_b64_tr_b16 v[200:201], v241 offset:17216\n"
"v_cvt_pk_bf16_f32 v233, v178, v179\n"
"v_mfma_f32_16x16x32_bf16 v[132:135], v[212:215], v[40:43], v[132:135]\n"
"ds_read_b64_tr_b16 v[202:203], v241 offset:17344\n"
"v_cvt_pk_bf16_f32 v234, v180, v181\n"
"v_mfma_f32_16x16x32_bf16 v[148:151], v[208:211], v[36:39], v[148:151]\n"
"ds_read_b64_tr_b16 v[204:205], v241 offset:17472\n"
"v_cvt_pk_bf16_f32 v235, v182, v183\n"
"v_mfma_f32_16x16x32_bf16 v[148:151], v[212:215], v[44:47], v[148:151]\n"
"ds_read_b64_tr_b16 v[206:207], v241 offset:17600\n"
"s_waitcnt lgkmcnt(8)\n"
"v_mfma_f32_16x16x32_bf16 v[148:151], v[216:219], v[52:55], v[148:151]\n"
"ds_read_b64_tr_b16 v[208:209], v241 offset:16736\n"
"s_mov_b32 s70, 122960\n"
"v_mfma_f32_16x16x32_bf16 v[148:151], v[220:223], v[60:63], v[148:151]\n"
"buffer_load_dwordx4 v255, s[60:63], s70 offen offset:3120 lds\n"
"ds_read_b64_tr_b16 v[210:211], v241 offset:16864\n"
"v_mfma_f32_16x16x32_bf16 v[132:135], v[216:219], v[48:51], v[132:135]\n"
"ds_read_b64_tr_b16 v[212:213], v241 offset:16992\n"
"v_accvgpr_write_b32 a56, v232\n"
"v_mfma_f32_16x16x32_bf16 v[132:135], v[220:223], v[56:59], v[132:135]\n"
"ds_read_b64_tr_b16 v[214:215], v241 offset:17120\n"
"s_waitcnt lgkmcnt(8)\n"
"v_mfma_f32_16x16x32_bf16 v[136:139], v[192:195], v[32:35], v[136:139]\n"
"ds_read_b64_tr_b16 v[216:217], v241 offset:17248\n"
"v_accvgpr_write_b32 a57, v233\n"
"v_mfma_f32_16x16x32_bf16 v[136:139], v[196:199], v[40:43], v[136:139]\n"
"ds_read_b64_tr_b16 v[218:219], v241 offset:17376\n"
"v_accvgpr_write_b32 a58, v234\n"
"v_mfma_f32_16x16x32_bf16 v[152:155], v[192:195], v[36:39], v[152:155]\n"
"ds_read_b64_tr_b16 v[220:221], v241 offset:17504\n"
"v_accvgpr_write_b32 a59, v235\n"
"v_mfma_f32_16x16x32_bf16 v[152:155], v[196:199], v[44:47], v[152:155]\n"
"ds_read_b64_tr_b16 v[222:223], v241 offset:17632\n"
"s_mov_b32 s69, 116224\n"
"s_waitcnt lgkmcnt(8)\n"
"v_mfma_f32_16x16x32_bf16 v[152:155], v[200:203], v[52:55], v[152:155]\n"
"ds_read_b64_tr_b16 v[192:193], v254 offset:16640\n"
"s_add_u32 m0, s68, 37440\n"
"v_mfma_f32_16x16x32_bf16 v[152:155], v[204:207], v[60:63], v[152:155]\n"
"buffer_load_dwordx4 v255, s[60:63], s69 offen offset:0 lds\n"
"ds_read_b64_tr_b16 v[194:195], v254 offset:16768\n"
"v_mfma_f32_16x16x32_bf16 v[136:139], v[200:203], v[48:51], v[136:139]\n"
"ds_read_b64_tr_b16 v[196:197], v254 offset:16896\n"
"s_lshl_b32 s80, s87, 2\n"
"v_mfma_f32_16x16x32_bf16 v[136:139], v[204:207], v[56:59], v[136:139]\n"
"ds_read_b64_tr_b16 v[198:199], v254 offset:17024\n"
"s_waitcnt lgkmcnt(8)\n"
"v_mfma_f32_16x16x32_bf16 v[140:143], v[208:211], v[32:35], v[140:143]\n"
"ds_read_b64_tr_b16 v[200:201], v254 offset:17152\n"
"v_lshrrev_b32_e32 v245, 2, v242\n"
"v_mfma_f32_16x16x32_bf16 v[140:143], v[212:215], v[40:43], v[140:143]\n"
"ds_read_b64_tr_b16 v[202:203], v254 offset:17280\n"
"v_add_u32_e32 v243, s77, v245\n"
"v_mfma_f32_16x16x32_bf16 v[156:159], v[208:211], v[36:39], v[156:159]\n"
"ds_read_b64_tr_b16 v[204:205], v254 offset:17408\n"
"v_and_b32_e32 v244, 32, v245\n"
"v_mfma_f32_16x16x32_bf16 v[156:159], v[212:215], v[44:47], v[156:159]\n"
"ds_read_b64_tr_b16 v[206:207], v254 offset:17536\n"
"s_waitcnt lgkmcnt(8)\n"
"v_mfma_f32_16x16x32_bf16 v[156:159], v[216:219], v[52:55], v[156:159]\n"
"ds_read_b64_tr_b16 v[208:209], v254 offset:16672\n"
"s_mov_b32 s70, 122352\n"
"v_mfma_f32_16x16x32_bf16 v[156:159], v[220:223], v[60:63], v[156:159]\n"
"buffer_load_dwordx4 v255, s[60:63], s70 offen offset:1040 lds\n"
"ds_read_b64_tr_b16 v[210:211], v254 offset:16800\n"
"v_mfma_f32_16x16x32_bf16 v[140:143], v[216:219], v[48:51], v[140:143]\n"
"ds_read_b64_tr_b16 v[212:213], v254 offset:16928\n"
"v_add_u32_e32 v244, s80, v244\n"
"v_mfma_f32_16x16x32_bf16 v[140:143], v[220:223], v[56:59], v[140:143]\n"
"ds_read_b64_tr_b16 v[214:215], v254 offset:17056\n"
"s_waitcnt lgkmcnt(8)\n"
"v_mfma_f32_16x16x32_bf16 v[128:131], v[192:195], v[96:99], v[128:131]\n"
"ds_read_b64_tr_b16 v[216:217], v254 offset:17184\n"
"v_bfe_u32 v245, v242, 4, 3\n"
"v_mfma_f32_16x16x32_bf16 v[128:131], v[196:199], v[104:107], v[128:131]\n"
"ds_read_b64_tr_b16 v[218:219], v254 offset:17312\n"
"v_lshl_add_u32 v244, v245, 7, v244\n"
"v_mfma_f32_16x16x32_bf16 v[144:147], v[192:195], v[100:103], v[144:147]\n"
"ds_read_b64_tr_b16 v[220:221], v254 offset:17440\n"
"v_cmp_gt_u32_e32 vcc, 256, v242\n"
"v_mfma_f32_16x16x32_bf16 v[144:147], v[196:199], v[108:111], v[144:147]\n"
"ds_read_b64_tr_b16 v[222:223], v254 offset:17568\n"
"s_waitcnt lgkmcnt(8)\n"
"v_mfma_f32_16x16x32_bf16 v[144:147], v[200:203], v[116:119], v[144:147]\n"
"ds_read_b64_tr_b16 v[192:193], v254 offset:16704\n"
"s_mov_b32 s69, 117728\n"
"v_mfma_f32_16x16x32_bf16 v[144:147], v[204:207], v[124:127], v[144:147]\n"
"buffer_load_dwordx4 v255, s[60:63], s69 offen offset:2080 lds\n"
"ds_read_b64_tr_b16 v[194:195], v254 offset:16832\n"
"v_mfma_f32_16x16x32_bf16 v[128:131], v[200:203], v[112:115], v[128:131]\n"
"ds_read_b64_tr_b16 v[196:197], v254 offset:16960\n"
"v_cvt_pk_bf16_f32 v236, v184, v185\n"
"v_mfma_f32_16x16x32_bf16 v[128:131], v[204:207], v[120:123], v[128:131]\n"
"ds_read_b64_tr_b16 v[198:199], v254 offset:17088\n"
"s_waitcnt lgkmcnt(8)\n"
"v_mfma_f32_16x16x32_bf16 v[132:135], v[208:211], v[96:99], v[132:135]\n"
"ds_read_b64_tr_b16 v[200:201], v254 offset:17216\n"
"v_cvt_pk_bf16_f32 v237, v186, v187\n"
"v_mfma_f32_16x16x32_bf16 v[132:135], v[212:215], v[104:107], v[132:135]\n"
"ds_read_b64_tr_b16 v[202:203], v254 offset:17344\n"
"v_cvt_pk_bf16_f32 v238, v188, v189\n"
"v_mfma_f32_16x16x32_bf16 v[148:151], v[208:211], v[100:103], v[148:151]\n"
"ds_read_b64_tr_b16 v[204:205], v254 offset:17472\n"
"v_cvt_pk_bf16_f32 v239, v190, v191\n"
"v_mfma_f32_16x16x32_bf16 v[148:151], v[212:215], v[108:111], v[148:151]\n"
"ds_read_b64_tr_b16 v[206:207], v254 offset:17600\n"
"s_waitcnt lgkmcnt(8)\n"
"v_mfma_f32_16x16x32_bf16 v[148:151], v[216:219], v[116:119], v[148:151]\n"
"ds_read_b64_tr_b16 v[208:209], v254 offset:16736\n"
"s_mov_b32 s70, 123856\n"
"v_mfma_f32_16x16x32_bf16 v[148:151], v[220:223], v[124:127], v[148:151]\n"
"buffer_load_dwordx4 v255, s[60:63], s70 offen offset:3120 lds\n"
"ds_read_b64_tr_b16 v[210:211], v254 offset:16864\n"
"v_mfma_f32_16x16x32_bf16 v[132:135], v[216:219], v[112:115], v[132:135]\n"
"ds_read_b64_tr_b16 v[212:213], v254 offset:16992\n"
"v_accvgpr_write_b32 a60, v236\n"
"v_mfma_f32_16x16x32_bf16 v[132:135], v[220:223], v[120:123], v[132:135]\n"
"ds_read_b64_tr_b16 v[214:215], v254 offset:17120\n"
"s_waitcnt lgkmcnt(8)\n"
"v_mfma_f32_16x16x32_bf16 v[136:139], v[192:195], v[96:99], v[136:139]\n"
"ds_read_b64_tr_b16 v[216:217], v254 offset:17248\n"
"s_waitcnt vmcnt(8)\n"
"v_mfma_f32_16x16x32_bf16 v[136:139], v[196:199], v[104:107], v[136:139]\n"
"ds_read_b64_tr_b16 v[218:219], v254 offset:17376\n"
"v_accvgpr_write_b32 a61, v237\n"
"v_mfma_f32_16x16x32_bf16 v[152:155], v[192:195], v[100:103], v[152:155]\n"
"ds_read_b64_tr_b16 v[220:221], v254 offset:17504\n"
"s_barrier\n"
"v_mfma_f32_16x16x32_bf16 v[152:155], v[196:199], v[108:111], v[152:155]\n"
"ds_read_b64_tr_b16 v[222:223], v254 offset:17632\n"
"s_mov_b32 s69, 768\n"
"s_waitcnt lgkmcnt(8)\n"
"v_mfma_f32_16x16x32_bf16 v[152:155], v[200:203], v[116:119], v[152:155]\n"
"ds_read_b64_tr_b16 v[192:193], v241 offset:0\n"
"s_add_u32 m0, s68, 49920\n"
"v_mfma_f32_16x16x32_bf16 v[152:155], v[204:207], v[124:127], v[152:155]\n"
"buffer_load_dwordx4 v255, s[60:63], s69 offen offset:0 lds\n"
"ds_read_b64_tr_b16 v[194:195], v241 offset:128\n"
"v_mfma_f32_16x16x32_bf16 v[136:139], v[200:203], v[112:115], v[136:139]\n"
"ds_read_b64_tr_b16 v[196:197], v241 offset:256\n"
"v_accvgpr_write_b32 a62, v238\n"
"v_mfma_f32_16x16x32_bf16 v[136:139], v[204:207], v[120:123], v[136:139]\n"
"ds_read_b64_tr_b16 v[198:199], v241 offset:384\n"
"s_waitcnt lgkmcnt(8)\n"
"v_mfma_f32_16x16x32_bf16 v[140:143], v[208:211], v[96:99], v[140:143]\n"
"ds_read_b64_tr_b16 v[200:201], v241 offset:512\n"
"v_accvgpr_write_b32 a63, v239\n"
"v_mfma_f32_16x16x32_bf16 v[140:143], v[212:215], v[104:107], v[140:143]\n"
"ds_read_b64_tr_b16 v[202:203], v241 offset:640\n"
"v_mov_b32_e32 v245, 0x10000000\n"
"v_mfma_f32_16x16x32_bf16 v[156:159], v[208:211], v[100:103], v[156:159]\n"
"ds_read_b64_tr_b16 v[204:205], v241 offset:768\n"
"v_cndmask_b32_e32 v243, v245, v243, vcc\n"
"v_mfma_f32_16x16x32_bf16 v[156:159], v[212:215], v[108:111], v[156:159]\n"
"ds_read_b64_tr_b16 v[206:207], v241 offset:896\n"
"s_waitcnt lgkmcnt(8)\n"
"v_mfma_f32_16x16x32_bf16 v[156:159], v[216:219], v[116:119], v[156:159]\n"
"ds_read_b64_tr_b16 v[208:209], v241 offset:32\n"
"s_mov_b32 s70, 6896\n"
"v_mfma_f32_16x16x32_bf16 v[156:159], v[220:223], v[124:127], v[156:159]\n"
"buffer_load_dwordx4 v255, s[60:63], s70 offen offset:1040 lds\n"
"ds_read_b64_tr_b16 v[210:211], v241 offset:160\n"
"v_mfma_f32_16x16x32_bf16 v[140:143], v[216:219], v[112:115], v[140:143]\n"
"ds_read_b64_tr_b16 v[212:213], v241 offset:288\n"
"v_cndmask_b32_e32 v244, v245, v244, vcc\n"
"v_mfma_f32_16x16x32_bf16 v[140:143], v[220:223], v[120:123], v[140:143]\n"
"ds_read_b64_tr_b16 v[214:215], v241 offset:416\n"
"s_waitcnt lgkmcnt(8)\n"
"v_mfma_f32_16x16x32_bf16 v[160:163], v[192:195], v[0:3], 0\n"
"ds_read_b64_tr_b16 v[216:217], v241 offset:544\n"
"v_cmp_gt_u32_e32 vcc, 128, v242\n"
"v_mfma_f32_16x16x32_bf16 v[160:163], v[196:199], v[8:11], v[160:163]\n"
"ds_read_b64_tr_b16 v[218:219], v241 offset:672\n"
"v_mov_b32_e32 v240, 0x10000000\n"
"v_mfma_f32_16x16x32_bf16 v[176:179], v[192:195], v[4:7], 0\n"
"ds_read_b64_tr_b16 v[220:221], v241 offset:800\n"
"v_cndmask_b32_e32 v240, v240, v244, vcc\n"
"v_mfma_f32_16x16x32_bf16 v[176:179], v[196:199], v[12:15], v[176:179]\n"
"ds_read_b64_tr_b16 v[222:223], v241 offset:928\n"
"s_waitcnt lgkmcnt(8)\n"
"v_mfma_f32_16x16x32_bf16 v[176:179], v[200:203], v[20:23], v[176:179]\n"
"ds_read_b64_tr_b16 v[192:193], v241 offset:64\n"
"s_mov_b32 s69, 2272\n"
"v_mfma_f32_16x16x32_bf16 v[176:179], v[204:207], v[28:31], v[176:179]\n"
"buffer_load_dwordx4 v255, s[60:63], s69 offen offset:2080 lds\n"
"ds_read_b64_tr_b16 v[194:195], v241 offset:192\n"
"v_mfma_f32_16x16x32_bf16 v[160:163], v[200:203], v[16:19], v[160:163]\n"
"ds_read_b64_tr_b16 v[196:197], v241 offset:320\n"
"v_cvt_pk_bf16_f32 v224, v128, v129\n"
"v_mfma_f32_16x16x32_bf16 v[160:163], v[204:207], v[24:27], v[160:163]\n"
"ds_read_b64_tr_b16 v[198:199], v241 offset:448\n"
"s_waitcnt lgkmcnt(8)\n"
"v_mfma_f32_16x16x32_bf16 v[164:167], v[208:211], v[0:3], 0\n"
"ds_read_b64_tr_b16 v[200:201], v241 offset:576\n"
"v_cvt_pk_bf16_f32 v225, v130, v131\n"
"v_mfma_f32_16x16x32_bf16 v[164:167], v[212:215], v[8:11], v[164:167]\n"
"ds_read_b64_tr_b16 v[202:203], v241 offset:704\n"
"v_cvt_pk_bf16_f32 v226, v132, v133\n"
"v_mfma_f32_16x16x32_bf16 v[180:183], v[208:211], v[4:7], 0\n"
"ds_read_b64_tr_b16 v[204:205], v241 offset:832\n"
"v_cvt_pk_bf16_f32 v227, v134, v135\n"
"v_mfma_f32_16x16x32_bf16 v[180:183], v[212:215], v[12:15], v[180:183]\n"
"ds_read_b64_tr_b16 v[206:207], v241 offset:960\n"
"s_waitcnt lgkmcnt(8)\n"
"v_mfma_f32_16x16x32_bf16 v[180:183], v[216:219], v[20:23], v[180:183]\n"
"ds_read_b64_tr_b16 v[208:209], v241 offset:96\n"
"s_mov_b32 s70, 8400\n"
"v_mfma_f32_16x16x32_bf16 v[180:183], v[220:223], v[28:31], v[180:183]\n"
"buffer_load_dwordx4 v255, s[60:63], s70 offen offset:3120 lds\n"
"ds_read_b64_tr_b16 v[210:211], v241 offset:224\n"
"v_mfma_f32_16x16x32_bf16 v[164:167], v[216:219], v[16:19], v[164:167]\n"
"ds_read_b64_tr_b16 v[212:213], v241 offset:352\n"
"v_accvgpr_write_b32 a64, v224\n"
"v_mfma_f32_16x16x32_bf16 v[164:167], v[220:223], v[24:27], v[164:167]\n"
"ds_read_b64_tr_b16 v[214:215], v241 offset:480\n"
"s_waitcnt lgkmcnt(8)\n"
"v_mfma_f32_16x16x32_bf16 v[168:171], v[192:195], v[0:3], 0\n"
"ds_read_b64_tr_b16 v[216:217], v241 offset:608\n"
"v_accvgpr_write_b32 a65, v225\n"
"v_mfma_f32_16x16x32_bf16 v[168:171], v[196:199], v[8:11], v[168:171]\n"
"ds_read_b64_tr_b16 v[218:219], v241 offset:736\n"
"v_accvgpr_write_b32 a66, v226\n"
"v_mfma_f32_16x16x32_bf16 v[184:187], v[192:195], v[4:7], 0\n"
"ds_read_b64_tr_b16 v[220:221], v241 offset:864\n"
"v_accvgpr_write_b32 a67, v227\n"
"v_mfma_f32_16x16x32_bf16 v[184:187], v[196:199], v[12:15], v[184:187]\n"
"ds_read_b64_tr_b16 v[222:223], v241 offset:992\n"
"s_mov_b32 s69, 1664\n"
"s_waitcnt lgkmcnt(8)\n"
"v_mfma_f32_16x16x32_bf16 v[184:187], v[200:203], v[20:23], v[184:187]\n"
"ds_read_b64_tr_b16 v[192:193], v254 offset:0\n"
"s_add_u32 m0, s68, 54080\n"
"v_mfma_f32_16x16x32_bf16 v[184:187], v[204:207], v[28:31], v[184:187]\n"
"buffer_load_dwordx4 v255, s[60:63], s69 offen offset:0 lds\n"
"ds_read_b64_tr_b16 v[194:195], v254 offset:128\n"
"v_mfma_f32_16x16x32_bf16 v[168:171], v[200:203], v[16:19], v[168:171]\n"
"ds_read_b64_tr_b16 v[196:197], v254 offset:256\n"
"v_bfe_u32 v245, v242, 4, 4\n"
"v_mfma_f32_16x16x32_bf16 v[168:171], v[204:207], v[24:27], v[168:171]\n"
"ds_read_b64_tr_b16 v[198:199], v254 offset:384\n"
"s_waitcnt lgkmcnt(8)\n"
"v_mfma_f32_16x16x32_bf16 v[172:175], v[208:211], v[0:3], 0\n"
"ds_read_b64_tr_b16 v[200:201], v254 offset:512\n"
"v_mul_u32_u24_e32 v247, 896, v245\n"
"v_mfma_f32_16x16x32_bf16 v[172:175], v[212:215], v[8:11], v[172:175]\n"
"ds_read_b64_tr_b16 v[202:203], v254 offset:640\n"
"v_bfe_u32 v245, v242, 8, 1\n"
"v_mfma_f32_16x16x32_bf16 v[188:191], v[208:211], v[4:7], 0\n"
"ds_read_b64_tr_b16 v[204:205], v254 offset:768\n"
"v_lshl_add_u32 v247, v245, 5, v247\n"
"v_mfma_f32_16x16x32_bf16 v[188:191], v[212:215], v[12:15], v[188:191]\n"
"ds_read_b64_tr_b16 v[206:207], v254 offset:896\n"
"s_waitcnt lgkmcnt(8)\n"
"v_mfma_f32_16x16x32_bf16 v[188:191], v[216:219], v[20:23], v[188:191]\n"
"ds_read_b64_tr_b16 v[208:209], v254 offset:32\n"
"s_mov_b32 s70, 7792\n"
"v_mfma_f32_16x16x32_bf16 v[188:191], v[220:223], v[28:31], v[188:191]\n"
"buffer_load_dwordx4 v255, s[60:63], s70 offen offset:1040 lds\n"
"ds_read_b64_tr_b16 v[210:211], v254 offset:160\n"
"v_mfma_f32_16x16x32_bf16 v[172:175], v[216:219], v[16:19], v[172:175]\n"
"ds_read_b64_tr_b16 v[212:213], v254 offset:288\n"
"v_bfe_u32 v245, v242, 9, 1\n"
"v_mfma_f32_16x16x32_bf16 v[172:175], v[220:223], v[24:27], v[172:175]\n"
"ds_read_b64_tr_b16 v[214:215], v254 offset:416\n"
"s_waitcnt lgkmcnt(8)\n"
"v_mfma_f32_16x16x32_bf16 v[160:163], v[192:195], v[64:67], v[160:163]\n"
"ds_read_b64_tr_b16 v[216:217], v254 offset:544\n"
"v_lshl_add_u32 v247, v245, 4, v247\n"
"v_mfma_f32_16x16x32_bf16 v[160:163], v[196:199], v[72:75], v[160:163]\n"
"ds_read_b64_tr_b16 v[218:219], v254 offset:672\n"
"v_mfma_f32_16x16x32_bf16 v[176:179], v[192:195], v[68:71], v[176:179]\n"
"ds_read_b64_tr_b16 v[220:221], v254 offset:800\n"
"v_mfma_f32_16x16x32_bf16 v[176:179], v[196:199], v[76:79], v[176:179]\n"
"ds_read_b64_tr_b16 v[222:223], v254 offset:928\n"
"s_waitcnt lgkmcnt(8)\n"
"v_mfma_f32_16x16x32_bf16 v[176:179], v[200:203], v[84:87], v[176:179]\n"
"ds_read_b64_tr_b16 v[192:193], v254 offset:64\n"
"s_mov_b32 s69, 3168\n"
"v_mfma_f32_16x16x32_bf16 v[176:179], v[204:207], v[92:95], v[176:179]\n"
"buffer_load_dwordx4 v255, s[60:63], s69 offen offset:2080 lds\n"
"ds_read_b64_tr_b16 v[194:195], v254 offset:192\n"
"v_mfma_f32_16x16x32_bf16 v[160:163], v[200:203], v[80:83], v[160:163]\n"
"ds_read_b64_tr_b16 v[196:197], v254 offset:320\n"
"v_cvt_pk_bf16_f32 v228, v136, v137\n"
"v_mfma_f32_16x16x32_bf16 v[160:163], v[204:207], v[88:91], v[160:163]\n"
"ds_read_b64_tr_b16 v[198:199], v254 offset:448\n"
"s_waitcnt lgkmcnt(8)\n"
"v_mfma_f32_16x16x32_bf16 v[164:167], v[208:211], v[64:67], v[164:167]\n"
"ds_read_b64_tr_b16 v[200:201], v254 offset:576\n"
"v_cvt_pk_bf16_f32 v229, v138, v139\n"
"v_mfma_f32_16x16x32_bf16 v[164:167], v[212:215], v[72:75], v[164:167]\n"
"ds_read_b64_tr_b16 v[202:203], v254 offset:704\n"
"v_cvt_pk_bf16_f32 v230, v140, v141\n"
"v_mfma_f32_16x16x32_bf16 v[180:183], v[208:211], v[68:71], v[180:183]\n"
"ds_read_b64_tr_b16 v[204:205], v254 offset:832\n"
"v_cvt_pk_bf16_f32 v231, v142, v143\n"
"v_mfma_f32_16x16x32_bf16 v[180:183], v[212:215], v[76:79], v[180:183]\n"
"ds_read_b64_tr_b16 v[206:207], v254 offset:960\n"
"s_waitcnt lgkmcnt(8)\n"
"v_mfma_f32_16x16x32_bf16 v[180:183], v[216:219], v[84:87], v[180:183]\n"
"ds_read_b64_tr_b16 v[208:209], v254 offset:96\n"
"s_mov_b32 s70, 9296\n"
"v_mfma_f32_16x16x32_bf16 v[180:183], v[220:223], v[92:95], v[180:183]\n"
"buffer_load_dwordx4 v255, s[60:63], s70 offen offset:3120 lds\n"
"ds_read_b64_tr_b16 v[210:211], v254 offset:224\n"
"v_mfma_f32_16x16x32_bf16 v[164:167], v[216:219], v[80:83], v[164:167]\n"
"ds_read_b64_tr_b16 v[212:213], v254 offset:352\n"
"v_accvgpr_write_b32 a68, v228\n"
"v_mfma_f32_16x16x32_bf16 v[164:167], v[220:223], v[88:91], v[164:167]\n"
"ds_read_b64_tr_b16 v[214:215], v254 offset:480\n"
"s_waitcnt lgkmcnt(8)\n"
"v_mfma_f32_16x16x32_bf16 v[168:171], v[192:195], v[64:67], v[168:171]\n"
"ds_read_b64_tr_b16 v[216:217], v254 offset:608\n"
"s_waitcnt vmcnt(8)\n"
"v_mfma_f32_16x16x32_bf16 v[168:171], v[196:199], v[72:75], v[168:171]\n"
"ds_read_b64_tr_b16 v[218:219], v254 offset:736\n"
"v_accvgpr_write_b32 a69, v229\n"
"v_mfma_f32_16x16x32_bf16 v[184:187], v[192:195], v[68:71], v[184:187]\n"
"ds_read_b64_tr_b16 v[220:221], v254 offset:864\n"
"s_barrier\n"
"v_mfma_f32_16x16x32_bf16 v[184:187], v[196:199], v[76:79], v[184:187]\n"
"ds_read_b64_tr_b16 v[222:223], v254 offset:992\n"
"s_mov_b32 s69, 115456\n"
"s_waitcnt lgkmcnt(8)\n"
"v_mfma_f32_16x16x32_bf16 v[184:187], v[200:203], v[84:87], v[184:187]\n"
"ds_read_b64_tr_b16 v[192:193], v241 offset:33280\n"
"s_add_u32 m0, s68, 16640\n"
"v_mfma_f32_16x16x32_bf16 v[184:187], v[204:207], v[92:95], v[184:187]\n"
"buffer_load_dwordx4 v255, s[60:63], s69 offen offset:0 lds\n"
"ds_read_b64_tr_b16 v[194:195], v241 offset:33408\n"
"v_mfma_f32_16x16x32_bf16 v[168:171], v[200:203], v[80:83], v[168:171]\n"
"ds_read_b64_tr_b16 v[196:197], v241 offset:33536\n"
"v_accvgpr_write_b32 a70, v230\n"
"v_mfma_f32_16x16x32_bf16 v[168:171], v[204:207], v[88:91], v[168:171]\n"
"ds_read_b64_tr_b16 v[198:199], v241 offset:33664\n"
"s_waitcnt lgkmcnt(8)\n"
"v_mfma_f32_16x16x32_bf16 v[172:175], v[208:211], v[64:67], v[172:175]\n"
"ds_read_b64_tr_b16 v[200:201], v241 offset:33792\n"
"s_mov_b32 s70, 121584\n"
"v_mfma_f32_16x16x32_bf16 v[172:175], v[212:215], v[72:75], v[172:175]\n"
"buffer_load_dwordx4 v255, s[60:63], s70 offen offset:1040 lds\n"
"ds_read_b64_tr_b16 v[202:203], v241 offset:33920\n"
"v_mfma_f32_16x16x32_bf16 v[188:191], v[208:211], v[68:71], v[188:191]\n"
"ds_read_b64_tr_b16 v[204:205], v241 offset:34048\n"
"v_accvgpr_write_b32 a71, v231\n"
"v_mfma_f32_16x16x32_bf16 v[188:191], v[212:215], v[76:79], v[188:191]\n"
"ds_read_b64_tr_b16 v[206:207], v241 offset:34176\n"
"s_waitcnt lgkmcnt(8)\n"
"v_mfma_f32_16x16x32_bf16 v[188:191], v[216:219], v[84:87], v[188:191]\n"
"ds_read_b64_tr_b16 v[208:209], v241 offset:33312\n"
"s_mov_b32 s69, 116960\n"
"v_mfma_f32_16x16x32_bf16 v[188:191], v[220:223], v[92:95], v[188:191]\n"
"buffer_load_dwordx4 v255, s[60:63], s69 offen offset:2080 lds\n"
"ds_read_b64_tr_b16 v[210:211], v241 offset:33440\n"
"v_mfma_f32_16x16x32_bf16 v[172:175], v[216:219], v[80:83], v[172:175]\n"
"ds_read_b64_tr_b16 v[212:213], v241 offset:33568\n"
"v_mfma_f32_16x16x32_bf16 v[172:175], v[220:223], v[88:91], v[172:175]\n"
"ds_read_b64_tr_b16 v[214:215], v241 offset:33696\n"
"s_waitcnt lgkmcnt(8)\n"
"v_mfma_f32_16x16x32_bf16 v[160:163], v[192:195], v[32:35], v[160:163]\n"
"ds_read_b64_tr_b16 v[216:217], v241 offset:33824\n"
"s_mov_b32 s70, 123088\n"
"v_mfma_f32_16x16x32_bf16 v[160:163], v[196:199], v[40:43], v[160:163]\n"
"buffer_load_dwordx4 v255, s[60:63], s70 offen offset:3120 lds\n"
"ds_read_b64_tr_b16 v[218:219], v241 offset:33952\n"
"v_mfma_f32_16x16x32_bf16 v[176:179], v[192:195], v[36:39], v[176:179]\n"
"ds_read_b64_tr_b16 v[220:221], v241 offset:34080\n"
"v_mfma_f32_16x16x32_bf16 v[176:179], v[196:199], v[44:47], v[176:179]\n"
"ds_read_b64_tr_b16 v[222:223], v241 offset:34208\n"
"s_mov_b32 s69, 116352\n"
"s_waitcnt lgkmcnt(8)\n"
"v_mfma_f32_16x16x32_bf16 v[176:179], v[200:203], v[52:55], v[176:179]\n"
"ds_read_b64_tr_b16 v[192:193], v241 offset:33344\n"
"s_add_u32 m0, s68, 20800\n"
"v_mfma_f32_16x16x32_bf16 v[176:179], v[204:207], v[60:63], v[176:179]\n"
"buffer_load_dwordx4 v255, s[60:63], s69 offen offset:0 lds\n"
"ds_read_b64_tr_b16 v[194:195], v241 offset:33472\n"
"v_mfma_f32_16x16x32_bf16 v[160:163], v[200:203], v[48:51], v[160:163]\n"
"ds_read_b64_tr_b16 v[196:197], v241 offset:33600\n"
"v_cvt_pk_bf16_f32 v232, v144, v145\n"
"v_mfma_f32_16x16x32_bf16 v[160:163], v[204:207], v[56:59], v[160:163]\n"
"ds_read_b64_tr_b16 v[198:199], v241 offset:33728\n"
"s_waitcnt lgkmcnt(8)\n"
"v_mfma_f32_16x16x32_bf16 v[164:167], v[208:211], v[32:35], v[164:167]\n"
"ds_read_b64_tr_b16 v[200:201], v241 offset:33856\n"
"s_mov_b32 s70, 122480\n"
"v_mfma_f32_16x16x32_bf16 v[164:167], v[212:215], v[40:43], v[164:167]\n"
"buffer_load_dwordx4 v255, s[60:63], s70 offen offset:1040 lds\n"
"ds_read_b64_tr_b16 v[202:203], v241 offset:33984\n"
"v_mfma_f32_16x16x32_bf16 v[180:183], v[208:211], v[36:39], v[180:183]\n"
"ds_read_b64_tr_b16 v[204:205], v241 offset:34112\n"
"v_cvt_pk_bf16_f32 v233, v146, v147\n"
"v_mfma_f32_16x16x32_bf16 v[180:183], v[212:215], v[44:47], v[180:183]\n"
"ds_read_b64_tr_b16 v[206:207], v241 offset:34240\n"
"s_waitcnt lgkmcnt(8)\n"
"v_mfma_f32_16x16x32_bf16 v[180:183], v[216:219], v[52:55], v[180:183]\n"
"ds_read_b64_tr_b16 v[208:209], v241 offset:33376\n"
"s_mov_b32 s69, 117856\n"
"v_mfma_f32_16x16x32_bf16 v[180:183], v[220:223], v[60:63], v[180:183]\n"
"buffer_load_dwordx4 v255, s[60:63], s69 offen offset:2080 lds\n"
"ds_read_b64_tr_b16 v[210:211], v241 offset:33504\n"
"v_mfma_f32_16x16x32_bf16 v[164:167], v[216:219], v[48:51], v[164:167]\n"
"ds_read_b64_tr_b16 v[212:213], v241 offset:33632\n"
"buffer_load_dword v245, v244, s[36:39], s35 offen offset:0 sc1\n"
"v_cvt_pk_bf16_f32 v234, v148, v149\n"
"v_mfma_f32_16x16x32_bf16 v[164:167], v[220:223], v[56:59], v[164:167]\n"
"ds_read_b64_tr_b16 v[214:215], v241 offset:33760\n"
"s_waitcnt lgkmcnt(8)\n"
"v_mfma_f32_16x16x32_bf16 v[168:171], v[192:195], v[32:35], v[168:171]\n"
"ds_read_b64_tr_b16 v[216:217], v241 offset:33888\n"
"s_mov_b32 s70, 123984\n"
"v_mfma_f32_16x16x32_bf16 v[168:171], v[196:199], v[40:43], v[168:171]\n"
"buffer_load_dwordx4 v255, s[60:63], s70 offen offset:3120 lds\n"
"ds_read_b64_tr_b16 v[218:219], v241 offset:34016\n"
"v_mfma_f32_16x16x32_bf16 v[184:187], v[192:195], v[36:39], v[184:187]\n"
"ds_read_b64_tr_b16 v[220:221], v241 offset:34144\n"
"buffer_load_dword v246, v243, s[20:23], s35 offen offset:0 sc1\n"
"v_cvt_pk_bf16_f32 v235, v150, v151\n"
"v_mfma_f32_16x16x32_bf16 v[184:187], v[196:199], v[44:47], v[184:187]\n"
"ds_read_b64_tr_b16 v[222:223], v241 offset:34272\n"
"s_waitcnt lgkmcnt(8)\n"
"v_mfma_f32_16x16x32_bf16 v[184:187], v[200:203], v[52:55], v[184:187]\n"
"ds_read_b64_tr_b16 v[192:193], v254 offset:33280\n"
"v_accvgpr_write_b32 a72, v232\n"
"v_mfma_f32_16x16x32_bf16 v[184:187], v[204:207], v[60:63], v[184:187]\n"
"ds_read_b64_tr_b16 v[194:195], v254 offset:33408\n"
"s_mov_b32 s81, 0\n"
"v_accvgpr_write_b32 a73, v233\n"
"v_mfma_f32_16x16x32_bf16 v[168:171], v[200:203], v[48:51], v[168:171]\n"
"ds_read_b64_tr_b16 v[196:197], v254 offset:33536\n"
"buffer_store_dwordx4 a[0:3], v242, s[64:67], s81 offen offset:0 sc1\n"
"v_accvgpr_write_b32 a74, v234\n"
"v_mfma_f32_16x16x32_bf16 v[168:171], v[204:207], v[56:59], v[168:171]\n"
"ds_read_b64_tr_b16 v[198:199], v254 offset:33664\n"
"s_waitcnt lgkmcnt(8)\n"
"v_mfma_f32_16x16x32_bf16 v[172:175], v[208:211], v[32:35], v[172:175]\n"
"ds_read_b64_tr_b16 v[200:201], v254 offset:33792\n"
"v_accvgpr_write_b32 a75, v235\n"
"v_mfma_f32_16x16x32_bf16 v[172:175], v[212:215], v[40:43], v[172:175]\n"
"ds_read_b64_tr_b16 v[202:203], v254 offset:33920\n"
"v_mfma_f32_16x16x32_bf16 v[188:191], v[208:211], v[36:39], v[188:191]\n"
"ds_read_b64_tr_b16 v[204:205], v254 offset:34048\n"
"v_mfma_f32_16x16x32_bf16 v[188:191], v[212:215], v[44:47], v[188:191]\n"
"ds_read_b64_tr_b16 v[206:207], v254 offset:34176\n"
"s_waitcnt lgkmcnt(8)\n"
"v_mfma_f32_16x16x32_bf16 v[188:191], v[216:219], v[52:55], v[188:191]\n"
"ds_read_b64_tr_b16 v[208:209], v254 offset:33312\n"
"v_mfma_f32_16x16x32_bf16 v[188:191], v[220:223], v[60:63], v[188:191]\n"
"ds_read_b64_tr_b16 v[210:211], v254 offset:33440\n"
"buffer_store_dwordx4 a[4:7], v242, s[64:67], s81 offen offset:1024 sc1\n"
"v_mfma_f32_16x16x32_bf16 v[172:175], v[216:219], v[48:51], v[172:175]\n"
"ds_read_b64_tr_b16 v[212:213], v254 offset:33568\n"
"v_mfma_f32_16x16x32_bf16 v[172:175], v[220:223], v[56:59], v[172:175]\n"
"ds_read_b64_tr_b16 v[214:215], v254 offset:33696\n"
"s_waitcnt lgkmcnt(8)\n"
"v_mfma_f32_16x16x32_bf16 v[160:163], v[192:195], v[96:99], v[160:163]\n"
"ds_read_b64_tr_b16 v[216:217], v254 offset:33824\n"
"v_mfma_f32_16x16x32_bf16 v[160:163], v[196:199], v[104:107], v[160:163]\n"
"ds_read_b64_tr_b16 v[218:219], v254 offset:33952\n"
"v_mfma_f32_16x16x32_bf16 v[176:179], v[192:195], v[100:103], v[176:179]\n"
"ds_read_b64_tr_b16 v[220:221], v254 offset:34080\n"
"v_mfma_f32_16x16x32_bf16 v[176:179], v[196:199], v[108:111], v[176:179]\n"
"ds_read_b64_tr_b16 v[222:223], v254 offset:34208\n"
"s_waitcnt lgkmcnt(8)\n"
"s_mov_b32 s81, 118784\n"
"v_mfma_f32_16x16x32_bf16 v[176:179], v[200:203], v[116:119], v[176:179]\n"
"ds_read_b64_tr_b16 v[192:193], v254 offset:33344\n"
"buffer_store_dwordx4 a[8:11], v242, s[56:59], s81 offen offset:0 sc1\n"
"v_cvt_pk_bf16_f32 v236, v152, v153\n"
"v_mfma_f32_16x16x32_bf16 v[176:179], v[204:207], v[124:127], v[176:179]\n"
"ds_read_b64_tr_b16 v[194:195], v254 offset:33472\n"
"v_cvt_pk_bf16_f32 v237, v154, v155\n"
"v_mfma_f32_16x16x32_bf16 v[160:163], v[200:203], v[112:115], v[160:163]\n"
"ds_read_b64_tr_b16 v[196:197], v254 offset:33600\n"
"v_cvt_pk_bf16_f32 v238, v156, v157\n"
"v_mfma_f32_16x16x32_bf16 v[160:163], v[204:207], v[120:123], v[160:163]\n"
"ds_read_b64_tr_b16 v[198:199], v254 offset:33728\n"
"s_waitcnt lgkmcnt(8)\n"
"v_mfma_f32_16x16x32_bf16 v[164:167], v[208:211], v[96:99], v[164:167]\n"
"ds_read_b64_tr_b16 v[200:201], v254 offset:33856\n"
"v_cvt_pk_bf16_f32 v239, v158, v159\n"
"v_mfma_f32_16x16x32_bf16 v[164:167], v[212:215], v[104:107], v[164:167]\n"
"ds_read_b64_tr_b16 v[202:203], v254 offset:33984\n"
"v_accvgpr_write_b32 a76, v236\n"
"v_mfma_f32_16x16x32_bf16 v[180:183], v[208:211], v[100:103], v[180:183]\n"
"ds_read_b64_tr_b16 v[204:205], v254 offset:34112\n"
"v_accvgpr_write_b32 a77, v237\n"
"v_mfma_f32_16x16x32_bf16 v[180:183], v[212:215], v[108:111], v[180:183]\n"
"ds_read_b64_tr_b16 v[206:207], v254 offset:34240\n"
"s_waitcnt lgkmcnt(8)\n"
"buffer_store_dwordx4 a[12:15], v242, s[56:59], s81 offen offset:1024 sc1\n"
"v_mfma_f32_16x16x32_bf16 v[180:183], v[216:219], v[116:119], v[180:183]\n"
"ds_read_b64_tr_b16 v[208:209], v254 offset:33376\n"
"v_accvgpr_write_b32 a78, v238\n"
"v_mfma_f32_16x16x32_bf16 v[180:183], v[220:223], v[124:127], v[180:183]\n"
"ds_read_b64_tr_b16 v[210:211], v254 offset:33504\n"
"v_accvgpr_write_b32 a79, v239\n"
"v_mfma_f32_16x16x32_bf16 v[164:167], v[216:219], v[112:115], v[164:167]\n"
"ds_read_b64_tr_b16 v[212:213], v254 offset:33632\n"
"v_mfma_f32_16x16x32_bf16 v[164:167], v[220:223], v[120:123], v[164:167]\n"
"ds_read_b64_tr_b16 v[214:215], v254 offset:33760\n"
"s_waitcnt lgkmcnt(8)\n"
"v_mfma_f32_16x16x32_bf16 v[168:171], v[192:195], v[96:99], v[168:171]\n"
"ds_read_b64_tr_b16 v[216:217], v254 offset:33888\n"
"s_waitcnt vmcnt(14)\n"
"v_mfma_f32_16x16x32_bf16 v[168:171], v[196:199], v[104:107], v[168:171]\n"
"ds_read_b64_tr_b16 v[218:219], v254 offset:34016\n"
"s_mov_b32 s81, 2048\n"
"s_branch .Lpo13\n"
".Lpg13:\n"
"s_setpc_b64 s[78:79]\n"
".Lpo13:\n"
"v_mfma_f32_16x16x32_bf16 v[184:187], v[192:195], v[100:103], v[184:187]\n"
"ds_read_b64_tr_b16 v[220:221], v254 offset:34144\n"
"s_barrier\n"
"buffer_store_dwordx4 a[16:19], v242, s[64:67], s81 offen offset:0 sc1\n"
"v_mfma_f32_16x16x32_bf16 v[184:187], v[196:199], v[108:111], v[184:187]\n"
"ds_read_b64_tr_b16 v[222:223], v254 offset:34272\n"
"s_waitcnt lgkmcnt(8)\n"
"v_mfma_f32_16x16x32_bf16 v[184:187], v[200:203], v[116:119], v[184:187]\n"
"ds_read_b64_tr_b16 v[192:193], v241 offset:49920\n"
"v_mfma_f32_16x16x32_bf16 v[184:187], v[204:207], v[124:127], v[184:187]\n"
"ds_read_b64_tr_b16 v[194:195], v241 offset:50048\n"
"v_mfma_f32_16x16x32_bf16 v[168:171], v[200:203], v[112:115], v[168:171]\n"
"ds_read_b64_tr_b16 v[196:197], v241 offset:50176\n"
"v_mfma_f32_16x16x32_bf16 v[168:171], v[204:207], v[120:123], v[168:171]\n"
"ds_read_b64_tr_b16 v[198:199], v241 offset:50304\n"
"s_waitcnt lgkmcnt(8)\n"
"v_mfma_f32_16x16x32_bf16 v[172:175], v[208:211], v[96:99], v[172:175]\n"
"ds_read_b64_tr_b16 v[200:201], v241 offset:50432\n"
"v_mfma_f32_16x16x32_bf16 v[172:175], v[212:215], v[104:107], v[172:175]\n"
"ds_read_b64_tr_b16 v[202:203], v241 offset:50560\n"
"buffer_store_dwordx4 a[20:23], v242, s[64:67], s81 offen offset:1024 sc1\n"
"v_mfma_f32_16x16x32_bf16 v[188:191], v[208:211], v[100:103], v[188:191]\n"
"ds_read_b64_tr_b16 v[204:205], v241 offset:50688\n"
"v_mfma_f32_16x16x32_bf16 v[188:191], v[212:215], v[108:111], v[188:191]\n"
"ds_read_b64_tr_b16 v[206:207], v241 offset:50816\n"
"s_waitcnt lgkmcnt(8)\n"
"v_mfma_f32_16x16x32_bf16 v[188:191], v[216:219], v[116:119], v[188:191]\n"
"ds_read_b64_tr_b16 v[208:209], v241 offset:49952\n"
"v_mfma_f32_16x16x32_bf16 v[188:191], v[220:223], v[124:127], v[188:191]\n"
"ds_read_b64_tr_b16 v[210:211], v241 offset:50080\n"
"v_mfma_f32_16x16x32_bf16 v[172:175], v[216:219], v[112:115], v[172:175]\n"
"ds_read_b64_tr_b16 v[212:213], v241 offset:50208\n"
"v_mfma_f32_16x16x32_bf16 v[172:175], v[220:223], v[120:123], v[172:175]\n"
"ds_read_b64_tr_b16 v[214:215], v241 offset:50336\n"
"s_waitcnt lgkmcnt(8)\n"
"s_mov_b32 s81, 120832\n"
"v_mfma_f32_16x16x32_bf16 v[128:131], v[192:195], v[0:3], 0\n"
"ds_read_b64_tr_b16 v[216:217], v241 offset:50464\n"
"buffer_store_dwordx4 a[24:27], v242, s[56:59], s81 offen offset:0 sc1\n"
"v_mfma_f32_16x16x32_bf16 v[128:131], v[196:199], v[8:11], v[128:131]\n"
"ds_read_b64_tr_b16 v[218:219], v241 offset:50592\n"
"v_mfma_f32_16x16x32_bf16 v[144:147], v[192:195], v[4:7], 0\n"
"ds_read_b64_tr_b16 v[220:221], v241 offset:50720\n"
"v_mfma_f32_16x16x32_bf16 v[144:147], v[196:199], v[12:15], v[144:147]\n"
"ds_read_b64_tr_b16 v[222:223], v241 offset:50848\n"
"s_waitcnt lgkmcnt(8)\n"
"v_mfma_f32_16x16x32_bf16 v[144:147], v[200:203], v[20:23], v[144:147]\n"
"ds_read_b64_tr_b16 v[192:193], v241 offset:49984\n"
"v_cvt_pk_bf16_f32 v224, v160, v161\n"
"v_mfma_f32_16x16x32_bf16 v[144:147], v[204:207], v[28:31], v[144:147]\n"
"ds_read_b64_tr_b16 v[194:195], v241 offset:50112\n"
"v_cvt_pk_bf16_f32 v225, v162, v163\n"
"v_mfma_f32_16x16x32_bf16 v[128:131], v[200:203], v[16:19], v[128:131]\n"
"ds_read_b64_tr_b16 v[196:197], v241 offset:50240\n"
"v_cvt_pk_bf16_f32 v226, v164, v165\n"
"v_mfma_f32_16x16x32_bf16 v[128:131], v[204:207], v[24:27], v[128:131]\n"
"ds_read_b64_tr_b16 v[198:199], v241 offset:50368\n"
"s_waitcnt lgkmcnt(8)\n"
"buffer_store_dwordx4 a[28:31], v242, s[56:59], s81 offen offset:1024 sc1\n"
"v_mfma_f32_16x16x32_bf16 v[132:135], v[208:211], v[0:3], 0\n"
"ds_read_b64_tr_b16 v[200:201], v241 offset:50496\n"
"v_cvt_pk_bf16_f32 v227, v166, v167\n"
"v_mfma_f32_16x16x32_bf16 v[132:135], v[212:215], v[8:11], v[132:135]\n"
"ds_read_b64_tr_b16 v[202:203], v241 offset:50624\n"
"v_accvgpr_write_b32 a80, v224\n"
"v_mfma_f32_16x16x32_bf16 v[148:151], v[208:211], v[4:7], 0\n"
"ds_read_b64_tr_b16 v[204:205], v241 offset:50752\n"
"v_accvgpr_write_b32 a81, v225\n"
"v_mfma_f32_16x16x32_bf16 v[148:151], v[212:215], v[12:15], v[148:151]\n"
"ds_read_b64_tr_b16 v[206:207], v241 offset:50880\n"
"s_waitcnt lgkmcnt(8)\n"
"v_mfma_f32_16x16x32_bf16 v[148:151], v[216:219], v[20:23], v[148:151]\n"
"ds_read_b64_tr_b16 v[208:209], v241 offset:50016\n"
"v_accvgpr_write_b32 a82, v226\n"
"v_mfma_f32_16x16x32_bf16 v[148:151], v[220:223], v[28:31], v[148:151]\n"
"ds_read_b64_tr_b16 v[210:211], v241 offset:50144\n"
"s_mov_b32 s81, 4096\n"
"v_accvgpr_write_b32 a83, v227\n"
"v_mfma_f32_16x16x32_bf16 v[132:135], v[216:219], v[16:19], v[132:135]\n"
"ds_read_b64_tr_b16 v[212:213], v241 offset:50272\n"
"buffer_store_dwordx4 a[32:35], v242, s[64:67], s81 offen offset:0 sc1\n"
"v_mfma_f32_16x16x32_bf16 v[132:135], v[220:223], v[24:27], v[132:135]\n"
"ds_read_b64_tr_b16 v[214:215], v241 offset:50400\n"
"s_waitcnt lgkmcnt(8)\n"
"v_mfma_f32_16x16x32_bf16 v[136:139], v[192:195], v[0:3], 0\n"
"ds_read_b64_tr_b16 v[216:217], v241 offset:50528\n"
"v_mfma_f32_16x16x32_bf16 v[136:139], v[196:199], v[8:11], v[136:139]\n"
"ds_read_b64_tr_b16 v[218:219], v241 offset:50656\n"
"v_mfma_f32_16x16x32_bf16 v[152:155], v[192:195], v[4:7], 0\n"
"ds_read_b64_tr_b16 v[220:221], v241 offset:50784\n"
"v_mfma_f32_16x16x32_bf16 v[152:155], v[196:199], v[12:15], v[152:155]\n"
"ds_read_b64_tr_b16 v[222:223], v241 offset:50912\n"
"s_waitcnt lgkmcnt(8)\n"
"v_mfma_f32_16x16x32_bf16 v[152:155], v[200:203], v[20:23], v[152:155]\n"
"ds_read_b64_tr_b16 v[192:193], v254 offset:49920\n"
"v_cvt_pk_bf16_f32 v228, v168, v169\n"
"v_mfma_f32_16x16x32_bf16 v[152:155], v[204:207], v[28:31], v[152:155]\n"
"ds_read_b64_tr_b16 v[194:195], v254 offset:50048\n"
"buffer_store_dwordx4 a[36:39], v242, s[64:67], s81 offen offset:1024 sc1\n"
"v_cvt_pk_bf16_f32 v229, v170, v171\n"
"v_mfma_f32_16x16x32_bf16 v[136:139], v[200:203], v[16:19], v[136:139]\n"
"ds_read_b64_tr_b16 v[196:197], v254 offset:50176\n"
"v_cvt_pk_bf16_f32 v230, v172, v173\n"
"v_mfma_f32_16x16x32_bf16 v[136:139], v[204:207], v[24:27], v[136:139]\n"
"ds_read_b64_tr_b16 v[198:199], v254 offset:50304\n"
"s_waitcnt lgkmcnt(8)\n"
"v_mfma_f32_16x16x32_bf16 v[140:143], v[208:211], v[0:3], 0\n"
"ds_read_b64_tr_b16 v[200:201], v254 offset:50432\n"
"v_cvt_pk_bf16_f32 v231, v174, v175\n"
"v_mfma_f32_16x16x32_bf16 v[140:143], v[212:215], v[8:11], v[140:143]\n"
"ds_read_b64_tr_b16 v[202:203], v254 offset:50560\n"
"v_accvgpr_write_b32 a84, v228\n"
"v_mfma_f32_16x16x32_bf16 v[156:159], v[208:211], v[4:7], 0\n"
"ds_read_b64_tr_b16 v[204:205], v254 offset:50688\n"
"v_accvgpr_write_b32 a85, v229\n"
"v_mfma_f32_16x16x32_bf16 v[156:159], v[212:215], v[12:15], v[156:159]\n"
"ds_read_b64_tr_b16 v[206:207], v254 offset:50816\n"
"s_waitcnt lgkmcnt(8)\n"
"s_mov_b32 s81, 122880\n"
"v_mfma_f32_16x16x32_bf16 v[156:159], v[216:219], v[20:23], v[156:159]\n"
"ds_read_b64_tr_b16 v[208:209], v254 offset:49952\n"
"buffer_store_dwordx4 a[40:43], v242, s[56:59], s81 offen offset:0 sc1\n"
"v_accvgpr_write_b32 a86, v230\n"
"v_mfma_f32_16x16x32_bf16 v[156:159], v[220:223], v[28:31], v[156:159]\n"
"ds_read_b64_tr_b16 v[210:211], v254 offset:50080\n"
"v_accvgpr_write_b32 a87, v231\n"
"v_mfma_f32_16x16x32_bf16 v[140:143], v[216:219], v[16:19], v[140:143]\n"
"ds_read_b64_tr_b16 v[212:213], v254 offset:50208\n"
"v_mfma_f32_16x16x32_bf16 v[140:143], v[220:223], v[24:27], v[140:143]\n"
"ds_read_b64_tr_b16 v[214:215], v254 offset:50336\n"
"s_waitcnt lgkmcnt(8)\n"
"v_mfma_f32_16x16x32_bf16 v[128:131], v[192:195], v[64:67], v[128:131]\n"
"ds_read_b64_tr_b16 v[216:217], v254 offset:50464\n"
"v_mfma_f32_16x16x32_bf16 v[128:131], v[196:199], v[72:75], v[128:131]\n"
"ds_read_b64_tr_b16 v[218:219], v254 offset:50592\n"
"v_mfma_f32_16x16x32_bf16 v[144:147], v[192:195], v[68:71], v[144:147]\n"
"ds_read_b64_tr_b16 v[220:221], v254 offset:50720\n"
"v_mfma_f32_16x16x32_bf16 v[144:147], v[196:199], v[76:79], v[144:147]\n"
"ds_read_b64_tr_b16 v[222:223], v254 offset:50848\n"
"s_waitcnt lgkmcnt(8)\n"
"buffer_store_dwordx4 a[44:47], v242, s[56:59], s81 offen offset:1024 sc1\n"
"v_mfma_f32_16x16x32_bf16 v[144:147], v[200:203], v[84:87], v[144:147]\n"
"ds_read_b64_tr_b16 v[192:193], v254 offset:49984\n"
"v_cvt_pk_bf16_f32 v232, v176, v177\n"
"v_mfma_f32_16x16x32_bf16 v[144:147], v[204:207], v[92:95], v[144:147]\n"
"ds_read_b64_tr_b16 v[194:195], v254 offset:50112\n"
"v_cvt_pk_bf16_f32 v233, v178, v179\n"
"v_mfma_f32_16x16x32_bf16 v[128:131], v[200:203], v[80:83], v[128:131]\n"
"ds_read_b64_tr_b16 v[196:197], v254 offset:50240\n"
"v_cvt_pk_bf16_f32 v234, v180, v181\n"
"v_mfma_f32_16x16x32_bf16 v[128:131], v[204:207], v[88:91], v[128:131]\n"
"ds_read_b64_tr_b16 v[198:199], v254 offset:50368\n"
"s_waitcnt lgkmcnt(8)\n"
"v_mfma_f32_16x16x32_bf16 v[132:135], v[208:211], v[64:67], v[132:135]\n"
"ds_read_b64_tr_b16 v[200:201], v254 offset:50496\n"
"v_cvt_pk_bf16_f32 v235, v182, v183\n"
"v_mfma_f32_16x16x32_bf16 v[132:135], v[212:215], v[72:75], v[132:135]\n"
"ds_read_b64_tr_b16 v[202:203], v254 offset:50624\n"
"s_mov_b32 s81, 6144\n"
"v_accvgpr_write_b32 a88, v232\n"
"v_mfma_f32_16x16x32_bf16 v[148:151], v[208:211], v[68:71], v[148:151]\n"
"ds_read_b64_tr_b16 v[204:205], v254 offset:50752\n"
"buffer_store_dwordx4 a[48:51], v242, s[64:67], s81 offen offset:0 sc1\n"
"v_accvgpr_write_b32 a89, v233\n"
"v_mfma_f32_16x16x32_bf16 v[148:151], v[212:215], v[76:79], v[148:151]\n"
"ds_read_b64_tr_b16 v[206:207], v254 offset:50880\n"
"s_waitcnt lgkmcnt(8)\n"
"v_mfma_f32_16x16x32_bf16 v[148:151], v[216:219], v[84:87], v[148:151]\n"
"ds_read_b64_tr_b16 v[208:209], v254 offset:50016\n"
"v_accvgpr_write_b32 a90, v234\n"
"v_mfma_f32_16x16x32_bf16 v[148:151], v[220:223], v[92:95], v[148:151]\n"
"ds_read_b64_tr_b16 v[210:211], v254 offset:50144\n"
"v_accvgpr_write_b32 a91, v235\n"
"v_mfma_f32_16x16x32_bf16 v[132:135], v[216:219], v[80:83], v[132:135]\n"
"ds_read_b64_tr_b16 v[212:213], v254 offset:50272\n"
"v_mfma_f32_16x16x32_bf16 v[132:135], v[220:223], v[88:91], v[132:135]\n"
"ds_read_b64_tr_b16 v[214:215], v254 offset:50400\n"
"s_waitcnt lgkmcnt(8)\n"
"v_mfma_f32_16x16x32_bf16 v[136:139], v[192:195], v[64:67], v[136:139]\n"
"ds_read_b64_tr_b16 v[216:217], v254 offset:50528\n"
"s_waitcnt vmcnt(14)\n"
"v_mfma_f32_16x16x32_bf16 v[136:139], v[196:199], v[72:75], v[136:139]\n"
"ds_read_b64_tr_b16 v[218:219], v254 offset:50656\n"
"buffer_store_dwordx4 a[52:55], v242, s[64:67], s81 offen offset:1024 sc1\n"
"v_mfma_f32_16x16x32_bf16 v[152:155], v[192:195], v[68:71], v[152:155]\n"
"ds_read_b64_tr_b16 v[220:221], v254 offset:50784\n"
"s_barrier\n"
"v_mfma_f32_16x16x32_bf16 v[152:155], v[196:199], v[76:79], v[152:155]\n"
"ds_read_b64_tr_b16 v[222:223], v254 offset:50912\n"
"s_waitcnt lgkmcnt(8)\n"
"v_mfma_f32_16x16x32_bf16 v[152:155], v[200:203], v[84:87], v[152:155]\n"
"ds_read_b64_tr_b16 v[192:193], v241 offset:16640\n"
"v_cvt_pk_bf16_f32 v236, v184, v185\n"
"v_mfma_f32_16x16x32_bf16 v[152:155], v[204:207], v[92:95], v[152:155]\n"
"ds_read_b64_tr_b16 v[194:195], v241 offset:16768\n"
"v_cvt_pk_bf16_f32 v237, v186, v187\n"
"v_mfma_f32_16x16x32_bf16 v[136:139], v[200:203], v[80:83], v[136:139]\n"
"ds_read_b64_tr_b16 v[196:197], v241 offset:16896\n"
"v_cvt_pk_bf16_f32 v238, v188, v189\n"
"v_mfma_f32_16x16x32_bf16 v[136:139], v[204:207], v[88:91], v[136:139]\n"
"ds_read_b64_tr_b16 v[198:199], v241 offset:17024\n"
"s_waitcnt lgkmcnt(8)\n"
"s_mov_b32 s81, 124928\n"
"v_mfma_f32_16x16x32_bf16 v[140:143], v[208:211], v[64:67], v[140:143]\n"
"ds_read_b64_tr_b16 v[200:201], v241 offset:17152\n"
"buffer_store_dwordx4 a[56:59], v242, s[56:59], s81 offen offset:0 sc1\n"
"v_cvt_pk_bf16_f32 v239, v190, v191\n"
"v_mfma_f32_16x16x32_bf16 v[140:143], v[212:215], v[72:75], v[140:143]\n"
"ds_read_b64_tr_b16 v[202:203], v241 offset:17280\n"
"v_accvgpr_write_b32 a92, v236\n"
"v_mfma_f32_16x16x32_bf16 v[156:159], v[208:211], v[68:71], v[156:159]\n"
"ds_read_b64_tr_b16 v[204:205], v241 offset:17408\n"
"v_accvgpr_write_b32 a93, v237\n"
"v_mfma_f32_16x16x32_bf16 v[156:159], v[212:215], v[76:79], v[156:159]\n"
"ds_read_b64_tr_b16 v[206:207], v241 offset:17536\n"
"s_waitcnt lgkmcnt(8)\n"
"v_mfma_f32_16x16x32_bf16 v[156:159], v[216:219], v[84:87], v[156:159]\n"
"ds_read_b64_tr_b16 v[208:209], v241 offset:16672\n"
"v_accvgpr_write_b32 a94, v238\n"
"v_mfma_f32_16x16x32_bf16 v[156:159], v[220:223], v[92:95], v[156:159]\n"
"ds_read_b64_tr_b16 v[210:211], v241 offset:16800\n"
"v_accvgpr_write_b32 a95, v239\n"
"v_mfma_f32_16x16x32_bf16 v[140:143], v[216:219], v[80:83], v[140:143]\n"
"ds_read_b64_tr_b16 v[212:213], v241 offset:16928\n"
"v_mfma_f32_16x16x32_bf16 v[140:143], v[220:223], v[88:91], v[140:143]\n"
"ds_read_b64_tr_b16 v[214:215], v241 offset:17056\n"
"s_waitcnt lgkmcnt(8)\n"
"buffer_store_dwordx4 a[60:63], v242, s[56:59], s81 offen offset:1024 sc1\n"
"v_mfma_f32_16x16x32_bf16 v[128:131], v[192:195], v[32:35], v[128:131]\n"
"ds_read_b64_tr_b16 v[216:217], v241 offset:17184\n"
"v_mfma_f32_16x16x32_bf16 v[128:131], v[196:199], v[40:43], v[128:131]\n"
"ds_read_b64_tr_b16 v[218:219], v241 offset:17312\n"
"v_mfma_f32_16x16x32_bf16 v[144:147], v[192:195], v[36:39], v[144:147]\n"
"ds_read_b64_tr_b16 v[220:221], v241 offset:17440\n"
"v_mfma_f32_16x16x32_bf16 v[144:147], v[196:199], v[44:47], v[144:147]\n"
"ds_read_b64_tr_b16 v[222:223], v241 offset:17568\n"
"s_waitcnt lgkmcnt(8)\n"
"v_mfma_f32_16x16x32_bf16 v[144:147], v[200:203], v[52:55], v[144:147]\n"
"ds_read_b64_tr_b16 v[192:193], v241 offset:16704\n"
"v_mfma_f32_16x16x32_bf16 v[144:147], v[204:207], v[60:63], v[144:147]\n"
"ds_read_b64_tr_b16 v[194:195], v241 offset:16832\n"
"s_mov_b32 s81, 8192\n"
"v_mfma_f32_16x16x32_bf16 v[128:131], v[200:203], v[48:51], v[128:131]\n"
"ds_read_b64_tr_b16 v[196:197], v241 offset:16960\n"
"buffer_store_dwordx4 a[64:67], v242, s[64:67], s81 offen offset:0 sc1\n"
"v_mfma_f32_16x16x32_bf16 v[128:131], v[204:207], v[56:59], v[128:131]\n"
"ds_read_b64_tr_b16 v[198:199], v241 offset:17088\n"
"s_waitcnt lgkmcnt(8)\n"
"v_mfma_f32_16x16x32_bf16 v[132:135], v[208:211], v[32:35], v[132:135]\n"
"ds_read_b64_tr_b16 v[200:201], v241 offset:17216\n"
"v_mfma_f32_16x16x32_bf16 v[132:135], v[212:215], v[40:43], v[132:135]\n"
"ds_read_b64_tr_b16 v[202:203], v241 offset:17344\n"
"v_mfma_f32_16x16x32_bf16 v[148:151], v[208:211], v[36:39], v[148:151]\n"
"ds_read_b64_tr_b16 v[204:205], v241 offset:17472\n"
"v_mfma_f32_16x16x32_bf16 v[148:151], v[212:215], v[44:47], v[148:151]\n"
"ds_read_b64_tr_b16 v[206:207], v241 offset:17600\n"
"s_waitcnt lgkmcnt(8)\n"
"v_mfma_f32_16x16x32_bf16 v[148:151], v[216:219], v[52:55], v[148:151]\n"
"ds_read_b64_tr_b16 v[208:209], v241 offset:16736\n"
"v_mfma_f32_16x16x32_bf16 v[148:151], v[220:223], v[60:63], v[148:151]\n"
"ds_read_b64_tr_b16 v[210:211], v241 offset:16864\n"
"buffer_store_dwordx4 a[68:71], v242, s[64:67], s81 offen offset:1024 sc1\n"
"v_mfma_f32_16x16x32_bf16 v[132:135], v[216:219], v[48:51], v[132:135]\n"
"ds_read_b64_tr_b16 v[212:213], v241 offset:16992\n"
"v_mfma_f32_16x16x32_bf16 v[132:135], v[220:223], v[56:59], v[132:135]\n"
"ds_read_b64_tr_b16 v[214:215], v241 offset:17120\n"
"s_waitcnt lgkmcnt(8)\n"
"v_mfma_f32_16x16x32_bf16 v[136:139], v[192:195], v[32:35], v[136:139]\n"
"ds_read_b64_tr_b16 v[216:217], v241 offset:17248\n"
"v_mfma_f32_16x16x32_bf16 v[136:139], v[196:199], v[40:43], v[136:139]\n"
"ds_read_b64_tr_b16 v[218:219], v241 offset:17376\n"
"v_mfma_f32_16x16x32_bf16 v[152:155], v[192:195], v[36:39], v[152:155]\n"
"ds_read_b64_tr_b16 v[220:221], v241 offset:17504\n"
"v_mfma_f32_16x16x32_bf16 v[152:155], v[196:199], v[44:47], v[152:155]\n"
"ds_read_b64_tr_b16 v[222:223], v241 offset:17632\n"
"s_waitcnt lgkmcnt(8)\n"
"s_mov_b32 s81, 126976\n"
"v_mfma_f32_16x16x32_bf16 v[152:155], v[200:203], v[52:55], v[152:155]\n"
"ds_read_b64_tr_b16 v[192:193], v254 offset:16640\n"
"buffer_store_dwordx4 a[72:75], v242, s[56:59], s81 offen offset:0 sc1\n"
"v_mfma_f32_16x16x32_bf16 v[152:155], v[204:207], v[60:63], v[152:155]\n"
"ds_read_b64_tr_b16 v[194:195], v254 offset:16768\n"
"v_mfma_f32_16x16x32_bf16 v[136:139], v[200:203], v[48:51], v[136:139]\n"
"ds_read_b64_tr_b16 v[196:197], v254 offset:16896\n"
"v_mfma_f32_16x16x32_bf16 v[136:139], v[204:207], v[56:59], v[136:139]\n"
"ds_read_b64_tr_b16 v[198:199], v254 offset:17024\n"
"s_waitcnt lgkmcnt(8)\n"
"v_mfma_f32_16x16x32_bf16 v[140:143], v[208:211], v[32:35], v[140:143]\n"
"ds_read_b64_tr_b16 v[200:201], v254 offset:17152\n"
"v_mfma_f32_16x16x32_bf16 v[140:143], v[212:215], v[40:43], v[140:143]\n"
"ds_read_b64_tr_b16 v[202:203], v254 offset:17280\n"
"v_mfma_f32_16x16x32_bf16 v[156:159], v[208:211], v[36:39], v[156:159]\n"
"ds_read_b64_tr_b16 v[204:205], v254 offset:17408\n"
"v_mfma_f32_16x16x32_bf16 v[156:159], v[212:215], v[44:47], v[156:159]\n"
"ds_read_b64_tr_b16 v[206:207], v254 offset:17536\n"
"s_waitcnt lgkmcnt(8)\n"
"buffer_store_dwordx4 a[76:79], v242, s[56:59], s81 offen offset:1024 sc1\n"
"v_mfma_f32_16x16x32_bf16 v[156:159], v[216:219], v[52:55], v[156:159]\n"
"ds_read_b64_tr_b16 v[208:209], v254 offset:16672\n"
"v_mfma_f32_16x16x32_bf16 v[156:159], v[220:223], v[60:63], v[156:159]\n"
"ds_read_b64_tr_b16 v[210:211], v254 offset:16800\n"
"v_mfma_f32_16x16x32_bf16 v[140:143], v[216:219], v[48:51], v[140:143]\n"
"ds_read_b64_tr_b16 v[212:213], v254 offset:16928\n"
"v_mfma_f32_16x16x32_bf16 v[140:143], v[220:223], v[56:59], v[140:143]\n"
"ds_read_b64_tr_b16 v[214:215], v254 offset:17056\n"
"s_waitcnt lgkmcnt(8)\n"
"v_mfma_f32_16x16x32_bf16 v[128:131], v[192:195], v[96:99], v[128:131]\n"
"ds_read_b64_tr_b16 v[216:217], v254 offset:17184\n"
"v_mfma_f32_16x16x32_bf16 v[128:131], v[196:199], v[104:107], v[128:131]\n"
"ds_read_b64_tr_b16 v[218:219], v254 offset:17312\n"
"s_mov_b32 s81, 10240\n"
"v_mfma_f32_16x16x32_bf16 v[144:147], v[192:195], v[100:103], v[144:147]\n"
"ds_read_b64_tr_b16 v[220:221], v254 offset:17440\n"
"buffer_store_dwordx4 a[80:83], v242, s[64:67], s81 offen offset:0 sc1\n"
"v_mfma_f32_16x16x32_bf16 v[144:147], v[196:199], v[108:111], v[144:147]\n"
"ds_read_b64_tr_b16 v[222:223], v254 offset:17568\n"
"s_waitcnt lgkmcnt(8)\n"
"v_mfma_f32_16x16x32_bf16 v[144:147], v[200:203], v[116:119], v[144:147]\n"
"ds_read_b64_tr_b16 v[192:193], v254 offset:16704\n"
"v_mfma_f32_16x16x32_bf16 v[144:147], v[204:207], v[124:127], v[144:147]\n"
"ds_read_b64_tr_b16 v[194:195], v254 offset:16832\n"
"v_mfma_f32_16x16x32_bf16 v[128:131], v[200:203], v[112:115], v[128:131]\n"
"ds_read_b64_tr_b16 v[196:197], v254 offset:16960\n"
"v_mfma_f32_16x16x32_bf16 v[128:131], v[204:207], v[120:123], v[128:131]\n"
"ds_read_b64_tr_b16 v[198:199], v254 offset:17088\n"
"s_waitcnt lgkmcnt(8)\n"
"v_mfma_f32_16x16x32_bf16 v[132:135], v[208:211], v[96:99], v[132:135]\n"
"ds_read_b64_tr_b16 v[200:201], v254 offset:17216\n"
"v_mfma_f32_16x16x32_bf16 v[132:135], v[212:215], v[104:107], v[132:135]\n"
"ds_read_b64_tr_b16 v[202:203], v254 offset:17344\n"
"buffer_store_dwordx4 a[84:87], v242, s[64:67], s81 offen offset:1024 sc1\n"
"v_mfma_f32_16x16x32_bf16 v[148:151], v[208:211], v[100:103], v[148:151]\n"
"ds_read_b64_tr_b16 v[204:205], v254 offset:17472\n"
"v_mfma_f32_16x16x32_bf16 v[148:151], v[212:215], v[108:111], v[148:151]\n"
"ds_read_b64_tr_b16 v[206:207], v254 offset:17600\n"
"s_waitcnt lgkmcnt(8)\n"
"v_mfma_f32_16x16x32_bf16 v[148:151], v[216:219], v[116:119], v[148:151]\n"
"ds_read_b64_tr_b16 v[208:209], v254 offset:16736\n"
"v_mfma_f32_16x16x32_bf16 v[148:151], v[220:223], v[124:127], v[148:151]\n"
"ds_read_b64_tr_b16 v[210:211], v254 offset:16864\n"
"v_mfma_f32_16x16x32_bf16 v[132:135], v[216:219], v[112:115], v[132:135]\n"
"ds_read_b64_tr_b16 v[212:213], v254 offset:16992\n"
"v_mfma_f32_16x16x32_bf16 v[132:135], v[220:223], v[120:123], v[132:135]\n"
"ds_read_b64_tr_b16 v[214:215], v254 offset:17120\n"
"s_waitcnt lgkmcnt(8)\n"
"s_mov_b32 s81, 129024\n"
"v_mfma_f32_16x16x32_bf16 v[136:139], v[192:195], v[96:99], v[136:139]\n"
"ds_read_b64_tr_b16 v[216:217], v254 offset:17248\n"
"buffer_store_dwordx4 a[88:91], v242, s[56:59], s81 offen offset:0 sc1\n"
"v_mfma_f32_16x16x32_bf16 v[136:139], v[196:199], v[104:107], v[136:139]\n"
"ds_read_b64_tr_b16 v[218:219], v254 offset:17376\n"
"v_mfma_f32_16x16x32_bf16 v[152:155], v[192:195], v[100:103], v[152:155]\n"
"ds_read_b64_tr_b16 v[220:221], v254 offset:17504\n"
"v_mfma_f32_16x16x32_bf16 v[152:155], v[196:199], v[108:111], v[152:155]\n"
"ds_read_b64_tr_b16 v[222:223], v254 offset:17632\n"
"s_waitcnt lgkmcnt(8)\n"
"v_mfma_f32_16x16x32_bf16 v[152:155], v[200:203], v[116:119], v[152:155]\n"
"v_mfma_f32_16x16x32_bf16 v[152:155], v[204:207], v[124:127], v[152:155]\n"
"v_mfma_f32_16x16x32_bf16 v[136:139], v[200:203], v[112:115], v[136:139]\n"
"v_mfma_f32_16x16x32_bf16 v[136:139], v[204:207], v[120:123], v[136:139]\n"
"s_waitcnt lgkmcnt(4)\n"
"buffer_store_dwordx4 a[92:95], v242, s[56:59], s81 offen offset:1024 sc1\n"
"v_mfma_f32_16x16x32_bf16 v[140:143], v[208:211], v[96:99], v[140:143]\n"
"v_mfma_f32_16x16x32_bf16 v[140:143], v[212:215], v[104:107], v[140:143]\n"
"v_mfma_f32_16x16x32_bf16 v[156:159], v[208:211], v[100:103], v[156:159]\n"
"v_mfma_f32_16x16x32_bf16 v[156:159], v[212:215], v[108:111], v[156:159]\n"
"s_waitcnt lgkmcnt(0)\n"
"v_mfma_f32_16x16x32_bf16 v[156:159], v[216:219], v[116:119], v[156:159]\n"
"v_mfma_f32_16x16x32_bf16 v[156:159], v[220:223], v[124:127], v[156:159]\n"
"v_mfma_f32_16x16x32_bf16 v[140:143], v[216:219], v[112:115], v[140:143]\n"
"v_mfma_f32_16x16x32_bf16 v[140:143], v[220:223], v[120:123], v[140:143]\n"
"v_cvt_pk_bf16_f32 v224, v128, v129\n"
"v_cvt_pk_bf16_f32 v225, v130, v131\n"
"v_cvt_pk_bf16_f32 v226, v132, v133\n"
"v_cvt_pk_bf16_f32 v227, v134, v135\n"
"s_mov_b32 s81, 12288\n"
"buffer_store_dwordx4 v[224:227], v242, s[64:67], s81 offen offset:0 sc1\n"
"v_cvt_pk_bf16_f32 v228, v136, v137\n"
"v_cvt_pk_bf16_f32 v229, v138, v139\n"
"v_cvt_pk_bf16_f32 v230, v140, v141\n"
"v_cvt_pk_bf16_f32 v231, v142, v143\n"
"buffer_store_dwordx4 v[228:231], v242, s[64:67], s81 offen offset:1024 sc1\n"
"v_cvt_pk_bf16_f32 v232, v144, v145\n"
"v_cvt_pk_bf16_f32 v233, v146, v147\n"
"v_cvt_pk_bf16_f32 v234, v148, v149\n"
"v_cvt_pk_bf16_f32 v235, v150, v151\n"
"s_mov_b32 s81, 131072\n"
"buffer_store_dwordx4 v[232:235], v242, s[56:59], s81 offen offset:0 sc1\n"
"v_cvt_pk_bf16_f32 v236, v152, v153\n"
"v_cvt_pk_bf16_f32 v237, v154, v155\n"
"v_cvt_pk_bf16_f32 v238, v156, v157\n"
"v_cvt_pk_bf16_f32 v239, v158, v159\n"
"buffer_store_dwordx4 v[236:239], v242, s[56:59], s81 offen offset:1024 sc1\n"
"s_lshr_b32 s77, s87, 1\n"
"s_cmp_lg_u32 s77, s83\n"
"s_cbranch_scc1 .Lown2\n"
"s_bitcmp1_b32 s87, 0\n"
"s_cbranch_scc1 .Lown1\n"
"ds_write_b128 v253, a[0:3] offset:0\n"
"ds_write_b128 v253, a[4:7] offset:1024\n"
"ds_write_b128 v253, a[16:19] offset:2048\n"
"ds_write_b128 v253, a[20:23] offset:3072\n"
"ds_write_b128 v253, a[32:35] offset:4096\n"
"ds_write_b128 v253, a[36:39] offset:5120\n"
"ds_write_b128 v253, a[48:51] offset:6144\n"
"ds_write_b128 v253, a[52:55] offset:7168\n"
"ds_write_b128 v253, a[64:67] offset:8192\n"
"ds_write_b128 v253, a[68:71] offset:9216\n"
"ds_write_b128 v253, a[80:83] offset:10240\n"
"ds_write_b128 v253, a[84:87] offset:11264\n"
"ds_write_b128 v253, v[224:227] offset:12288\n"
"ds_write_b128 v253, v[228:231] offset:13312\n"
"s_waitcnt lgkmcnt(0)\n"
"s_branch .Lown2\n"
".Lown1:\n"
"ds_write_b128 v253, a[8:11] offset:0\n"
"ds_write_b128 v253, a[12:15] offset:1024\n"
"ds_write_b128 v253, a[24:27] offset:2048\n"
"ds_write_b128 v253, a[28:31] offset:3072\n"
"ds_write_b128 v253, a[40:43] offset:4096\n"
"ds_write_b128 v253, a[44:47] offset:5120\n"
"ds_write_b128 v253, a[56:59] offset:6144\n"
"ds_write_b128 v253, a[60:63] offset:7168\n"
"ds_write_b128 v253, a[72:75] offset:8192\n"
"ds_write_b128 v253, a[76:79] offset:9216\n"
"ds_write_b128 v253, a[88:91] offset:10240\n"
"ds_write_b128 v253, a[92:95] offset:11264\n"
"ds_write_b128 v253, v[232:235] offset:12288\n"
"ds_write_b128 v253, v[236:239] offset:13312\n"
"s_waitcnt lgkmcnt(0)\n"
".Lown2:\n"
"s_waitcnt vmcnt(12)\n"
"s_barrier\n"
"s_cmp_lg_u32 s83, 0\n"
"s_cbranch_scc1 .Lnsa\n"
"buffer_store_dword v246, v240, s[20:23], 0 offen offset:0 sc1\n"
".Lnsa:\n"
"s_sleep 1\n"
"buffer_load_dword v248, v243, s[36:39], 0 offen offset:0 sc1\n"
"s_waitcnt vmcnt(0)\n"
"s_barrier\n"
"s_cmp_lg_u32 s83, 0\n"
"s_cbranch_scc1 .Lnsb\n"
"buffer_store_dword v246, v244, s[20:23], 0 offen offset:0 sc1\n"
".Lnsb:\n"
"s_cmp_lg_u32 s83, 1\n"
"s_cbranch_scc1 .Lgoa\n"
"s_mov_b32 s80, 0x40000\n"
"s_branch .Lcka\n"
".Lspina:\n"
"buffer_load_dword v248, v243, s[36:39], 0 offen sc1\n"
"s_waitcnt vmcnt(0)\n"
".Lcka:\n"
"v_cmp_ne_u32_e32 vcc, v245, v248\n"
"s_nop 1\n"
"s_and_b32 s77, vcc_lo, 255\n"
"s_cbranch_scc0 .Lgoa\n"
"s_sub_u32 s80, s80, 1\n"
"s_cbranch_scc0 .Lspina\n"
".Lgoa:\n"
"s_barrier\n"
"s_cmp_lg_u32 s83, 1\n"
"s_cbranch_scc1 .Lgob\n"
"s_mov_b32 s80, 0x40000\n"
".Lspinb:\n"
"buffer_load_dword v248, v243, s[36:39], 0 offen sc1\n"
"s_waitcnt vmcnt(0)\n"
".Lckb:\n"
"v_cmp_ne_u32_e32 vcc, v245, v248\n"
"s_nop 1\n"
"s_and_b32 s77, vcc_lo, 65280\n"
"s_cbranch_scc0 .Lgob\n"
"s_sub_u32 s80, s80, 1\n"
"s_cbranch_scc0 .Lspinb\n"
".Lgob:\n"
"ds_read_b128 v[0:3], v252 offset:0\n"
"buffer_load_dwordx4 v[4:7], v242, s[24:27], s45 offen offset:0 sc1\n"
"buffer_load_dwordx4 v[8:11], v242, s[24:27], s46 offen offset:0 sc1\n"
"buffer_load_dwordx4 v[12:15], v242, s[24:27], s47 offen offset:0 sc1\n"
"buffer_load_dwordx4 v[16:19], v242, s[24:27], s48 offen offset:0 sc1\n"
"buffer_load_dwordx4 v[20:23], v242, s[24:27], s49 offen offset:0 sc1\n"
"buffer_load_dwordx4 v[24:27], v242, s[24:27], s50 offen offset:0 sc1\n"
"buffer_load_dwordx4 v[28:31], v242, s[24:27], s51 offen offset:0 sc1\n"
"ds_read_b128 v[32:35], v252 offset:4096\n"
"s_add_u32 s70, s45, 4096\n"
"buffer_load_dwordx4 v[36:39], v242, s[24:27], s70 offen offset:0 sc1\n"
"s_add_u32 s69, s46, 4096\n"
"buffer_load_dwordx4 v[40:43], v242, s[24:27], s69 offen offset:0 sc1\n"
"s_add_u32 s70, s47, 4096\n"
"buffer_load_dwordx4 v[44:47], v242, s[24:27], s70 offen offset:0 sc1\n"
"s_add_u32 s69, s48, 4096\n"
"buffer_load_dwordx4 v[48:51], v242, s[24:27], s69 offen offset:0 sc1\n"
"s_add_u32 s70, s49, 4096\n"
"buffer_load_dwordx4 v[52:55], v242, s[24:27], s70 offen offset:0 sc1\n"
"s_add_u32 s69, s50, 4096\n"
"buffer_load_dwordx4 v[56:59], v242, s[24:27], s69 offen offset:0 sc1\n"
"s_add_u32 s70, s51, 4096\n"
"buffer_load_dwordx4 v[60:63], v242, s[24:27], s70 offen offset:0 sc1\n"
"s_barrier\n"
"buffer_load_dword v240, v243, s[20:23], s35 offen offset:0 sc1\n"
"ds_read_b128 v[64:67], v252 offset:8192\n"
"s_add_u32 s70, s45, 8192\n"
"buffer_load_dwordx4 v[68:71], v242, s[24:27], s70 offen offset:0 sc1\n"
"s_add_u32 s69, s46, 8192\n"
"buffer_load_dwordx4 v[72:75], v242, s[24:27], s69 offen offset:0 sc1\n"
"s_add_u32 s70, s47, 8192\n"
"buffer_load_dwordx4 v[76:79], v242, s[24:27], s70 offen offset:0 sc1\n"
"s_add_u32 s69, s48, 8192\n"
"buffer_load_dwordx4 v[80:83], v242, s[24:27], s69 offen offset:0 sc1\n"
"s_add_u32 s70, s49, 8192\n"
"buffer_load_dwordx4 v[84:87], v242, s[24:27], s70 offen offset:0 sc1\n"
"s_add_u32 s69, s50, 8192\n"
"buffer_load_dwordx4 v[88:91], v242, s[24:27], s69 offen offset:0 sc1\n"
"s_add_u32 s70, s51, 8192\n"
"buffer_load_dwordx4 v[92:95], v242, s[24:27], s70 offen offset:0 sc1\n"
"ds_read_b128 v[96:99], v252 offset:12288\n"
"s_add_u32 s70, s45, s44\n"
"buffer_load_dwordx4 v[100:103], v242, s[24:27], s70 offen offset:0 sc1\n"
"s_add_u32 s69, s46, s44\n"
"buffer_load_dwordx4 v[104:107], v242, s[24:27], s69 offen offset:0 sc1\n"
"s_add_u32 s70, s47, s44\n"
"buffer_load_dwordx4 v[108:111], v242, s[24:27], s70 offen offset:0 sc1\n"
"s_add_u32 s69, s48, s44\n"
"buffer_load_dwordx4 v[112:115], v242, s[24:27], s69 offen offset:0 sc1\n"
"s_add_u32 s70, s49, s44\n"
"buffer_load_dwordx4 v[116:119], v242, s[24:27], s70 offen offset:0 sc1\n"
"s_add_u32 s69, s50, s44\n"
"buffer_load_dwordx4 v[120:123], v242, s[24:27], s69 offen offset:0 sc1\n"
"s_add_u32 s70, s51, s44\n"
"buffer_load_dwordx4 v[124:127], v242, s[24:27], s70 offen offset:0 sc1\n"
"s_waitcnt lgkmcnt(3)\n"
"v_lshlrev_b32_e32 v128, 16, v0\n"
"v_and_b32_e32 v129, 0xffff0000, v0\n"
"v_lshlrev_b32_e32 v130, 16, v1\n"
"v_and_b32_e32 v131, 0xffff0000, v1\n"
"v_lshlrev_b32_e32 v132, 16, v2\n"
"v_and_b32_e32 v133, 0xffff0000, v2\n"
"v_lshlrev_b32_e32 v134, 16, v3\n"
"v_and_b32_e32 v135, 0xffff0000, v3\n"
"s_waitcnt vmcnt(28)\n"
"v_dot2c_f32_bf16_e32 v128, s71, v4\n"
"v_dot2c_f32_bf16_e32 v129, s76, v4\n"
"v_dot2c_f32_bf16_e32 v130, s71, v5\n"
"v_dot2c_f32_bf16_e32 v131, s76, v5\n"
"v_dot2c_f32_bf16_e32 v132, s71, v6\n"
"v_dot2c_f32_bf16_e32 v133, s76, v6\n"
"v_dot2c_f32_bf16_e32 v134, s71, v7\n"
"v_dot2c_f32_bf16_e32 v135, s76, v7\n"
"s_waitcnt vmcnt(27)\n"
"v_dot2c_f32_bf16_e32 v128, s71, v8\n"
"v_dot2c_f32_bf16_e32 v129, s76, v8\n"
"v_dot2c_f32_bf16_e32 v130, s71, v9\n"
"v_dot2c_f32_bf16_e32 v131, s76, v9\n"
"v_dot2c_f32_bf16_e32 v132, s71, v10\n"
"v_dot2c_f32_bf16_e32 v133, s76, v10\n"
"v_dot2c_f32_bf16_e32 v134, s71, v11\n"
"v_dot2c_f32_bf16_e32 v135, s76, v11\n"
"s_waitcnt vmcnt(26)\n"
"v_dot2c_f32_bf16_e32 v128, s71, v12\n"
"v_dot2c_f32_bf16_e32 v129, s76, v12\n"
"v_dot2c_f32_bf16_e32 v130, s71, v13\n"
"v_dot2c_f32_bf16_e32 v131, s76, v13\n"
"v_dot2c_f32_bf16_e32 v132, s71, v14\n"
"v_dot2c_f32_bf16_e32 v133, s76, v14\n"
"v_dot2c_f32_bf16_e32 v134, s71, v15\n"
"v_dot2c_f32_bf16_e32 v135, s76, v15\n"
"s_waitcnt vmcnt(25)\n"
"v_dot2c_f32_bf16_e32 v128, s71, v16\n"
"v_dot2c_f32_bf16_e32 v129, s76, v16\n"
"v_dot2c_f32_bf16_e32 v130, s71, v17\n"
"v_dot2c_f32_bf16_e32 v131, s76, v17\n"
"v_dot2c_f32_bf16_e32 v132, s71, v18\n"
"v_dot2c_f32_bf16_e32 v133, s76, v18\n"
"v_dot2c_f32_bf16_e32 v134, s71, v19\n"
"v_dot2c_f32_bf16_e32 v135, s76, v19\n"
"s_waitcnt vmcnt(24)\n"
"v_dot2c_f32_bf16_e32 v128, s71, v20\n"
"v_dot2c_f32_bf16_e32 v129, s76, v20\n"
"v_dot2c_f32_bf16_e32 v130, s71, v21\n"
"v_dot2c_f32_bf16_e32 v131, s76, v21\n"
"v_dot2c_f32_bf16_e32 v132, s71, v22\n"
"v_dot2c_f32_bf16_e32 v133, s76, v22\n"
"v_dot2c_f32_bf16_e32 v134, s71, v23\n"
"v_dot2c_f32_bf16_e32 v135, s76, v23\n"
"s_waitcnt vmcnt(23)\n"
"v_dot2c_f32_bf16_e32 v128, s71, v24\n"
"v_dot2c_f32_bf16_e32 v129, s76, v24\n"
"v_dot2c_f32_bf16_e32 v130, s71, v25\n"
"s_branch .Lpo14\n"
".Lpg14:\n"
"s_setpc_b64 s[78:79]\n"
".Lpo14:\n"
"v_dot2c_f32_bf16_e32 v131, s76, v25\n"
"v_dot2c_f32_bf16_e32 v132, s71, v26\n"
"v_dot2c_f32_bf16_e32 v133, s76, v26\n"
"v_dot2c_f32_bf16_e32 v134, s71, v27\n"
"v_dot2c_f32_bf16_e32 v135, s76, v27\n"
"s_waitcnt vmcnt(22)\n"
"v_dot2c_f32_bf16_e32 v128, s71, v28\n"
"v_dot2c_f32_bf16_e32 v129, s76, v28\n"
"v_dot2c_f32_bf16_e32 v130, s71, v29\n"
"v_dot2c_f32_bf16_e32 v131, s76, v29\n"
"v_dot2c_f32_bf16_e32 v132, s71, v30\n"
"v_dot2c_f32_bf16_e32 v133, s76, v30\n"
"v_dot2c_f32_bf16_e32 v134, s71, v31\n"
"v_dot2c_f32_bf16_e32 v135, s76, v31\n"
"s_nop 2\n"
"v_cvt_pk_bf16_f32 v160, v128, v129\n"
"v_cvt_pk_bf16_f32 v161, v130, v131\n"
"v_cvt_pk_bf16_f32 v162, v132, v133\n"
"v_cvt_pk_bf16_f32 v163, v134, v135\n"
"s_nop 0\n"
"v_permlane16_swap_b32_e32 v160, v162\n"
"v_permlane16_swap_b32_e32 v161, v163\n"
"s_waitcnt lgkmcnt(2)\n"
"v_lshlrev_b32_e32 v136, 16, v32\n"
"v_and_b32_e32 v137, 0xffff0000, v32\n"
"v_lshlrev_b32_e32 v138, 16, v33\n"
"v_and_b32_e32 v139, 0xffff0000, v33\n"
"v_lshlrev_b32_e32 v140, 16, v34\n"
"v_and_b32_e32 v141, 0xffff0000, v34\n"
"v_lshlrev_b32_e32 v142, 16, v35\n"
"v_and_b32_e32 v143, 0xffff0000, v35\n"
"s_waitcnt vmcnt(21)\n"
"v_dot2c_f32_bf16_e32 v136, s71, v36\n"
"v_dot2c_f32_bf16_e32 v137, s76, v36\n"
"v_dot2c_f32_bf16_e32 v138, s71, v37\n"
"v_dot2c_f32_bf16_e32 v139, s76, v37\n"
"v_dot2c_f32_bf16_e32 v140, s71, v38\n"
"v_dot2c_f32_bf16_e32 v141, s76, v38\n"
"v_dot2c_f32_bf16_e32 v142, s71, v39\n"
"v_dot2c_f32_bf16_e32 v143, s76, v39\n"
"s_waitcnt vmcnt(20)\n"
"v_dot2c_f32_bf16_e32 v136, s71, v40\n"
"v_dot2c_f32_bf16_e32 v137, s76, v40\n"
"v_dot2c_f32_bf16_e32 v138, s71, v41\n"
"v_dot2c_f32_bf16_e32 v139, s76, v41\n"
"v_dot2c_f32_bf16_e32 v140, s71, v42\n"
"v_dot2c_f32_bf16_e32 v141, s76, v42\n"
"v_dot2c_f32_bf16_e32 v142, s71, v43\n"
"v_dot2c_f32_bf16_e32 v143, s76, v43\n"
"s_waitcnt vmcnt(19)\n"
"v_dot2c_f32_bf16_e32 v136, s71, v44\n"
"v_dot2c_f32_bf16_e32 v137, s76, v44\n"
"v_dot2c_f32_bf16_e32 v138, s71, v45\n"
"v_dot2c_f32_bf16_e32 v139, s76, v45\n"
"v_dot2c_f32_bf16_e32 v140, s71, v46\n"
"v_dot2c_f32_bf16_e32 v141, s76, v46\n"
"v_dot2c_f32_bf16_e32 v142, s71, v47\n"
"v_dot2c_f32_bf16_e32 v143, s76, v47\n"
"s_waitcnt vmcnt(18)\n"
"v_dot2c_f32_bf16_e32 v136, s71, v48\n"
"v_dot2c_f32_bf16_e32 v137, s76, v48\n"
"v_dot2c_f32_bf16_e32 v138, s71, v49\n"
"v_dot2c_f32_bf16_e32 v139, s76, v49\n"
"v_dot2c_f32_bf16_e32 v140, s71, v50\n"
"v_dot2c_f32_bf16_e32 v141, s76, v50\n"
"v_dot2c_f32_bf16_e32 v142, s71, v51\n"
"v_dot2c_f32_bf16_e32 v143, s76, v51\n"
"s_waitcnt vmcnt(17)\n"
"v_dot2c_f32_bf16_e32 v136, s71, v52\n"
"v_dot2c_f32_bf16_e32 v137, s76, v52\n"
"v_dot2c_f32_bf16_e32 v138, s71, v53\n"
"v_dot2c_f32_bf16_e32 v139, s76, v53\n"
"v_dot2c_f32_bf16_e32 v140, s71, v54\n"
"v_dot2c_f32_bf16_e32 v141, s76, v54\n"
"v_dot2c_f32_bf16_e32 v142, s71, v55\n"
"v_dot2c_f32_bf16_e32 v143, s76, v55\n"
"s_waitcnt vmcnt(16)\n"
"v_dot2c_f32_bf16_e32 v136, s71, v56\n"
"v_dot2c_f32_bf16_e32 v137, s76, v56\n"
"v_dot2c_f32_bf16_e32 v138, s71, v57\n"
"v_dot2c_f32_bf16_e32 v139, s76, v57\n"
"v_dot2c_f32_bf16_e32 v140, s71, v58\n"
"v_dot2c_f32_bf16_e32 v141, s76, v58\n"
"v_dot2c_f32_bf16_e32 v142, s71, v59\n"
"v_dot2c_f32_bf16_e32 v143, s76, v59\n"
"s_waitcnt vmcnt(15)\n"
"v_dot2c_f32_bf16_e32 v136, s71, v60\n"
"v_dot2c_f32_bf16_e32 v137, s76, v60\n"
"v_dot2c_f32_bf16_e32 v138, s71, v61\n"
"v_dot2c_f32_bf16_e32 v139, s76, v61\n"
"v_dot2c_f32_bf16_e32 v140, s71, v62\n"
"v_dot2c_f32_bf16_e32 v141, s76, v62\n"
"v_dot2c_f32_bf16_e32 v142, s71, v63\n"
"v_dot2c_f32_bf16_e32 v143, s76, v63\n"
"s_nop 2\n"
"v_cvt_pk_bf16_f32 v164, v136, v137\n"
"v_cvt_pk_bf16_f32 v165, v138, v139\n"
"v_cvt_pk_bf16_f32 v166, v140, v141\n"
"v_cvt_pk_bf16_f32 v167, v142, v143\n"
"s_nop 0\n"
"v_permlane16_swap_b32_e32 v164, v166\n"
"v_permlane16_swap_b32_e32 v165, v167\n"
"buffer_store_dwordx4 v[160:163], v247, s[28:31], s90 offen offset:0 sc1\n"
"buffer_store_dwordx4 v[164:167], v247, s[28:31], s90 offen offset:256 sc1\n"
"s_waitcnt lgkmcnt(1)\n"
"v_lshlrev_b32_e32 v144, 16, v64\n"
"v_and_b32_e32 v145, 0xffff0000, v64\n"
"v_lshlrev_b32_e32 v146, 16, v65\n"
"v_and_b32_e32 v147, 0xffff0000, v65\n"
"v_lshlrev_b32_e32 v148, 16, v66\n"
"v_and_b32_e32 v149, 0xffff0000, v66\n"
"v_lshlrev_b32_e32 v150, 16, v67\n"
"v_and_b32_e32 v151, 0xffff0000, v67\n"
"s_waitcnt vmcnt(15)\n"
"v_dot2c_f32_bf16_e32 v144, s71, v68\n"
"v_dot2c_f32_bf16_e32 v145, s76, v68\n"
"v_dot2c_f32_bf16_e32 v146, s71, v69\n"
"v_dot2c_f32_bf16_e32 v147, s76, v69\n"
"v_dot2c_f32_bf16_e32 v148, s71, v70\n"
"v_dot2c_f32_bf16_e32 v149, s76, v70\n"
"v_dot2c_f32_bf16_e32 v150, s71, v71\n"
"v_dot2c_f32_bf16_e32 v151, s76, v71\n"
"s_waitcnt vmcnt(14)\n"
"v_dot2c_f32_bf16_e32 v144, s71, v72\n"
"v_dot2c_f32_bf16_e32 v145, s76, v72\n"
"v_dot2c_f32_bf16_e32 v146, s71, v73\n"
"v_dot2c_f32_bf16_e32 v147, s76, v73\n"
"v_dot2c_f32_bf16_e32 v148, s71, v74\n"
"v_dot2c_f32_bf16_e32 v149, s76, v74\n"
"v_dot2c_f32_bf16_e32 v150, s71, v75\n"
"v_dot2c_f32_bf16_e32 v151, s76, v75\n"
"s_waitcnt vmcnt(13)\n"
"v_dot2c_f32_bf16_e32 v144, s71, v76\n"
"v_dot2c_f32_bf16_e32 v145, s76, v76\n"
"v_dot2c_f32_bf16_e32 v146, s71, v77\n"
"v_dot2c_f32_bf16_e32 v147, s76, v77\n"
"v_dot2c_f32_bf16_e32 v148, s71, v78\n"
"v_dot2c_f32_bf16_e32 v149, s76, v78\n"
"v_dot2c_f32_bf16_e32 v150, s71, v79\n"
"v_dot2c_f32_bf16_e32 v151, s76, v79\n"
"s_waitcnt vmcnt(12)\n"
"v_dot2c_f32_bf16_e32 v144, s71, v80\n"
"v_dot2c_f32_bf16_e32 v145, s76, v80\n"
"v_dot2c_f32_bf16_e32 v146, s71, v81\n"
"v_dot2c_f32_bf16_e32 v147, s76, v81\n"
"v_dot2c_f32_bf16_e32 v148, s71, v82\n"
"v_dot2c_f32_bf16_e32 v149, s76, v82\n"
"v_dot2c_f32_bf16_e32 v150, s71, v83\n"
"v_dot2c_f32_bf16_e32 v151, s76, v83\n"
"s_waitcnt vmcnt(11)\n"
"v_dot2c_f32_bf16_e32 v144, s71, v84\n"
"v_dot2c_f32_bf16_e32 v145, s76, v84\n"
"v_dot2c_f32_bf16_e32 v146, s71, v85\n"
"v_dot2c_f32_bf16_e32 v147, s76, v85\n"
"v_dot2c_f32_bf16_e32 v148, s71, v86\n"
"v_dot2c_f32_bf16_e32 v149, s76, v86\n"
"v_dot2c_f32_bf16_e32 v150, s71, v87\n"
"v_dot2c_f32_bf16_e32 v151, s76, v87\n"
"s_waitcnt vmcnt(10)\n"
"v_dot2c_f32_bf16_e32 v144, s71, v88\n"
"v_dot2c_f32_bf16_e32 v145, s76, v88\n"
"v_dot2c_f32_bf16_e32 v146, s71, v89\n"
"v_dot2c_f32_bf16_e32 v147, s76, v89\n"
"v_dot2c_f32_bf16_e32 v148, s71, v90\n"
"v_dot2c_f32_bf16_e32 v149, s76, v90\n"
"v_dot2c_f32_bf16_e32 v150, s71, v91\n"
"v_dot2c_f32_bf16_e32 v151, s76, v91\n"
"s_waitcnt vmcnt(9)\n"
"v_dot2c_f32_bf16_e32 v144, s71, v92\n"
"v_dot2c_f32_bf16_e32 v145, s76, v92\n"
"v_dot2c_f32_bf16_e32 v146, s71, v93\n"
"v_dot2c_f32_bf16_e32 v147, s76, v93\n"
"v_dot2c_f32_bf16_e32 v148, s71, v94\n"
"v_dot2c_f32_bf16_e32 v149, s76, v94\n"
"v_dot2c_f32_bf16_e32 v150, s71, v95\n"
"v_dot2c_f32_bf16_e32 v151, s76, v95\n"
"s_nop 2\n"
"v_cvt_pk_bf16_f32 v168, v144, v145\n"
"v_cvt_pk_bf16_f32 v169, v146, v147\n"
"v_cvt_pk_bf16_f32 v170, v148, v149\n"
"v_cvt_pk_bf16_f32 v171, v150, v151\n"
"s_nop 0\n"
"v_permlane16_swap_b32_e32 v168, v170\n"
"v_permlane16_swap_b32_e32 v169, v171\n"
"buffer_store_dwordx4 v[168:171], v247, s[28:31], s90 offen offset:512 sc1\n"
"s_waitcnt lgkmcnt(0)\n"
"v_lshlrev_b32_e32 v152, 16, v96\n"
"v_and_b32_e32 v153, 0xffff0000, v96\n"
"v_lshlrev_b32_e32 v154, 16, v97\n"
"v_and_b32_e32 v155, 0xffff0000, v97\n"
"v_lshlrev_b32_e32 v156, 16, v98\n"
"v_and_b32_e32 v157, 0xffff0000, v98\n"
"v_lshlrev_b32_e32 v158, 16, v99\n"
"v_and_b32_e32 v159, 0xffff0000, v99\n"
"s_waitcnt vmcnt(9)\n"
"v_dot2c_f32_bf16_e32 v152, s71, v100\n"
"v_dot2c_f32_bf16_e32 v153, s76, v100\n"
"v_dot2c_f32_bf16_e32 v154, s71, v101\n"
"v_dot2c_f32_bf16_e32 v155, s76, v101\n"
"v_dot2c_f32_bf16_e32 v156, s71, v102\n"
"v_dot2c_f32_bf16_e32 v157, s76, v102\n"
"v_dot2c_f32_bf16_e32 v158, s71, v103\n"
"v_dot2c_f32_bf16_e32 v159, s76, v103\n"
"s_waitcnt vmcnt(8)\n"
"v_dot2c_f32_bf16_e32 v152, s71, v104\n"
"v_dot2c_f32_bf16_e32 v153, s76, v104\n"
"v_dot2c_f32_bf16_e32 v154, s71, v105\n"
"v_dot2c_f32_bf16_e32 v155, s76, v105\n"
"v_dot2c_f32_bf16_e32 v156, s71, v106\n"
"v_dot2c_f32_bf16_e32 v157, s76, v106\n"
"v_dot2c_f32_bf16_e32 v158, s71, v107\n"
"v_dot2c_f32_bf16_e32 v159, s76, v107\n"
"s_waitcnt vmcnt(7)\n"
"v_dot2c_f32_bf16_e32 v152, s71, v108\n"
"v_dot2c_f32_bf16_e32 v153, s76, v108\n"
"v_dot2c_f32_bf16_e32 v154, s71, v109\n"
"v_dot2c_f32_bf16_e32 v155, s76, v109\n"
"v_dot2c_f32_bf16_e32 v156, s71, v110\n"
"v_dot2c_f32_bf16_e32 v157, s76, v110\n"
"v_dot2c_f32_bf16_e32 v158, s71, v111\n"
"v_dot2c_f32_bf16_e32 v159, s76, v111\n"
"s_waitcnt vmcnt(6)\n"
"v_dot2c_f32_bf16_e32 v152, s71, v112\n"
"v_dot2c_f32_bf16_e32 v153, s76, v112\n"
"v_dot2c_f32_bf16_e32 v154, s71, v113\n"
"v_dot2c_f32_bf16_e32 v155, s76, v113\n"
"v_dot2c_f32_bf16_e32 v156, s71, v114\n"
"v_dot2c_f32_bf16_e32 v157, s76, v114\n"
"v_dot2c_f32_bf16_e32 v158, s71, v115\n"
"v_dot2c_f32_bf16_e32 v159, s76, v115\n"
"s_waitcnt vmcnt(5)\n"
"v_dot2c_f32_bf16_e32 v152, s71, v116\n"
"v_dot2c_f32_bf16_e32 v153, s76, v116\n"
"v_dot2c_f32_bf16_e32 v154, s71, v117\n"
"v_dot2c_f32_bf16_e32 v155, s76, v117\n"
"v_dot2c_f32_bf16_e32 v156, s71, v118\n"
"v_dot2c_f32_bf16_e32 v157, s76, v118\n"
"v_dot2c_f32_bf16_e32 v158, s71, v119\n"
"v_dot2c_f32_bf16_e32 v159, s76, v119\n"
"s_waitcnt vmcnt(4)\n"
"v_dot2c_f32_bf16_e32 v152, s71, v120\n"
"v_dot2c_f32_bf16_e32 v153, s76, v120\n"
"v_dot2c_f32_bf16_e32 v154, s71, v121\n"
"v_dot2c_f32_bf16_e32 v155, s76, v121\n"
"v_dot2c_f32_bf16_e32 v156, s71, v122\n"
"v_dot2c_f32_bf16_e32 v157, s76, v122\n"
"v_dot2c_f32_bf16_e32 v158, s71, v123\n"
"v_dot2c_f32_bf16_e32 v159, s76, v123\n"
"s_waitcnt vmcnt(3)\n"
"v_dot2c_f32_bf16_e32 v152, s71, v124\n"
"v_dot2c_f32_bf16_e32 v153, s76, v124\n"
"v_dot2c_f32_bf16_e32 v154, s71, v125\n"
"v_dot2c_f32_bf16_e32 v155, s76, v125\n"
"v_dot2c_f32_bf16_e32 v156, s71, v126\n"
"v_dot2c_f32_bf16_e32 v157, s76, v126\n"
"v_dot2c_f32_bf16_e32 v158, s71, v127\n"
"v_dot2c_f32_bf16_e32 v159, s76, v127\n"
"s_nop 2\n"
"v_cvt_pk_bf16_f32 v172, v152, v153\n"
"v_cvt_pk_bf16_f32 v173, v154, v155\n"
"v_cvt_pk_bf16_f32 v174, v156, v157\n"
"v_cvt_pk_bf16_f32 v175, v158, v159\n"
"s_nop 0\n"
"v_permlane16_swap_b32_e32 v172, v174\n"
"v_permlane16_swap_b32_e32 v173, v175\n"
"s_add_u32 s90, s90, s34\n"
"buffer_store_dwordx4 v[172:175], v247, s[28:31], s90 offen offset:768 sc1\n"
"buffer_store_dword v240, v244, s[20:23], 0 offen offset:0 sc1\n"
"s_endpgm\n"
".Lst1_16:\n"
"s_call_b64 s[78:79], .Lslow1_1\n"
"s_branch .Lrt1_16\n"
".Lst2_17:\n"
"s_call_b64 s[78:79], .Lslow2\n"
"s_branch .Lrt2_17\n"
".Lst1_L0:\n"
"s_call_b64 s[78:79], .Lslow1_0\n"
"s_branch .Lrt1_L0\n"
".Lst2_L1:\n"
"s_call_b64 s[78:79], .Lslow2\n"
"s_branch .Lrt2_L1\n"
".Lst1_L2:\n"
"s_call_b64 s[78:79], .Lslow1_1\n"
"s_branch .Lrt1_L2\n"
".Lst2_L3:\n"
"s_call_b64 s[78:79], .Lslow2\n"
"s_branch .Lrt2_L3\n"
".Lst1_42:\n"
"s_call_b64 s[78:79], .Lslow1_0\n"
"s_branch .Lrt1_42\n"
".Lst2_43:\n"
"s_call_b64 s[78:79], .Lslow2\n"
"s_branch .Lrt2_43\n"
".Lst1_44:\n"
"s_call_b64 s[78:79], .Lslow1_1\n"
"s_branch .Lrt1_44\n"
".Lst2_45:\n"
"s_call_b64 s[78:79], .Lslow2\n"
"s_branch .Lrt2_45\n"
".Lslow1_0:\n"
"s_nop 7\n"
"v_max3_f32 v248, v128, v129, v130\n"
"v_max3_f32 v248, v248, v131, v144\n"
"v_max3_f32 v248, v248, v145, v146\n"
"v_max3_f32 v248, v248, v147, 0\n"
"v_mov_b32_e32 v249, v248\n"
"s_nop 1\n"
"v_permlane32_swap_b32_e32 v248, v249\n"
"v_max_f32_e32 v248, v248, v249\n"
"v_mov_b32_e32 v249, v248\n"
"s_nop 1\n"
"v_permlane16_swap_b32_e32 v248, v249\n"
"v_max_f32_e32 v248, v248, v249\n"
"v_sub_f32_e32 v128, v128, v248\n"
"v_sub_f32_e32 v129, v129, v248\n"
"v_sub_f32_e32 v130, v130, v248\n"
"v_sub_f32_e32 v131, v131, v248\n"
"v_sub_f32_e32 v144, v144, v248\n"
"v_sub_f32_e32 v145, v145, v248\n"
"v_sub_f32_e32 v146, v146, v248\n"
"v_sub_f32_e32 v147, v147, v248\n"
"v_sub_f32_e32 v208, v208, v248\n"
"v_sub_f32_e32 v209, v209, v248\n"
"v_sub_f32_e32 v210, v210, v248\n"
"v_sub_f32_e32 v211, v211, v248\n"
"v_exp_f32_e64 v248, -v248\n"
"s_nop 0\n"
"v_mul_f32_e32 v243, v248, v243\n"
"ds_write_b32 v251, v248 offset:0\n"
"s_waitcnt lgkmcnt(0)\n"
"v_max3_f32 v248, v132, v133, v134\n"
"v_max3_f32 v248, v248, v135, v148\n"
"v_max3_f32 v248, v248, v149, v150\n"
"v_max3_f32 v248, v248, v151, 0\n"
"v_mov_b32_e32 v249, v248\n"
"s_nop 1\n"
"v_permlane32_swap_b32_e32 v248, v249\n"
"v_max_f32_e32 v248, v248, v249\n"
"v_mov_b32_e32 v249, v248\n"
"s_nop 1\n"
"v_permlane16_swap_b32_e32 v248, v249\n"
"v_max_f32_e32 v248, v248, v249\n"
"v_sub_f32_e32 v132, v132, v248\n"
"v_sub_f32_e32 v133, v133, v248\n"
"v_sub_f32_e32 v134, v134, v248\n"
"v_sub_f32_e32 v135, v135, v248\n"
"v_sub_f32_e32 v148, v148, v248\n"
"v_sub_f32_e32 v149, v149, v248\n"
"v_sub_f32_e32 v150, v150, v248\n"
"v_sub_f32_e32 v151, v151, v248\n"
"v_sub_f32_e32 v212, v212, v248\n"
"v_sub_f32_e32 v213, v213, v248\n"
"v_sub_f32_e32 v214, v214, v248\n"
"v_sub_f32_e32 v215, v215, v248\n"
"v_exp_f32_e64 v248, -v248\n"
"s_nop 0\n"
"v_mul_f32_e32 v244, v248, v244\n"
"ds_write_b32 v251, v248 offset:256\n"
"s_waitcnt lgkmcnt(0)\n"
"v_max3_f32 v248, v136, v137, v138\n"
"v_max3_f32 v248, v248, v139, v152\n"
"v_max3_f32 v248, v248, v153, v154\n"
"v_max3_f32 v248, v248, v155, 0\n"
"v_mov_b32_e32 v249, v248\n"
"s_nop 1\n"
"v_permlane32_swap_b32_e32 v248, v249\n"
"v_max_f32_e32 v248, v248, v249\n"
"v_mov_b32_e32 v249, v248\n"
"s_nop 1\n"
"v_permlane16_swap_b32_e32 v248, v249\n"
"v_max_f32_e32 v248, v248, v249\n"
"v_sub_f32_e32 v136, v136, v248\n"
"v_sub_f32_e32 v137, v137, v248\n"
"v_sub_f32_e32 v138, v138, v248\n"
"v_sub_f32_e32 v139, v139, v248\n"
"v_sub_f32_e32 v152, v152, v248\n"
"v_sub_f32_e32 v153, v153, v248\n"
"v_sub_f32_e32 v154, v154, v248\n"
"v_sub_f32_e32 v155, v155, v248\n"
"v_sub_f32_e32 v216, v216, v248\n"
"v_sub_f32_e32 v217, v217, v248\n"
"v_sub_f32_e32 v218, v218, v248\n"
"v_sub_f32_e32 v219, v219, v248\n"
"v_exp_f32_e64 v248, -v248\n"
"s_nop 0\n"
"v_mul_f32_e32 v245, v248, v245\n"
"ds_write_b32 v251, v248 offset:512\n"
"s_waitcnt lgkmcnt(0)\n"
"v_max3_f32 v248, v140, v141, v142\n"
"v_max3_f32 v248, v248, v143, v156\n"
"v_max3_f32 v248, v248, v157, v158\n"
"v_max3_f32 v248, v248, v159, 0\n"
"v_mov_b32_e32 v249, v248\n"
"s_nop 1\n"
"v_permlane32_swap_b32_e32 v248, v249\n"
"v_max_f32_e32 v248, v248, v249\n"
"v_mov_b32_e32 v249, v248\n"
"s_nop 1\n"
"v_permlane16_swap_b32_e32 v248, v249\n"
"v_max_f32_e32 v248, v248, v249\n"
"v_sub_f32_e32 v140, v140, v248\n"
"v_sub_f32_e32 v141, v141, v248\n"
"v_sub_f32_e32 v142, v142, v248\n"
"v_sub_f32_e32 v143, v143, v248\n"
"v_sub_f32_e32 v156, v156, v248\n"
"v_sub_f32_e32 v157, v157, v248\n"
"v_sub_f32_e32 v158, v158, v248\n"
"v_sub_f32_e32 v159, v159, v248\n"
"v_sub_f32_e32 v220, v220, v248\n"
"v_sub_f32_e32 v221, v221, v248\n"
"v_sub_f32_e32 v222, v222, v248\n"
"v_sub_f32_e32 v223, v223, v248\n"
"v_exp_f32_e64 v248, -v248\n"
"s_nop 0\n"
"v_mul_f32_e32 v246, v248, v246\n"
"ds_write_b32 v251, v248 offset:768\n"
"s_waitcnt lgkmcnt(0)\n"
"s_nop 1\n"
"s_setpc_b64 s[78:79]\n"
".Lslow1_1:\n"
"s_nop 7\n"
"v_max3_f32 v248, v160, v161, v162\n"
"v_max3_f32 v248, v248, v163, v176\n"
"v_max3_f32 v248, v248, v177, v178\n"
"v_max3_f32 v248, v248, v179, 0\n"
"v_mov_b32_e32 v249, v248\n"
"s_nop 1\n"
"v_permlane32_swap_b32_e32 v248, v249\n"
"v_max_f32_e32 v248, v248, v249\n"
"v_mov_b32_e32 v249, v248\n"
"s_nop 1\n"
"v_permlane16_swap_b32_e32 v248, v249\n"
"v_max_f32_e32 v248, v248, v249\n"
"v_sub_f32_e32 v160, v160, v248\n"
"v_sub_f32_e32 v161, v161, v248\n"
"v_sub_f32_e32 v162, v162, v248\n"
"v_sub_f32_e32 v163, v163, v248\n"
"v_sub_f32_e32 v176, v176, v248\n"
"v_sub_f32_e32 v177, v177, v248\n"
"v_sub_f32_e32 v178, v178, v248\n"
"v_sub_f32_e32 v179, v179, v248\n"
"v_sub_f32_e32 v208, v208, v248\n"
"v_sub_f32_e32 v209, v209, v248\n"
"v_sub_f32_e32 v210, v210, v248\n"
"v_sub_f32_e32 v211, v211, v248\n"
"v_exp_f32_e64 v248, -v248\n"
"s_nop 0\n"
"v_mul_f32_e32 v243, v248, v243\n"
"ds_write_b32 v251, v248 offset:0\n"
"s_waitcnt lgkmcnt(0)\n"
"v_max3_f32 v248, v164, v165, v166\n"
"v_max3_f32 v248, v248, v167, v180\n"
"v_max3_f32 v248, v248, v181, v182\n"
"v_max3_f32 v248, v248, v183, 0\n"
"v_mov_b32_e32 v249, v248\n"
"s_nop 1\n"
"v_permlane32_swap_b32_e32 v248, v249\n"
"v_max_f32_e32 v248, v248, v249\n"
"v_mov_b32_e32 v249, v248\n"
"s_nop 1\n"
"v_permlane16_swap_b32_e32 v248, v249\n"
"v_max_f32_e32 v248, v248, v249\n"
"v_sub_f32_e32 v164, v164, v248\n"
"v_sub_f32_e32 v165, v165, v248\n"
"v_sub_f32_e32 v166, v166, v248\n"
"v_sub_f32_e32 v167, v167, v248\n"
"v_sub_f32_e32 v180, v180, v248\n"
"v_sub_f32_e32 v181, v181, v248\n"
"v_sub_f32_e32 v182, v182, v248\n"
"v_sub_f32_e32 v183, v183, v248\n"
"v_sub_f32_e32 v212, v212, v248\n"
"v_sub_f32_e32 v213, v213, v248\n"
"v_sub_f32_e32 v214, v214, v248\n"
"v_sub_f32_e32 v215, v215, v248\n"
"v_exp_f32_e64 v248, -v248\n"
"s_nop 0\n"
"v_mul_f32_e32 v244, v248, v244\n"
"ds_write_b32 v251, v248 offset:256\n"
"s_waitcnt lgkmcnt(0)\n"
"v_max3_f32 v248, v168, v169, v170\n"
"v_max3_f32 v248, v248, v171, v184\n"
"v_max3_f32 v248, v248, v185, v186\n"
"v_max3_f32 v248, v248, v187, 0\n"
"v_mov_b32_e32 v249, v248\n"
"s_nop 1\n"
"v_permlane32_swap_b32_e32 v248, v249\n"
"v_max_f32_e32 v248, v248, v249\n"
"v_mov_b32_e32 v249, v248\n"
"s_nop 1\n"
"v_permlane16_swap_b32_e32 v248, v249\n"
"v_max_f32_e32 v248, v248, v249\n"
"v_sub_f32_e32 v168, v168, v248\n"
"v_sub_f32_e32 v169, v169, v248\n"
"v_sub_f32_e32 v170, v170, v248\n"
"v_sub_f32_e32 v171, v171, v248\n"
"v_sub_f32_e32 v184, v184, v248\n"
"v_sub_f32_e32 v185, v185, v248\n"
"v_sub_f32_e32 v186, v186, v248\n"
"v_sub_f32_e32 v187, v187, v248\n"
"v_sub_f32_e32 v216, v216, v248\n"
"v_sub_f32_e32 v217, v217, v248\n"
"v_sub_f32_e32 v218, v218, v248\n"
"v_sub_f32_e32 v219, v219, v248\n"
"v_exp_f32_e64 v248, -v248\n"
"s_nop 0\n"
"v_mul_f32_e32 v245, v248, v245\n"
"ds_write_b32 v251, v248 offset:512\n"
"s_waitcnt lgkmcnt(0)\n"
"v_max3_f32 v248, v172, v173, v174\n"
"v_max3_f32 v248, v248, v175, v188\n"
"v_max3_f32 v248, v248, v189, v190\n"
"v_max3_f32 v248, v248, v191, 0\n"
"v_mov_b32_e32 v249, v248\n"
"s_nop 1\n"
"v_permlane32_swap_b32_e32 v248, v249\n"
"v_max_f32_e32 v248, v248, v249\n"
"v_mov_b32_e32 v249, v248\n"
"s_nop 1\n"
"v_permlane16_swap_b32_e32 v248, v249\n"
"v_max_f32_e32 v248, v248, v249\n"
"v_sub_f32_e32 v172, v172, v248\n"
"v_sub_f32_e32 v173, v173, v248\n"
"v_sub_f32_e32 v174, v174, v248\n"
"v_sub_f32_e32 v175, v175, v248\n"
"v_sub_f32_e32 v188, v188, v248\n"
"v_sub_f32_e32 v189, v189, v248\n"
"v_sub_f32_e32 v190, v190, v248\n"
"v_sub_f32_e32 v191, v191, v248\n"
"v_sub_f32_e32 v220, v220, v248\n"
"v_sub_f32_e32 v221, v221, v248\n"
"v_sub_f32_e32 v222, v222, v248\n"
"v_sub_f32_e32 v223, v223, v248\n"
"v_exp_f32_e64 v248, -v248\n"
"s_nop 0\n"
"v_mul_f32_e32 v246, v248, v246\n"
"ds_write_b32 v251, v248 offset:768\n"
"s_waitcnt lgkmcnt(0)\n"
"s_nop 1\n"
"s_setpc_b64 s[78:79]\n"
".Lslow2:\n"
"s_nop 7\n"
"s_nop 7\n"
"ds_read_b32 v250, v251 offset:0\n"
"s_waitcnt lgkmcnt(0)\n"
"v_accvgpr_read_b32 v248, a0\n"
"v_mul_f32_e32 v248, v250, v248\n"
"v_accvgpr_write_b32 a0, v248\n"
"v_accvgpr_read_b32 v249, a1\n"
"v_mul_f32_e32 v249, v250, v249\n"
"v_accvgpr_write_b32 a1, v249\n"
"v_accvgpr_read_b32 v248, a2\n"
"v_mul_f32_e32 v248, v250, v248\n"
"v_accvgpr_write_b32 a2, v248\n"
"v_accvgpr_read_b32 v249, a3\n"
"v_mul_f32_e32 v249, v250, v249\n"
"v_accvgpr_write_b32 a3, v249\n"
"v_accvgpr_read_b32 v248, a16\n"
"v_mul_f32_e32 v248, v250, v248\n"
"v_accvgpr_write_b32 a16, v248\n"
"v_accvgpr_read_b32 v249, a17\n"
"v_mul_f32_e32 v249, v250, v249\n"
"v_accvgpr_write_b32 a17, v249\n"
"v_accvgpr_read_b32 v248, a18\n"
"v_mul_f32_e32 v248, v250, v248\n"
"v_accvgpr_write_b32 a18, v248\n"
"v_accvgpr_read_b32 v249, a19\n"
"v_mul_f32_e32 v249, v250, v249\n"
"v_accvgpr_write_b32 a19, v249\n"
"v_accvgpr_read_b32 v248, a32\n"
"v_mul_f32_e32 v248, v250, v248\n"
"v_accvgpr_write_b32 a32, v248\n"
"v_accvgpr_read_b32 v249, a33\n"
"v_mul_f32_e32 v249, v250, v249\n"
"v_accvgpr_write_b32 a33, v249\n"
"v_accvgpr_read_b32 v248, a34\n"
"v_mul_f32_e32 v248, v250, v248\n"
"v_accvgpr_write_b32 a34, v248\n"
"v_accvgpr_read_b32 v249, a35\n"
"v_mul_f32_e32 v249, v250, v249\n"
"v_accvgpr_write_b32 a35, v249\n"
"v_accvgpr_read_b32 v248, a48\n"
"v_mul_f32_e32 v248, v250, v248\n"
"v_accvgpr_write_b32 a48, v248\n"
"v_accvgpr_read_b32 v249, a49\n"
"v_mul_f32_e32 v249, v250, v249\n"
"v_accvgpr_write_b32 a49, v249\n"
"v_accvgpr_read_b32 v248, a50\n"
"v_mul_f32_e32 v248, v250, v248\n"
"v_accvgpr_write_b32 a50, v248\n"
"v_accvgpr_read_b32 v249, a51\n"
"v_mul_f32_e32 v249, v250, v249\n"
"v_accvgpr_write_b32 a51, v249\n"
"v_accvgpr_read_b32 v248, a64\n"
"v_mul_f32_e32 v248, v250, v248\n"
"v_accvgpr_write_b32 a64, v248\n"
"v_accvgpr_read_b32 v249, a65\n"
"v_mul_f32_e32 v249, v250, v249\n"
"v_accvgpr_write_b32 a65, v249\n"
"v_accvgpr_read_b32 v248, a66\n"
"v_mul_f32_e32 v248, v250, v248\n"
"v_accvgpr_write_b32 a66, v248\n"
"v_accvgpr_read_b32 v249, a67\n"
"v_mul_f32_e32 v249, v250, v249\n"
"v_accvgpr_write_b32 a67, v249\n"
"v_accvgpr_read_b32 v248, a80\n"
"v_mul_f32_e32 v248, v250, v248\n"
"v_accvgpr_write_b32 a80, v248\n"
"v_accvgpr_read_b32 v249, a81\n"
"v_mul_f32_e32 v249, v250, v249\n"
"v_accvgpr_write_b32 a81, v249\n"
"v_accvgpr_read_b32 v248, a82\n"
"v_mul_f32_e32 v248, v250, v248\n"
"v_accvgpr_write_b32 a82, v248\n"
"v_accvgpr_read_b32 v249, a83\n"
"v_mul_f32_e32 v249, v250, v249\n"
"v_accvgpr_write_b32 a83, v249\n"
"v_accvgpr_read_b32 v248, a96\n"
"v_mul_f32_e32 v248, v250, v248\n"
"v_accvgpr_write_b32 a96, v248\n"
"v_accvgpr_read_b32 v249, a97\n"
"v_mul_f32_e32 v249, v250, v249\n"
"v_accvgpr_write_b32 a97, v249\n"
"v_accvgpr_read_b32 v248, a98\n"
"v_mul_f32_e32 v248, v250, v248\n"
"v_accvgpr_write_b32 a98, v248\n"
"v_accvgpr_read_b32 v249, a99\n"
"v_mul_f32_e32 v249, v250, v249\n"
"v_accvgpr_write_b32 a99, v249\n"
"v_accvgpr_read_b32 v248, a112\n"
"v_mul_f32_e32 v248, v250, v248\n"
"v_accvgpr_write_b32 a112, v248\n"
"v_accvgpr_read_b32 v249, a113\n"
"v_mul_f32_e32 v249, v250, v249\n"
"v_accvgpr_write_b32 a113, v249\n"
"v_accvgpr_read_b32 v248, a114\n"
"v_mul_f32_e32 v248, v250, v248\n"
"v_accvgpr_write_b32 a114, v248\n"
"v_accvgpr_read_b32 v249, a115\n"
"v_mul_f32_e32 v249, v250, v249\n"
"v_accvgpr_write_b32 a115, v249\n"
"v_accvgpr_read_b32 v248, a128\n"
"v_mul_f32_e32 v248, v250, v248\n"
"v_accvgpr_write_b32 a128, v248\n"
"v_accvgpr_read_b32 v249, a129\n"
"v_mul_f32_e32 v249, v250, v249\n"
"v_accvgpr_write_b32 a129, v249\n"
"v_accvgpr_read_b32 v248, a130\n"
"v_mul_f32_e32 v248, v250, v248\n"
"v_accvgpr_write_b32 a130, v248\n"
"v_accvgpr_read_b32 v249, a131\n"
"v_mul_f32_e32 v249, v250, v249\n"
"v_accvgpr_write_b32 a131, v249\n"
"v_accvgpr_read_b32 v248, a144\n"
"v_mul_f32_e32 v248, v250, v248\n"
"v_accvgpr_write_b32 a144, v248\n"
"v_accvgpr_read_b32 v249, a145\n"
"v_mul_f32_e32 v249, v250, v249\n"
"v_accvgpr_write_b32 a145, v249\n"
"v_accvgpr_read_b32 v248, a146\n"
"v_mul_f32_e32 v248, v250, v248\n"
"v_accvgpr_write_b32 a146, v248\n"
"v_accvgpr_read_b32 v249, a147\n"
"v_mul_f32_e32 v249, v250, v249\n"
"v_accvgpr_write_b32 a147, v249\n"
"v_accvgpr_read_b32 v248, a160\n"
"v_mul_f32_e32 v248, v250, v248\n"
"v_accvgpr_write_b32 a160, v248\n"
"v_accvgpr_read_b32 v249, a161\n"
"v_mul_f32_e32 v249, v250, v249\n"
"v_accvgpr_write_b32 a161, v249\n"
"v_accvgpr_read_b32 v248, a162\n"
"v_mul_f32_e32 v248, v250, v248\n"
"v_accvgpr_write_b32 a162, v248\n"
"v_accvgpr_read_b32 v249, a163\n"
"v_mul_f32_e32 v249, v250, v249\n"
"v_accvgpr_write_b32 a163, v249\n"
"v_accvgpr_read_b32 v248, a176\n"
"v_mul_f32_e32 v248, v250, v248\n"
"v_accvgpr_write_b32 a176, v248\n"
"v_accvgpr_read_b32 v249, a177\n"
"v_mul_f32_e32 v249, v250, v249\n"
"v_accvgpr_write_b32 a177, v249\n"
"v_accvgpr_read_b32 v248, a178\n"
"v_mul_f32_e32 v248, v250, v248\n"
"v_accvgpr_write_b32 a178, v248\n"
"v_accvgpr_read_b32 v249, a179\n"
"v_mul_f32_e32 v249, v250, v249\n"
"v_accvgpr_write_b32 a179, v249\n"
"v_accvgpr_read_b32 v248, a192\n"
"v_mul_f32_e32 v248, v250, v248\n"
"v_accvgpr_write_b32 a192, v248\n"
"v_accvgpr_read_b32 v249, a193\n"
"v_mul_f32_e32 v249, v250, v249\n"
"v_accvgpr_write_b32 a193, v249\n"
"v_accvgpr_read_b32 v248, a194\n"
"v_mul_f32_e32 v248, v250, v248\n"
"v_accvgpr_write_b32 a194, v248\n"
"v_accvgpr_read_b32 v249, a195\n"
"v_mul_f32_e32 v249, v250, v249\n"
"v_accvgpr_write_b32 a195, v249\n"
"v_accvgpr_read_b32 v248, a208\n"
"v_mul_f32_e32 v248, v250, v248\n"
"v_accvgpr_write_b32 a208, v248\n"
"v_accvgpr_read_b32 v249, a209\n"
"v_mul_f32_e32 v249, v250, v249\n"
"v_accvgpr_write_b32 a209, v249\n"
"v_accvgpr_read_b32 v248, a210\n"
"v_mul_f32_e32 v248, v250, v248\n"
"v_accvgpr_write_b32 a210, v248\n"
"v_accvgpr_read_b32 v249, a211\n"
"v_mul_f32_e32 v249, v250, v249\n"
"v_accvgpr_write_b32 a211, v249\n"
"v_accvgpr_read_b32 v248, a224\n"
"v_mul_f32_e32 v248, v250, v248\n"
"v_accvgpr_write_b32 a224, v248\n"
"v_accvgpr_read_b32 v249, a225\n"
"v_mul_f32_e32 v249, v250, v249\n"
"v_accvgpr_write_b32 a225, v249\n"
"v_accvgpr_read_b32 v248, a226\n"
"v_mul_f32_e32 v248, v250, v248\n"
"v_accvgpr_write_b32 a226, v248\n"
"v_accvgpr_read_b32 v249, a227\n"
"v_mul_f32_e32 v249, v250, v249\n"
"v_accvgpr_write_b32 a227, v249\n"
"v_accvgpr_read_b32 v248, a240\n"
"v_mul_f32_e32 v248, v250, v248\n"
"v_accvgpr_write_b32 a240, v248\n"
"v_accvgpr_read_b32 v249, a241\n"
"v_mul_f32_e32 v249, v250, v249\n"
"v_accvgpr_write_b32 a241, v249\n"
"v_accvgpr_read_b32 v248, a242\n"
"v_mul_f32_e32 v248, v250, v248\n"
"v_accvgpr_write_b32 a242, v248\n"
"v_accvgpr_read_b32 v249, a243\n"
"v_mul_f32_e32 v249, v250, v249\n"
"v_accvgpr_write_b32 a243, v249\n"
"ds_read_b32 v250, v251 offset:256\n"
"s_waitcnt lgkmcnt(0)\n"
"v_accvgpr_read_b32 v248, a4\n"
"v_mul_f32_e32 v248, v250, v248\n"
"v_accvgpr_write_b32 a4, v248\n"
"v_accvgpr_read_b32 v249, a5\n"
"v_mul_f32_e32 v249, v250, v249\n"
"v_accvgpr_write_b32 a5, v249\n"
"v_accvgpr_read_b32 v248, a6\n"
"v_mul_f32_e32 v248, v250, v248\n"
"v_accvgpr_write_b32 a6, v248\n"
"v_accvgpr_read_b32 v249, a7\n"
"v_mul_f32_e32 v249, v250, v249\n"
"v_accvgpr_write_b32 a7, v249\n"
"v_accvgpr_read_b32 v248, a20\n"
"v_mul_f32_e32 v248, v250, v248\n"
"v_accvgpr_write_b32 a20, v248\n"
"v_accvgpr_read_b32 v249, a21\n"
"v_mul_f32_e32 v249, v250, v249\n"
"v_accvgpr_write_b32 a21, v249\n"
"v_accvgpr_read_b32 v248, a22\n"
"v_mul_f32_e32 v248, v250, v248\n"
"v_accvgpr_write_b32 a22, v248\n"
"v_accvgpr_read_b32 v249, a23\n"
"v_mul_f32_e32 v249, v250, v249\n"
"v_accvgpr_write_b32 a23, v249\n"
"v_accvgpr_read_b32 v248, a36\n"
"v_mul_f32_e32 v248, v250, v248\n"
"v_accvgpr_write_b32 a36, v248\n"
"v_accvgpr_read_b32 v249, a37\n"
"v_mul_f32_e32 v249, v250, v249\n"
"v_accvgpr_write_b32 a37, v249\n"
"v_accvgpr_read_b32 v248, a38\n"
"v_mul_f32_e32 v248, v250, v248\n"
"v_accvgpr_write_b32 a38, v248\n"
"v_accvgpr_read_b32 v249, a39\n"
"v_mul_f32_e32 v249, v250, v249\n"
"v_accvgpr_write_b32 a39, v249\n"
"v_accvgpr_read_b32 v248, a52\n"
"v_mul_f32_e32 v248, v250, v248\n"
"v_accvgpr_write_b32 a52, v248\n"
"v_accvgpr_read_b32 v249, a53\n"
"v_mul_f32_e32 v249, v250, v249\n"
"v_accvgpr_write_b32 a53, v249\n"
"v_accvgpr_read_b32 v248, a54\n"
"v_mul_f32_e32 v248, v250, v248\n"
"v_accvgpr_write_b32 a54, v248\n"
"v_accvgpr_read_b32 v249, a55\n"
"v_mul_f32_e32 v249, v250, v249\n"
"v_accvgpr_write_b32 a55, v249\n"
"v_accvgpr_read_b32 v248, a68\n"
"v_mul_f32_e32 v248, v250, v248\n"
"v_accvgpr_write_b32 a68, v248\n"
"v_accvgpr_read_b32 v249, a69\n"
"v_mul_f32_e32 v249, v250, v249\n"
"v_accvgpr_write_b32 a69, v249\n"
"v_accvgpr_read_b32 v248, a70\n"
"v_mul_f32_e32 v248, v250, v248\n"
"v_accvgpr_write_b32 a70, v248\n"
"v_accvgpr_read_b32 v249, a71\n"
"v_mul_f32_e32 v249, v250, v249\n"
"v_accvgpr_write_b32 a71, v249\n"
"v_accvgpr_read_b32 v248, a84\n"
"v_mul_f32_e32 v248, v250, v248\n"
"v_accvgpr_write_b32 a84, v248\n"
"v_accvgpr_read_b32 v249, a85\n"
"v_mul_f32_e32 v249, v250, v249\n"
"v_accvgpr_write_b32 a85, v249\n"
"v_accvgpr_read_b32 v248, a86\n"
"v_mul_f32_e32 v248, v250, v248\n"
"v_accvgpr_write_b32 a86, v248\n"
"v_accvgpr_read_b32 v249, a87\n"
"v_mul_f32_e32 v249, v250, v249\n"
"v_accvgpr_write_b32 a87, v249\n"
"v_accvgpr_read_b32 v248, a100\n"
"v_mul_f32_e32 v248, v250, v248\n"
"v_accvgpr_write_b32 a100, v248\n"
"v_accvgpr_read_b32 v249, a101\n"
"v_mul_f32_e32 v249, v250, v249\n"
"v_accvgpr_write_b32 a101, v249\n"
"v_accvgpr_read_b32 v248, a102\n"
"v_mul_f32_e32 v248, v250, v248\n"
"v_accvgpr_write_b32 a102, v248\n"
"v_accvgpr_read_b32 v249, a103\n"
"v_mul_f32_e32 v249, v250, v249\n"
"v_accvgpr_write_b32 a103, v249\n"
"v_accvgpr_read_b32 v248, a116\n"
"v_mul_f32_e32 v248, v250, v248\n"
"v_accvgpr_write_b32 a116, v248\n"
"v_accvgpr_read_b32 v249, a117\n"
"v_mul_f32_e32 v249, v250, v249\n"
"v_accvgpr_write_b32 a117, v249\n"
"v_accvgpr_read_b32 v248, a118\n"
"v_mul_f32_e32 v248, v250, v248\n"
"v_accvgpr_write_b32 a118, v248\n"
"v_accvgpr_read_b32 v249, a119\n"
"v_mul_f32_e32 v249, v250, v249\n"
"v_accvgpr_write_b32 a119, v249\n"
"v_accvgpr_read_b32 v248, a132\n"
"v_mul_f32_e32 v248, v250, v248\n"
"v_accvgpr_write_b32 a132, v248\n"
"v_accvgpr_read_b32 v249, a133\n"
"v_mul_f32_e32 v249, v250, v249\n"
"v_accvgpr_write_b32 a133, v249\n"
"v_accvgpr_read_b32 v248, a134\n"
"v_mul_f32_e32 v248, v250, v248\n"
"v_accvgpr_write_b32 a134, v248\n"
"v_accvgpr_read_b32 v249, a135\n"
"v_mul_f32_e32 v249, v250, v249\n"
"v_accvgpr_write_b32 a135, v249\n"
"v_accvgpr_read_b32 v248, a148\n"
"v_mul_f32_e32 v248, v250, v248\n"
"v_accvgpr_write_b32 a148, v248\n"
"v_accvgpr_read_b32 v249, a149\n"
"v_mul_f32_e32 v249, v250, v249\n"
"v_accvgpr_write_b32 a149, v249\n"
"v_accvgpr_read_b32 v248, a150\n"
"v_mul_f32_e32 v248, v250, v248\n"
"v_accvgpr_write_b32 a150, v248\n"
"v_accvgpr_read_b32 v249, a151\n"
"v_mul_f32_e32 v249, v250, v249\n"
"v_accvgpr_write_b32 a151, v249\n"
"v_accvgpr_read_b32 v248, a164\n"
"v_mul_f32_e32 v248, v250, v248\n"
"v_accvgpr_write_b32 a164, v248\n"
"v_accvgpr_read_b32 v249, a165\n"
"v_mul_f32_e32 v249, v250, v249\n"
"v_accvgpr_write_b32 a165, v249\n"
"v_accvgpr_read_b32 v248, a166\n"
"v_mul_f32_e32 v248, v250, v248\n"
"v_accvgpr_write_b32 a166, v248\n"
"v_accvgpr_read_b32 v249, a167\n"
"v_mul_f32_e32 v249, v250, v249\n"
"v_accvgpr_write_b32 a167, v249\n"
"v_accvgpr_read_b32 v248, a180\n"
"v_mul_f32_e32 v248, v250, v248\n"
"v_accvgpr_write_b32 a180, v248\n"
"v_accvgpr_read_b32 v249, a181\n"
"v_mul_f32_e32 v249, v250, v249\n"
"v_accvgpr_write_b32 a181, v249\n"
"v_accvgpr_read_b32 v248, a182\n"
"v_mul_f32_e32 v248, v250, v248\n"
"v_accvgpr_write_b32 a182, v248\n"
"v_accvgpr_read_b32 v249, a183\n"
"v_mul_f32_e32 v249, v250, v249\n"
"v_accvgpr_write_b32 a183, v249\n"
"v_accvgpr_read_b32 v248, a196\n"
"v_mul_f32_e32 v248, v250, v248\n"
"v_accvgpr_write_b32 a196, v248\n"
"v_accvgpr_read_b32 v249, a197\n"
"v_mul_f32_e32 v249, v250, v249\n"
"v_accvgpr_write_b32 a197, v249\n"
"v_accvgpr_read_b32 v248, a198\n"
"v_mul_f32_e32 v248, v250, v248\n"
"v_accvgpr_write_b32 a198, v248\n"
"v_accvgpr_read_b32 v249, a199\n"
"v_mul_f32_e32 v249, v250, v249\n"
"v_accvgpr_write_b32 a199, v249\n"
"v_accvgpr_read_b32 v248, a212\n"
"v_mul_f32_e32 v248, v250, v248\n"
"v_accvgpr_write_b32 a212, v248\n"
"v_accvgpr_read_b32 v249, a213\n"
"v_mul_f32_e32 v249, v250, v249\n"
"v_accvgpr_write_b32 a213, v249\n"
"v_accvgpr_read_b32 v248, a214\n"
"v_mul_f32_e32 v248, v250, v248\n"
"v_accvgpr_write_b32 a214, v248\n"
"v_accvgpr_read_b32 v249, a215\n"
"v_mul_f32_e32 v249, v250, v249\n"
"v_accvgpr_write_b32 a215, v249\n"
"v_accvgpr_read_b32 v248, a228\n"
"v_mul_f32_e32 v248, v250, v248\n"
"v_accvgpr_write_b32 a228, v248\n"
"v_accvgpr_read_b32 v249, a229\n"
"v_mul_f32_e32 v249, v250, v249\n"
"v_accvgpr_write_b32 a229, v249\n"
"v_accvgpr_read_b32 v248, a230\n"
"v_mul_f32_e32 v248, v250, v248\n"
"v_accvgpr_write_b32 a230, v248\n"
"v_accvgpr_read_b32 v249, a231\n"
"v_mul_f32_e32 v249, v250, v249\n"
"v_accvgpr_write_b32 a231, v249\n"
"v_accvgpr_read_b32 v248, a244\n"
"v_mul_f32_e32 v248, v250, v248\n"
"v_accvgpr_write_b32 a244, v248\n"
"v_accvgpr_read_b32 v249, a245\n"
"v_mul_f32_e32 v249, v250, v249\n"
"v_accvgpr_write_b32 a245, v249\n"
"v_accvgpr_read_b32 v248, a246\n"
"v_mul_f32_e32 v248, v250, v248\n"
"v_accvgpr_write_b32 a246, v248\n"
"v_accvgpr_read_b32 v249, a247\n"
"v_mul_f32_e32 v249, v250, v249\n"
"v_accvgpr_write_b32 a247, v249\n"
"ds_read_b32 v250, v251 offset:512\n"
"s_waitcnt lgkmcnt(0)\n"
"v_accvgpr_read_b32 v248, a8\n"
"v_mul_f32_e32 v248, v250, v248\n"
"v_accvgpr_write_b32 a8, v248\n"
"v_accvgpr_read_b32 v249, a9\n"
"v_mul_f32_e32 v249, v250, v249\n"
"v_accvgpr_write_b32 a9, v249\n"
"v_accvgpr_read_b32 v248, a10\n"
"v_mul_f32_e32 v248, v250, v248\n"
"v_accvgpr_write_b32 a10, v248\n"
"v_accvgpr_read_b32 v249, a11\n"
"v_mul_f32_e32 v249, v250, v249\n"
"v_accvgpr_write_b32 a11, v249\n"
"v_accvgpr_read_b32 v248, a24\n"
"v_mul_f32_e32 v248, v250, v248\n"
"v_accvgpr_write_b32 a24, v248\n"
"v_accvgpr_read_b32 v249, a25\n"
"v_mul_f32_e32 v249, v250, v249\n"
"v_accvgpr_write_b32 a25, v249\n"
"v_accvgpr_read_b32 v248, a26\n"
"v_mul_f32_e32 v248, v250, v248\n"
"v_accvgpr_write_b32 a26, v248\n"
"v_accvgpr_read_b32 v249, a27\n"
"v_mul_f32_e32 v249, v250, v249\n"
"v_accvgpr_write_b32 a27, v249\n"
"v_accvgpr_read_b32 v248, a40\n"
"v_mul_f32_e32 v248, v250, v248\n"
"v_accvgpr_write_b32 a40, v248\n"
"v_accvgpr_read_b32 v249, a41\n"
"v_mul_f32_e32 v249, v250, v249\n"
"v_accvgpr_write_b32 a41, v249\n"
"v_accvgpr_read_b32 v248, a42\n"
"v_mul_f32_e32 v248, v250, v248\n"
"v_accvgpr_write_b32 a42, v248\n"
"v_accvgpr_read_b32 v249, a43\n"
"v_mul_f32_e32 v249, v250, v249\n"
"v_accvgpr_write_b32 a43, v249\n"
"v_accvgpr_read_b32 v248, a56\n"
"v_mul_f32_e32 v248, v250, v248\n"
"v_accvgpr_write_b32 a56, v248\n"
"v_accvgpr_read_b32 v249, a57\n"
"v_mul_f32_e32 v249, v250, v249\n"
"v_accvgpr_write_b32 a57, v249\n"
"v_accvgpr_read_b32 v248, a58\n"
"v_mul_f32_e32 v248, v250, v248\n"
"v_accvgpr_write_b32 a58, v248\n"
"v_accvgpr_read_b32 v249, a59\n"
"v_mul_f32_e32 v249, v250, v249\n"
"v_accvgpr_write_b32 a59, v249\n"
"v_accvgpr_read_b32 v248, a72\n"
"v_mul_f32_e32 v248, v250, v248\n"
"v_accvgpr_write_b32 a72, v248\n"
"v_accvgpr_read_b32 v249, a73\n"
"v_mul_f32_e32 v249, v250, v249\n"
"v_accvgpr_write_b32 a73, v249\n"
"v_accvgpr_read_b32 v248, a74\n"
"v_mul_f32_e32 v248, v250, v248\n"
"v_accvgpr_write_b32 a74, v248\n"
"v_accvgpr_read_b32 v249, a75\n"
"v_mul_f32_e32 v249, v250, v249\n"
"v_accvgpr_write_b32 a75, v249\n"
"v_accvgpr_read_b32 v248, a88\n"
"v_mul_f32_e32 v248, v250, v248\n"
"v_accvgpr_write_b32 a88, v248\n"
"v_accvgpr_read_b32 v249, a89\n"
"v_mul_f32_e32 v249, v250, v249\n"
"v_accvgpr_write_b32 a89, v249\n"
"v_accvgpr_read_b32 v248, a90\n"
"v_mul_f32_e32 v248, v250, v248\n"
"v_accvgpr_write_b32 a90, v248\n"
"v_accvgpr_read_b32 v249, a91\n"
"v_mul_f32_e32 v249, v250, v249\n"
"v_accvgpr_write_b32 a91, v249\n"
"v_accvgpr_read_b32 v248, a104\n"
"v_mul_f32_e32 v248, v250, v248\n"
"v_accvgpr_write_b32 a104, v248\n"
"v_accvgpr_read_b32 v249, a105\n"
"v_mul_f32_e32 v249, v250, v249\n"
"v_accvgpr_write_b32 a105, v249\n"
"v_accvgpr_read_b32 v248, a106\n"
"v_mul_f32_e32 v248, v250, v248\n"
"v_accvgpr_write_b32 a106, v248\n"
"v_accvgpr_read_b32 v249, a107\n"
"v_mul_f32_e32 v249, v250, v249\n"
"v_accvgpr_write_b32 a107, v249\n"
"v_accvgpr_read_b32 v248, a120\n"
"v_mul_f32_e32 v248, v250, v248\n"
"v_accvgpr_write_b32 a120, v248\n"
"v_accvgpr_read_b32 v249, a121\n"
"v_mul_f32_e32 v249, v250, v249\n"
"v_accvgpr_write_b32 a121, v249\n"
"v_accvgpr_read_b32 v248, a122\n"
"v_mul_f32_e32 v248, v250, v248\n"
"v_accvgpr_write_b32 a122, v248\n"
"v_accvgpr_read_b32 v249, a123\n"
"v_mul_f32_e32 v249, v250, v249\n"
"v_accvgpr_write_b32 a123, v249\n"
"v_accvgpr_read_b32 v248, a136\n"
"v_mul_f32_e32 v248, v250, v248\n"
"v_accvgpr_write_b32 a136, v248\n"
"v_accvgpr_read_b32 v249, a137\n"
"v_mul_f32_e32 v249, v250, v249\n"
"v_accvgpr_write_b32 a137, v249\n"
"v_accvgpr_read_b32 v248, a138\n"
"v_mul_f32_e32 v248, v250, v248\n"
"v_accvgpr_write_b32 a138, v248\n"
"v_accvgpr_read_b32 v249, a139\n"
"v_mul_f32_e32 v249, v250, v249\n"
"v_accvgpr_write_b32 a139, v249\n"
"v_accvgpr_read_b32 v248, a152\n"
"v_mul_f32_e32 v248, v250, v248\n"
"v_accvgpr_write_b32 a152, v248\n"
"v_accvgpr_read_b32 v249, a153\n"
"v_mul_f32_e32 v249, v250, v249\n"
"v_accvgpr_write_b32 a153, v249\n"
"v_accvgpr_read_b32 v248, a154\n"
"v_mul_f32_e32 v248, v250, v248\n"
"v_accvgpr_write_b32 a154, v248\n"
"v_accvgpr_read_b32 v249, a155\n"
"v_mul_f32_e32 v249, v250, v249\n"
"v_accvgpr_write_b32 a155, v249\n"
"v_accvgpr_read_b32 v248, a168\n"
"v_mul_f32_e32 v248, v250, v248\n"
"v_accvgpr_write_b32 a168, v248\n"
"v_accvgpr_read_b32 v249, a169\n"
"v_mul_f32_e32 v249, v250, v249\n"
"v_accvgpr_write_b32 a169, v249\n"
"v_accvgpr_read_b32 v248, a170\n"
"v_mul_f32_e32 v248, v250, v248\n"
"v_accvgpr_write_b32 a170, v248\n"
"v_accvgpr_read_b32 v249, a171\n"
"v_mul_f32_e32 v249, v250, v249\n"
"v_accvgpr_write_b32 a171, v249\n"
"v_accvgpr_read_b32 v248, a184\n"
"v_mul_f32_e32 v248, v250, v248\n"
"v_accvgpr_write_b32 a184, v248\n"
"v_accvgpr_read_b32 v249, a185\n"
"v_mul_f32_e32 v249, v250, v249\n"
"v_accvgpr_write_b32 a185, v249\n"
"v_accvgpr_read_b32 v248, a186\n"
"v_mul_f32_e32 v248, v250, v248\n"
"v_accvgpr_write_b32 a186, v248\n"
"v_accvgpr_read_b32 v249, a187\n"
"v_mul_f32_e32 v249, v250, v249\n"
"v_accvgpr_write_b32 a187, v249\n"
"v_accvgpr_read_b32 v248, a200\n"
"v_mul_f32_e32 v248, v250, v248\n"
"v_accvgpr_write_b32 a200, v248\n"
"v_accvgpr_read_b32 v249, a201\n"
"v_mul_f32_e32 v249, v250, v249\n"
"v_accvgpr_write_b32 a201, v249\n"
"v_accvgpr_read_b32 v248, a202\n"
"v_mul_f32_e32 v248, v250, v248\n"
"v_accvgpr_write_b32 a202, v248\n"
"v_accvgpr_read_b32 v249, a203\n"
"v_mul_f32_e32 v249, v250, v249\n"
"v_accvgpr_write_b32 a203, v249\n"
"v_accvgpr_read_b32 v248, a216\n"
"v_mul_f32_e32 v248, v250, v248\n"
"v_accvgpr_write_b32 a216, v248\n"
"v_accvgpr_read_b32 v249, a217\n"
"v_mul_f32_e32 v249, v250, v249\n"
"v_accvgpr_write_b32 a217, v249\n"
"v_accvgpr_read_b32 v248, a218\n"
"v_mul_f32_e32 v248, v250, v248\n"
"v_accvgpr_write_b32 a218, v248\n"
"v_accvgpr_read_b32 v249, a219\n"
"v_mul_f32_e32 v249, v250, v249\n"
"v_accvgpr_write_b32 a219, v249\n"
"v_accvgpr_read_b32 v248, a232\n"
"v_mul_f32_e32 v248, v250, v248\n"
"v_accvgpr_write_b32 a232, v248\n"
"v_accvgpr_read_b32 v249, a233\n"
"v_mul_f32_e32 v249, v250, v249\n"
"v_accvgpr_write_b32 a233, v249\n"
"v_accvgpr_read_b32 v248, a234\n"
"v_mul_f32_e32 v248, v250, v248\n"
"v_accvgpr_write_b32 a234, v248\n"
"v_accvgpr_read_b32 v249, a235\n"
"v_mul_f32_e32 v249, v250, v249\n"
"v_accvgpr_write_b32 a235, v249\n"
"v_accvgpr_read_b32 v248, a248\n"
"v_mul_f32_e32 v248, v250, v248\n"
"v_accvgpr_write_b32 a248, v248\n"
"v_accvgpr_read_b32 v249, a249\n"
"v_mul_f32_e32 v249, v250, v249\n"
"v_accvgpr_write_b32 a249, v249\n"
"v_accvgpr_read_b32 v248, a250\n"
"v_mul_f32_e32 v248, v250, v248\n"
"v_accvgpr_write_b32 a250, v248\n"
"v_accvgpr_read_b32 v249, a251\n"
"v_mul_f32_e32 v249, v250, v249\n"
"v_accvgpr_write_b32 a251, v249\n"
"ds_read_b32 v250, v251 offset:768\n"
"s_waitcnt lgkmcnt(0)\n"
"v_accvgpr_read_b32 v248, a12\n"
"v_mul_f32_e32 v248, v250, v248\n"
"v_accvgpr_write_b32 a12, v248\n"
"v_accvgpr_read_b32 v249, a13\n"
"v_mul_f32_e32 v249, v250, v249\n"
"v_accvgpr_write_b32 a13, v249\n"
"v_accvgpr_read_b32 v248, a14\n"
"v_mul_f32_e32 v248, v250, v248\n"
"v_accvgpr_write_b32 a14, v248\n"
"v_accvgpr_read_b32 v249, a15\n"
"v_mul_f32_e32 v249, v250, v249\n"
"v_accvgpr_write_b32 a15, v249\n"
"v_accvgpr_read_b32 v248, a28\n"
"v_mul_f32_e32 v248, v250, v248\n"
"v_accvgpr_write_b32 a28, v248\n"
"v_accvgpr_read_b32 v249, a29\n"
"v_mul_f32_e32 v249, v250, v249\n"
"v_accvgpr_write_b32 a29, v249\n"
"v_accvgpr_read_b32 v248, a30\n"
"v_mul_f32_e32 v248, v250, v248\n"
"v_accvgpr_write_b32 a30, v248\n"
"v_accvgpr_read_b32 v249, a31\n"
"v_mul_f32_e32 v249, v250, v249\n"
"v_accvgpr_write_b32 a31, v249\n"
"v_accvgpr_read_b32 v248, a44\n"
"v_mul_f32_e32 v248, v250, v248\n"
"v_accvgpr_write_b32 a44, v248\n"
"v_accvgpr_read_b32 v249, a45\n"
"v_mul_f32_e32 v249, v250, v249\n"
"v_accvgpr_write_b32 a45, v249\n"
"v_accvgpr_read_b32 v248, a46\n"
"v_mul_f32_e32 v248, v250, v248\n"
"v_accvgpr_write_b32 a46, v248\n"
"v_accvgpr_read_b32 v249, a47\n"
"v_mul_f32_e32 v249, v250, v249\n"
"v_accvgpr_write_b32 a47, v249\n"
"v_accvgpr_read_b32 v248, a60\n"
"v_mul_f32_e32 v248, v250, v248\n"
"v_accvgpr_write_b32 a60, v248\n"
"v_accvgpr_read_b32 v249, a61\n"
"v_mul_f32_e32 v249, v250, v249\n"
"v_accvgpr_write_b32 a61, v249\n"
"v_accvgpr_read_b32 v248, a62\n"
"v_mul_f32_e32 v248, v250, v248\n"
"v_accvgpr_write_b32 a62, v248\n"
"v_accvgpr_read_b32 v249, a63\n"
"v_mul_f32_e32 v249, v250, v249\n"
"v_accvgpr_write_b32 a63, v249\n"
"v_accvgpr_read_b32 v248, a76\n"
"v_mul_f32_e32 v248, v250, v248\n"
"v_accvgpr_write_b32 a76, v248\n"
"v_accvgpr_read_b32 v249, a77\n"
"v_mul_f32_e32 v249, v250, v249\n"
"v_accvgpr_write_b32 a77, v249\n"
"v_accvgpr_read_b32 v248, a78\n"
"v_mul_f32_e32 v248, v250, v248\n"
"v_accvgpr_write_b32 a78, v248\n"
"v_accvgpr_read_b32 v249, a79\n"
"v_mul_f32_e32 v249, v250, v249\n"
"v_accvgpr_write_b32 a79, v249\n"
"v_accvgpr_read_b32 v248, a92\n"
"v_mul_f32_e32 v248, v250, v248\n"
"v_accvgpr_write_b32 a92, v248\n"
"v_accvgpr_read_b32 v249, a93\n"
"v_mul_f32_e32 v249, v250, v249\n"
"v_accvgpr_write_b32 a93, v249\n"
"v_accvgpr_read_b32 v248, a94\n"
"v_mul_f32_e32 v248, v250, v248\n"
"v_accvgpr_write_b32 a94, v248\n"
"v_accvgpr_read_b32 v249, a95\n"
"v_mul_f32_e32 v249, v250, v249\n"
"v_accvgpr_write_b32 a95, v249\n"
"v_accvgpr_read_b32 v248, a108\n"
"v_mul_f32_e32 v248, v250, v248\n"
"v_accvgpr_write_b32 a108, v248\n"
"v_accvgpr_read_b32 v249, a109\n"
"v_mul_f32_e32 v249, v250, v249\n"
"v_accvgpr_write_b32 a109, v249\n"
"v_accvgpr_read_b32 v248, a110\n"
"v_mul_f32_e32 v248, v250, v248\n"
"v_accvgpr_write_b32 a110, v248\n"
"v_accvgpr_read_b32 v249, a111\n"
"v_mul_f32_e32 v249, v250, v249\n"
"v_accvgpr_write_b32 a111, v249\n"
"v_accvgpr_read_b32 v248, a124\n"
"v_mul_f32_e32 v248, v250, v248\n"
"v_accvgpr_write_b32 a124, v248\n"
"v_accvgpr_read_b32 v249, a125\n"
"v_mul_f32_e32 v249, v250, v249\n"
"v_accvgpr_write_b32 a125, v249\n"
"v_accvgpr_read_b32 v248, a126\n"
"v_mul_f32_e32 v248, v250, v248\n"
"v_accvgpr_write_b32 a126, v248\n"
"v_accvgpr_read_b32 v249, a127\n"
"v_mul_f32_e32 v249, v250, v249\n"
"v_accvgpr_write_b32 a127, v249\n"
"v_accvgpr_read_b32 v248, a140\n"
"v_mul_f32_e32 v248, v250, v248\n"
"v_accvgpr_write_b32 a140, v248\n"
"v_accvgpr_read_b32 v249, a141\n"
"v_mul_f32_e32 v249, v250, v249\n"
"v_accvgpr_write_b32 a141, v249\n"
"v_accvgpr_read_b32 v248, a142\n"
"v_mul_f32_e32 v248, v250, v248\n"
"v_accvgpr_write_b32 a142, v248\n"
"v_accvgpr_read_b32 v249, a143\n"
"v_mul_f32_e32 v249, v250, v249\n"
"v_accvgpr_write_b32 a143, v249\n"
"v_accvgpr_read_b32 v248, a156\n"
"v_mul_f32_e32 v248, v250, v248\n"
"v_accvgpr_write_b32 a156, v248\n"
"v_accvgpr_read_b32 v249, a157\n"
"v_mul_f32_e32 v249, v250, v249\n"
"v_accvgpr_write_b32 a157, v249\n"
"v_accvgpr_read_b32 v248, a158\n"
"v_mul_f32_e32 v248, v250, v248\n"
"v_accvgpr_write_b32 a158, v248\n"
"v_accvgpr_read_b32 v249, a159\n"
"v_mul_f32_e32 v249, v250, v249\n"
"v_accvgpr_write_b32 a159, v249\n"
"v_accvgpr_read_b32 v248, a172\n"
"v_mul_f32_e32 v248, v250, v248\n"
"v_accvgpr_write_b32 a172, v248\n"
"v_accvgpr_read_b32 v249, a173\n"
"v_mul_f32_e32 v249, v250, v249\n"
"v_accvgpr_write_b32 a173, v249\n"
"v_accvgpr_read_b32 v248, a174\n"
"v_mul_f32_e32 v248, v250, v248\n"
"v_accvgpr_write_b32 a174, v248\n"
"v_accvgpr_read_b32 v249, a175\n"
"v_mul_f32_e32 v249, v250, v249\n"
"v_accvgpr_write_b32 a175, v249\n"
"v_accvgpr_read_b32 v248, a188\n"
"v_mul_f32_e32 v248, v250, v248\n"
"v_accvgpr_write_b32 a188, v248\n"
"v_accvgpr_read_b32 v249, a189\n"
"v_mul_f32_e32 v249, v250, v249\n"
"v_accvgpr_write_b32 a189, v249\n"
"v_accvgpr_read_b32 v248, a190\n"
"v_mul_f32_e32 v248, v250, v248\n"
"v_accvgpr_write_b32 a190, v248\n"
"v_accvgpr_read_b32 v249, a191\n"
"v_mul_f32_e32 v249, v250, v249\n"
"v_accvgpr_write_b32 a191, v249\n"
"v_accvgpr_read_b32 v248, a204\n"
"v_mul_f32_e32 v248, v250, v248\n"
"v_accvgpr_write_b32 a204, v248\n"
"v_accvgpr_read_b32 v249, a205\n"
"v_mul_f32_e32 v249, v250, v249\n"
"v_accvgpr_write_b32 a205, v249\n"
"v_accvgpr_read_b32 v248, a206\n"
"v_mul_f32_e32 v248, v250, v248\n"
"v_accvgpr_write_b32 a206, v248\n"
"v_accvgpr_read_b32 v249, a207\n"
"v_mul_f32_e32 v249, v250, v249\n"
"v_accvgpr_write_b32 a207, v249\n"
"v_accvgpr_read_b32 v248, a220\n"
"v_mul_f32_e32 v248, v250, v248\n"
"v_accvgpr_write_b32 a220, v248\n"
"v_accvgpr_read_b32 v249, a221\n"
"v_mul_f32_e32 v249, v250, v249\n"
"v_accvgpr_write_b32 a221, v249\n"
"v_accvgpr_read_b32 v248, a222\n"
"v_mul_f32_e32 v248, v250, v248\n"
"v_accvgpr_write_b32 a222, v248\n"
"v_accvgpr_read_b32 v249, a223\n"
"v_mul_f32_e32 v249, v250, v249\n"
"v_accvgpr_write_b32 a223, v249\n"
"v_accvgpr_read_b32 v248, a236\n"
"v_mul_f32_e32 v248, v250, v248\n"
"v_accvgpr_write_b32 a236, v248\n"
"v_accvgpr_read_b32 v249, a237\n"
"v_mul_f32_e32 v249, v250, v249\n"
"v_accvgpr_write_b32 a237, v249\n"
"v_accvgpr_read_b32 v248, a238\n"
"v_mul_f32_e32 v248, v250, v248\n"
"v_accvgpr_write_b32 a238, v248\n"
"v_accvgpr_read_b32 v249, a239\n"
"v_mul_f32_e32 v249, v250, v249\n"
"v_accvgpr_write_b32 a239, v249\n"
"v_accvgpr_read_b32 v248, a252\n"
"v_mul_f32_e32 v248, v250, v248\n"
"v_accvgpr_write_b32 a252, v248\n"
"v_accvgpr_read_b32 v249, a253\n"
"v_mul_f32_e32 v249, v250, v249\n"
"v_accvgpr_write_b32 a253, v249\n"
"v_accvgpr_read_b32 v248, a254\n"
"v_mul_f32_e32 v248, v250, v248\n"
"v_accvgpr_write_b32 a254, v248\n"
"v_accvgpr_read_b32 v249, a255\n"
"v_mul_f32_e32 v249, v250, v249\n"
"v_accvgpr_write_b32 a255, v249\n"
"s_nop 1\n"
"s_setpc_b64 s[78:79]\n"
".p2align 12\n"
  :: "s"(ka), "s"(blk), "s"(wid), "s"(lds)
  : "v0","v1","v2","v3","v4","v5","v6","v7","v8","v9","v10","v11","v12","v13","v14","v15","v16","v17","v18","v19","v20","v21","v22","v23","v24","v25","v26","v27","v28","v29","v30","v31","v32","v33","v34","v35","v36","v37","v38","v39","v40","v41","v42","v43","v44","v45","v46","v47","v48","v49","v50","v51","v52","v53","v54","v55","v56","v57","v58","v59","v60","v61","v62","v63","v64","v65","v66","v67","v68","v69","v70","v71","v72","v73","v74","v75","v76","v77","v78","v79","v80","v81","v82","v83","v84","v85","v86","v87","v88","v89","v90","v91","v92","v93","v94","v95","v96","v97","v98","v99","v100","v101","v102","v103","v104","v105","v106","v107","v108","v109","v110","v111","v112","v113","v114","v115","v116","v117","v118","v119","v120","v121","v122","v123","v124","v125","v126","v127","v128","v129","v130","v131","v132","v133","v134","v135","v136","v137","v138","v139","v140","v141","v142","v143","v144","v145","v146","v147","v148","v149","v150","v151","v152","v153","v154","v155","v156","v157","v158","v159","v160","v161","v162","v163","v164","v165","v166","v167","v168","v169","v170","v171","v172","v173","v174","v175","v176","v177","v178","v179","v180","v181","v182","v183","v184","v185","v186","v187","v188","v189","v190","v191","v192","v193","v194","v195","v196","v197","v198","v199","v200","v201","v202","v203","v204","v205","v206","v207","v208","v209","v210","v211","v212","v213","v214","v215","v216","v217","v218","v219","v220","v221","v222","v223","v224","v225","v226","v227","v228","v229","v230","v231","v232","v233","v234","v235","v236","v237","v238","v239","v240","v241","v242","v243","v244","v245","v246","v247","v248","v249","v250","v251","v252","v253","v254","v255","a0","a1","a2","a3","a4","a5","a6","a7","a8","a9","a10","a11","a12","a13","a14","a15","a16","a17","a18","a19","a20","a21","a22","a23","a24","a25","a26","a27","a28","a29","a30","a31","a32","a33","a34","a35","a36","a37","a38","a39","a40","a41","a42","a43","a44","a45","a46","a47","a48","a49","a50","a51","a52","a53","a54","a55","a56","a57","a58","a59","a60","a61","a62","a63","a64","a65","a66","a67","a68","a69","a70","a71","a72","a73","a74","a75","a76","a77","a78","a79","a80","a81","a82","a83","a84","a85","a86","a87","a88","a89","a90","a91","a92","a93","a94","a95","a96","a97","a98","a99","a100","a101","a102","a103","a104","a105","a106","a107","a108","a109","a110","a111","a112","a113","a114","a115","a116","a117","a118","a119","a120","a121","a122","a123","a124","a125","a126","a127","a128","a129","a130","a131","a132","a133","a134","a135","a136","a137","a138","a139","a140","a141","a142","a143","a144","a145","a146","a147","a148","a149","a150","a151","a152","a153","a154","a155","a156","a157","a158","a159","a160","a161","a162","a163","a164","a165","a166","a167","a168","a169","a170","a171","a172","a173","a174","a175","a176","a177","a178","a179","a180","a181","a182","a183","a184","a185","a186","a187","a188","a189","a190","a191","a192","a193","a194","a195","a196","a197","a198","a199","a200","a201","a202","a203","a204","a205","a206","a207","a208","a209","a210","a211","a212","a213","a214","a215","a216","a217","a218","a219","a220","a221","a222","a223","a224","a225","a226","a227","a228","a229","a230","a231","a232","a233","a234","a235","a236","a237","a238","a239","a240","a241","a242","a243","a244","a245","a246","a247","a248","a249","a250","a251","a252","a253","a254","a255","s4","s5","s6","s7","s8","s9","s10","s11","s12","s13","s14","s15","s16","s17","s20","s21","s22","s23","s24","s25","s26","s27","s28","s29","s30","s31","s34","s35","s36","s37","s38","s39","s40","s41","s42","s43","s44","s45","s46","s47","s48","s49","s50","s51","s52","s53","s54","s55","s56","s57","s58","s59","s60","s61","s62","s63","s64","s65","s66","s67","s68","s69","s70","s71","s72","s73","s74","s75","s76","s77","s78","s79","s80","s81","s82","s83","s84","s85","s86","s87","s88","s89","s90","s91","s92","s93","s94","s95","s96","s97","s98","s99","vcc","scc","memory");
}

extern "C" void kernel_launch(void* const* d_in, const int* in_sizes, int n_in, void* d_out, int out_size, void* d_ws, size_t ws_size, hipStream_t stream) {
  mha_block_kernel<<<256, 256, 0, stream>>>(d_in[0], d_in[1], d_in[2], d_in[3], d_in[4], d_ws, d_out);
}
```
